```python
import torch

def _softmax(x, axis=-1):
    return torch.softmax(x, dim=axis)
def _gelu(x):
    return torch.nn.functional.gelu(x, approximate="tanh")
def _silu(x):
    return torch.nn.functional.silu(x)
def _sigmoid(x):
    return torch.sigmoid(x)
def _layernorm(x, axis=-1, eps=1e-5):
    m = x.mean(dim=axis, keepdim=True)
    v = x.var(dim=axis, keepdim=True, unbiased=False)
    return (x - m) / torch.sqrt(v + eps)
def _scatter_add(tgt, idx, src, axis=0):
    return tgt.clone().index_add_(axis, idx, src)
def _segment_sum(src, seg, n):
    out = torch.zeros((n,) + src.shape[1:], dtype=src.dtype)
    return out.index_add_(0, seg, src)
def _pad(x, pw):
    flat = tuple(v for pair in reversed(pw) for v in pair)
    return torch.nn.functional.pad(x, flat)

kernel_name = 'concat_heads__gqa_block_cached__gqa_block_cached__near_ridge'

def setup_inputs():

    _w = (lambda a: (lambda t: t.to(torch.bfloat16) if t.is_floating_point() else t)(torch.as_tensor(a)))
    def _A_setup():
        B, Sq, D = 16, 80, 512
        Hq, Hkv, Sk, Dh = 32, 32, 80, 128
        assert Hq % Hkv == 0, (Hq, Hkv)
        return {
            "x": _w((lambda *s: torch.randn(*s, dtype=torch.bfloat16) * 0.8)(B, Sq, D)),
            "k_cache": _w((lambda *s: torch.randn(*s, dtype=torch.bfloat16) * 0.8)(B, Hkv, Sk, Dh)),
            "v_cache": _w((lambda *s: torch.randn(*s, dtype=torch.bfloat16) * 0.05)(B, Hkv, Sk, Dh)),
            "wq": _w((lambda *s: torch.randn(*s, dtype=torch.bfloat16) * 0.8)(D, Hq * Dh) * (D ** -0.5)),
            "wo": _w((lambda *s: torch.randn(*s, dtype=torch.bfloat16) * 0.05)(Hq * Dh, D) * ((Hq * Dh) ** -0.5)),
        }
    _A_in = _A_setup()

    def _B_setup():
        B, Sq, D = 16, 80, 512
        Hq, Hkv, Sk, Dh = 32, 32, 80, 128
        assert Hq % Hkv == 0, (Hq, Hkv)
        return {
            "x": _w((lambda *s: torch.randn(*s, dtype=torch.bfloat16) * 0.8)(B, Sq, D)),
            "k_cache": _w((lambda *s: torch.randn(*s, dtype=torch.bfloat16) * 0.8)(B, Hkv, Sk, Dh)),
            "v_cache": _w((lambda *s: torch.randn(*s, dtype=torch.bfloat16) * 0.05)(B, Hkv, Sk, Dh)),
            "wq": _w((lambda *s: torch.randn(*s, dtype=torch.bfloat16) * 0.8)(D, Hq * Dh) * (D ** -0.5)),
            "wo": _w((lambda *s: torch.randn(*s, dtype=torch.bfloat16) * 0.05)(Hq * Dh, D) * ((Hq * Dh) ** -0.5)),
        }
    _B_in = _B_setup()

    return {
        "A_x": _A_in["x"],
        "A_k_cache": _A_in["k_cache"],
        "A_v_cache": _A_in["v_cache"],
        "A_wq": _A_in["wq"],
        "A_wo": _A_in["wo"],
        "B_x": _B_in["x"],
        "B_k_cache": _B_in["k_cache"],
        "B_v_cache": _B_in["v_cache"],
        "B_wq": _B_in["wq"],
        "B_wo": _B_in["wo"],
    }

def reference(A_x, A_k_cache, A_v_cache, A_wq, A_wo, B_x, B_k_cache, B_v_cache, B_wq, B_wo):
    def _A_ref(x, k_cache, v_cache, wq, wo):
        B, Sq, D = x.shape
        Hq, Dh = 32, 128
        Hkv = k_cache.shape[1]
        rep = Hq // Hkv
        q = (lambda x, *d: x.permute(*d))((x @ wq).reshape(B, Sq, Hq, Dh), 0, 2, 1, 3)
        k = (lambda x, n, axis: torch.repeat_interleave(x, n, dim=axis))(k_cache, rep, axis=1)
        v = (lambda x, n, axis: torch.repeat_interleave(x, n, dim=axis))(v_cache, rep, axis=1)
        scores = torch.einsum("bhqd,bhkd->bhqk", q, k) * (Dh ** -0.5)
        p = _softmax(scores, axis=-1)
        o = torch.einsum("bhqk,bhkd->bhqd", p, v)
        return (lambda x, *d: x.permute(*d))(o, 0, 2, 1, 3).reshape(B, Sq, Hq * Dh) @ wo

    def _B_ref(x, k_cache, v_cache, wq, wo):
        B, Sq, D = x.shape
        Hq, Dh = 32, 128
        Hkv = k_cache.shape[1]
        rep = Hq // Hkv
        q = (lambda x, *d: x.permute(*d))((x @ wq).reshape(B, Sq, Hq, Dh), 0, 2, 1, 3)
        k = (lambda x, n, axis: torch.repeat_interleave(x, n, dim=axis))(k_cache, rep, axis=1)
        v = (lambda x, n, axis: torch.repeat_interleave(x, n, dim=axis))(v_cache, rep, axis=1)
        scores = torch.einsum("bhqd,bhkd->bhqk", q, k) * (Dh ** -0.5)
        p = _softmax(scores, axis=-1)
        o = torch.einsum("bhqk,bhkd->bhqd", p, v)
        return (lambda x, *d: x.permute(*d))(o, 0, 2, 1, 3).reshape(B, Sq, Hq * Dh) @ wo

    a = _A_ref(A_x, A_k_cache, A_v_cache, A_wq, A_wo)
    b = _B_ref(B_x, B_k_cache, B_v_cache, B_wq, B_wo)
    return torch.concatenate([a, b], axis=-1)
```

```cpp
#include <hip/hip_runtime.h>
#include <stdint.h>
#include <utility>

typedef unsigned u32;
typedef unsigned long long u64;
typedef unsigned char u8;
typedef u32 u32x4 __attribute__((ext_vector_type(4)));
typedef u32 u32x2 __attribute__((ext_vector_type(2)));
typedef float f32x4 __attribute__((ext_vector_type(4)));
typedef float f32x2 __attribute__((ext_vector_type(2)));
typedef __bf16 bf16x8 __attribute__((ext_vector_type(8)));
typedef __bf16 bf16x2 __attribute__((ext_vector_type(2)));
typedef short s16x4 __attribute__((ext_vector_type(4)));
#define LAS __attribute__((address_space(3)))
#define GAS __attribute__((address_space(1)))
#define DI __device__ __forceinline__
#define SB() __builtin_amdgcn_sched_barrier(0)

#define STAMPS 0
#define AUX_KV 2
#define AUX_X 0

struct Args {
  const u8* x[2];
  const u8* k[2];
  const u8* v[2];
  const u8* wq[2];
  const u8* wo[2];
  u8* out;
  u8* ws;
};

constexpr u32 WS_NONCE = 0;
constexpr u32 WS_FLAG = 4096;
constexpr u32 WS_STAMP = 65536;
constexpr u32 WS_SLAB = 1u << 20;

DI u32 cvtpk(float lo, float hi) {
  f32x2 v = {lo, hi};
  return __builtin_bit_cast(u32, __builtin_convertvector(v, bf16x2));
}
DI f32x4 mfma32(u32x4 a, u32x4 b, f32x4 c) {
  return __builtin_amdgcn_mfma_f32_16x16x32_bf16(__builtin_bit_cast(bf16x8, a), __builtin_bit_cast(bf16x8, b), c, 0, 0, 0);
}
DI f32x4 mfma16(u32x2 a, u32x2 b, f32x4 c) {
  return __builtin_amdgcn_mfma_f32_16x16x16bf16_1k(__builtin_bit_cast(s16x4, a), __builtin_bit_cast(s16x4, b), c, 0, 0, 0);
}
DI u32 permlo(u32 r0, u32 r1) { return __builtin_amdgcn_perm(r1, r0, 0x05040100u); }
DI u32 permhi(u32 r0, u32 r1) { return __builtin_amdgcn_perm(r1, r0, 0x07060302u); }
DI u32x4 ldsr(u32 a) { return *(LAS u32x4*)(uintptr_t)a; }
DI void ldsw(u32 a, u32x4 v) { *(LAS u32x4*)(uintptr_t)a = v; }
DI void bar() { asm volatile("s_waitcnt lgkmcnt(0)\n\ts_barrier" ::: "memory"); }
DI float xmax(float x) {
  auto s = __builtin_amdgcn_permlane16_swap(__float_as_uint(x), __float_as_uint(x), false, false);
  x = fmaxf(__uint_as_float(s[0]), __uint_as_float(s[1]));
  auto t = __builtin_amdgcn_permlane32_swap(__float_as_uint(x), __float_as_uint(x), false, false);
  return fmaxf(__uint_as_float(t[0]), __uint_as_float(t[1]));
}
DI float xsum(float x) {
  auto s = __builtin_amdgcn_permlane16_swap(__float_as_uint(x), __float_as_uint(x), false, false);
  x = __uint_as_float(s[0]) + __uint_as_float(s[1]);
  auto t = __builtin_amdgcn_permlane32_swap(__float_as_uint(x), __float_as_uint(x), false, false);
  return __uint_as_float(t[0]) + __uint_as_float(t[1]);
}
DI u64 tagof(u64 n) { return n * 6364136223846793005ull + 1442695040888963407ull; }

template <class F, int... I>
DI void sfor_impl(F&& f, std::integer_sequence<int, I...>) { (f(std::integral_constant<int, I>{}), ...); }
template <int N, class F>
DI void sfor(F&& f) { sfor_impl(f, std::make_integer_sequence<int, N>{}); }

DI u32 tperm(const u32x4 (&R)[8], int t, int p) {
  return (t & 1) ? permhi(R[2 * p][t >> 1], R[2 * p + 1][t >> 1]) : permlo(R[2 * p][t >> 1], R[2 * p + 1][t >> 1]);
}

#define DUMP(which)
#define NSTAMP 9
#define STAMP(n) do { if (STAMPS) { st[n] = __builtin_amdgcn_s_memtime(); } } while (0)

__global__ void __launch_bounds__(256, 1) fused_attn_kernel(Args a) {
  __shared__ __attribute__((aligned(1024))) u8 smem[163840];
  const int bid = blockIdx.x, xcd = bid & 7, idx = bid >> 3;
  const int br = xcd >> 2;
  const int hq = (xcd & 3) * 2 + (idx & 1), b = idx >> 1;
  const int tid = threadIdx.x;
  const int lane = tid & 63, w = __builtin_amdgcn_readfirstlane(tid >> 6);
  const int li = lane & 15, g = lane >> 4;
  const int h = hq * 4 + w;
  const int rot = (b ^ ((hq & 1) << 3)) & 15;
  const int grp = br * 16 + b;
  u64 st[12];
  u64 rt0 = 0;
  if (STAMPS) rt0 = __builtin_amdgcn_s_memrealtime();
  STAMP(0);

  const u32 lds0 = (u32)(uintptr_t)(LAS u8*)smem;
  __amdgpu_buffer_rsrc_t rx = __builtin_amdgcn_make_buffer_rsrc((void*)(a.x[br] + (size_t)b * 81920), 0, 81920, 0x00020000);
  __amdgpu_buffer_rsrc_t rk = __builtin_amdgcn_make_buffer_rsrc((void*)(a.k[br] + (size_t)(b * 32 + h) * 20480), 0, 20480, 0x00020000);
  __amdgpu_buffer_rsrc_t rv = __builtin_amdgcn_make_buffer_rsrc((void*)(a.v[br] + (size_t)(b * 32 + h) * 20480), 0, 20480, 0x00020000);
  __amdgpu_buffer_rsrc_t rq = __builtin_amdgcn_make_buffer_rsrc((void*)(a.wq[br] + h * 256), 0, 512 * 8192 - h * 256, 0x00020000);
  __amdgpu_buffer_rsrc_t ro = __builtin_amdgcn_make_buffer_rsrc((void*)(a.wo[br] + (size_t)hq * 512 * 1024 + w * 256), 0, 512 * 1024 - w * 256, 0x00020000);
  __amdgpu_buffer_rsrc_t rw = __builtin_amdgcn_make_buffer_rsrc((void*)(a.ws), 0, WS_SLAB + 32 * 8 * 8 * 10240, 0x00020000);

  u32x2 nv = __builtin_amdgcn_raw_buffer_load_b64(rw, (lane & 7) * 8, WS_NONCE + grp * 64, 16);

  u32x4 ring[4][8];
  u32x4 stg[10];
  const int vq = (8 * g) * 8192 + li * 16;
  const int vo = (8 * g) * 1024 + li * 16;
  const int vx = (20 * w + g) * 1024 + li * 16;
  const int vk = g * 256 + li * 16;
  const int vv = (4 * g) * 256 + li * 16;

#define LOADW(slot, s, j) ring[slot][j] = __builtin_amdgcn_raw_buffer_load_b128(rq, vq + (j) * 8192, (((s) ^ rot) * 32) * 8192, 0)
#define LOADO(slot, s, j) ring[slot][j] = __builtin_amdgcn_raw_buffer_load_b128(ro, vo + (j) * 1024, (((s) ^ rot) * 32) * 1024, 0)
#define LOADX(cc, m) stg[((cc) & 1) * 5 + (m)] = __builtin_amdgcn_raw_buffer_load_b128(rx, vx + (m) * 4096, (((cc) ^ (rot >> 2)) * 256), AUX_X)
#define LOADK(m) stg[(m) % 10] = __builtin_amdgcn_raw_buffer_load_b128(rk, vk + (m) * 1024, 0, AUX_KV)
#define LOADV(slot, s, j) ring[slot][j] = __builtin_amdgcn_raw_buffer_load_b128(rv, vv + (32 * (s) + 16 * ((j) >> 2) + ((j) & 3)) * 256, 0, AUX_KV)

  u32 xw[5];
#pragma unroll
  for (int m = 0; m < 5; ++m) {
    const int q = 20 * w + 4 * m + g;
    xw[m] = lds0 + ((q & 15) * 5 + (q >> 4)) * 1024 + ((li ^ (q & 15)) * 16) + ((rot >> 2) * 256);
  }
#define WRITEX(cc, m) ldsw(xw[m] ^ ((cc) * 256), stg[((cc) & 1) * 5 + (m)])
  const u32 kbase = lds0 + 81920 + w * 20480;
  const u32 kw0 = kbase + g * 256 + ((li ^ ((g + 4) & 15)) * 16);
#define WRITEK(m) ldsw(((kw0 ^ (16 * ((((4 * (m) + 4) & 12) ^ 4)))) + (m) * 1024), stg[(m) % 10])
  const u32 kr0 = kbase + li * 256 + (((4 * g) ^ ((li + 4) & 15)) * 16);
  const u32 bb = lds0 + li * 5120 + (((g ^ li) * 16) ^ (rot << 6));

  SB();
  sfor<5>([&](auto M_) { LOADX(0, M_.value); SB(); });
  sfor<8>([&](auto J_) { LOADW(0, 0, J_.value); SB(); });
  sfor<5>([&](auto M_) { LOADX(1, M_.value); SB(); });
  sfor<8>([&](auto J_) { LOADW(1, 1, J_.value); SB(); });
  sfor<8>([&](auto J_) { LOADW(2, 2, J_.value); SB(); });
  sfor<8>([&](auto J_) { LOADW(3, 3, J_.value); SB(); });
#pragma unroll
  for (int m = 0; m < 5; ++m) WRITEX(0, m);
  bar();
  STAMP(1);

  f32x4 acc[8][5];
#pragma unroll
  for (int t = 0; t < 8; ++t)
#pragma unroll
    for (int q = 0; q < 5; ++q) acc[t][q] = f32x4{0.f, 0.f, 0.f, 0.f};
  u32x4 B[2][5];
  u32x4 F[2];
#pragma unroll
  for (int q = 0; q < 5; ++q) B[0][q] = ldsr(bb + q * 1024);
#pragma unroll
  for (int p = 0; p < 4; ++p) F[0][p] = tperm(ring[0], 0, p);
  SB();

  sfor<16>([&](auto S_) {
    constexpr int s = decltype(S_)::value;
    if constexpr (s == 3 || s == 7) bar();
    const u32 bn = bb ^ ((s + 1) << 6);
    sfor<8>([&](auto T_) {
      constexpr int t = decltype(T_)::value;
      sfor<5>([&](auto Q_) {
        constexpr int q = decltype(Q_)::value;
        acc[t][q] = mfma32(F[t & 1], B[s & 1][q], acc[t][q]);
        if constexpr (q < 4) {
          if constexpr (t < 7) F[(t + 1) & 1][q] = tperm(ring[s & 3], t + 1, q);
          else if constexpr (s < 15) F[0][q] = tperm(ring[(s + 1) & 3], 0, q);
        } else {
          if constexpr (t < 5 && s < 15) B[(s + 1) & 1][t] = ldsr(bn + t * 1024);
          constexpr int sn = (t == 7) ? s + 4 : s + 3;
          constexpr int j = (t + 1) & 7;
          if constexpr (t == 7 || s >= 1) {
            if constexpr (sn < 16) LOADW(sn & 3, sn, j);
            else if constexpr (sn < 18) LOADV(sn & 3, sn - 16, j);
            else if constexpr (sn == 18) { if constexpr (j < 4) LOADV(sn & 3, 2, j); }
            else LOADO(sn & 3, 0, j);
          }
          if constexpr (s == 0 && t < 5) { LOADX(2, t); }
          if constexpr (s == 1 && t < 5) { WRITEX(1, t); LOADX(3, t); }
          if constexpr (s == 3 && t < 5) { WRITEX(2, t); LOADK(t); }
          if constexpr (s == 4 && t < 5) { WRITEX(3, t); LOADK(5 + t); }
          if constexpr (s == 7 && t < 5) { WRITEK(t); LOADK(10 + t); }
          if constexpr (s == 8 && t < 5) { WRITEK(5 + t); LOADK(15 + t); }
          if constexpr (s == 11 && t < 5) { WRITEK(10 + t); }
          if constexpr (s == 12 && t < 5) { WRITEK(15 + t); }
        }
        SB();
      });
    });
  });
  STAMP(2);
  DUMP(0);
#pragma unroll
  for (int j = 1; j < 8; ++j) LOADO(3, 0, j);
  SB();

  u32x4 Kf[5][4];
#pragma unroll
  for (int kt = 0; kt < 5; ++kt)
#pragma unroll
    for (int s = 0; s < 4; ++s) Kf[kt][s] = ldsr((kr0 ^ (s << 4)) + kt * 4096);
  u32x4 Pf[5][2];
  u32x2 Pl[5];
  constexpr float C2 = 0.08838834764831845f * 1.4426950408889634f;
#pragma unroll
  for (int q = 0; q < 5; ++q) {
    u32x4 Qf[4];
#pragma unroll
    for (int s = 0; s < 4; ++s)
#pragma unroll
      for (int p = 0; p < 4; ++p) Qf[s][p] = cvtpk(acc[2 * p][q][s], acc[2 * p + 1][q][s]);
    f32x4 S[5];
#pragma unroll
    for (int kt = 0; kt < 5; ++kt) {
      S[kt] = f32x4{0.f, 0.f, 0.f, 0.f};
#pragma unroll
      for (int s = 0; s < 4; ++s) S[kt] = mfma32(Kf[kt][s], Qf[s], S[kt]);
    }
    float m = S[0][0];
#pragma unroll
    for (int kt = 0; kt < 5; ++kt)
#pragma unroll
      for (int r = 0; r < 4; ++r) m = fmaxf(m, S[kt][r]);
    m = xmax(m);
    const float nm = -m * C2;
    float l = 0.f;
#pragma unroll
    for (int kt = 0; kt < 5; ++kt)
#pragma unroll
      for (int r = 0; r < 4; ++r) { S[kt][r] = __builtin_amdgcn_exp2f(__builtin_fmaf(S[kt][r], C2, nm)); l += S[kt][r]; }
    l = xsum(l);
    const float il = __builtin_amdgcn_rcpf(l);
#pragma unroll
    for (int s = 0; s < 2; ++s)
#pragma unroll
      for (int p = 0; p < 4; ++p) Pf[q][s][p] = cvtpk(S[2 * s + (p >> 1)][2 * (p & 1)] * il, S[2 * s + (p >> 1)][2 * (p & 1) + 1] * il);
    Pl[q][0] = cvtpk(S[4][0] * il, S[4][1] * il);
    Pl[q][1] = cvtpk(S[4][2] * il, S[4][3] * il);
  }
  STAMP(3);
#pragma unroll
  for (int t = 0; t < 8; ++t) {
    u32x4 V0, V1;
    u32x2 V2;
#pragma unroll
    for (int p = 0; p < 4; ++p) { V0[p] = tperm(ring[0], t, p); V1[p] = tperm(ring[1], t, p); }
#pragma unroll
    for (int p = 0; p < 2; ++p) V2[p] = tperm(ring[2], t, p);
#pragma unroll
    for (int q = 0; q < 5; ++q) {
      f32x4 o = mfma32(u32x4{V2[0], V2[1], 0u, 0u}, u32x4{Pl[q][0], Pl[q][1], 0u, 0u}, f32x4{0.f, 0.f, 0.f, 0.f});
      o = mfma32(V0, Pf[q][0], o);
      acc[t][q] = mfma32(V1, Pf[q][1], o);
    }
  }
  SB();
#pragma unroll
  for (int j = 0; j < 8; ++j) LOADO(0, 1, j);
#pragma unroll
  for (int j = 0; j < 8; ++j) LOADO(1, 2, j);
#pragma unroll
  for (int j = 0; j < 8; ++j) LOADO(2, 3, j);
  SB();
  DUMP(1);
  bar();
  {
    const u32 ow = lds0 + li * 5120 + (((16 * w + 4 * g) ^ li) * 16);
#pragma unroll
    for (int q = 0; q < 5; ++q)
#pragma unroll
      for (int r = 0; r < 4; ++r) {
        u32x4 o;
#pragma unroll
        for (int p = 0; p < 4; ++p) o[p] = cvtpk(acc[2 * p][q][r], acc[2 * p + 1][q][r]);
        ldsw((ow ^ (r << 4)) + q * 1024, o);
      }
  }
  bar();
  STAMP(4);

#pragma unroll
  for (int t = 0; t < 8; ++t)
#pragma unroll
    for (int q = 0; q < 5; ++q) acc[t][q] = f32x4{0.f, 0.f, 0.f, 0.f};
#pragma unroll
  for (int q = 0; q < 5; ++q) B[0][q] = ldsr(bb + q * 1024);
#pragma unroll
  for (int p = 0; p < 4; ++p) F[0][p] = tperm(ring[3], 0, p);
  SB();
  sfor<16>([&](auto S_) {
    constexpr int s = decltype(S_)::value;
    const u32 bn = bb ^ ((s + 1) << 6);
    sfor<8>([&](auto T_) {
      constexpr int t = decltype(T_)::value;
      sfor<5>([&](auto Q_) {
        constexpr int q = decltype(Q_)::value;
        acc[t][q] = mfma32(F[t & 1], B[s & 1][q], acc[t][q]);
        if constexpr (q < 4) {
          if constexpr (t < 7) F[(t + 1) & 1][q] = tperm(ring[(s + 3) & 3], t + 1, q);
          else if constexpr (s < 15) F[0][q] = tperm(ring[(s + 4) & 3], 0, q);
        } else {
          if constexpr (t < 5 && s < 15) B[(s + 1) & 1][t] = ldsr(bn + t * 1024);
          constexpr int sn = (t == 7) ? s + 4 : s + 3;
          constexpr int j = (t + 1) & 7;
          if constexpr ((t == 7 || s >= 1) && sn < 16) LOADO((sn + 3) & 3, sn, j);
        }
        SB();
      });
    });
  });
  STAMP(5);

  const u64 nonce = ((u64)nv[1] << 32) | nv[0];
  const u64 tag = tagof(nonce);
  const int own = 2 * w + (g >> 1);
  {
    const int vs = own * 81920 + (lane & 31) * 16;
    const int ss = WS_SLAB + grp * 655360 + hq * 10240;
    const u32 lw = lds0 + 81920 + (lane & 31) * 16;
#pragma unroll
    for (int q = 0; q < 5; ++q)
#pragma unroll
      for (int r = 0; r < 4; ++r) {
        if (own != hq) {
          u32x4 o;
#pragma unroll
          for (int p = 0; p < 4; ++p) o[p] = cvtpk(acc[2 * p][q][r], acc[2 * p + 1][q][r]);
          __builtin_amdgcn_raw_buffer_store_b128(o, rw, vs + (q * 4 + r) * 512, ss, 16);
        } else {
          u32x4 lo, hi;
#pragma unroll
          for (int p = 0; p < 4; ++p) { lo[p] = __float_as_uint(acc[p][q][r]); hi[p] = __float_as_uint(acc[4 + p][q][r]); }
          ldsw(lw + (q * 4 + r) * 512, lo);
          ldsw(lw + 10240 + (q * 4 + r) * 512, hi);
        }
      }
  }
  asm volatile("s_waitcnt vmcnt(0)" ::: "memory");
  bar();
  STAMP(6);
  if (w == 0) {
    if (lane < 8 && lane != hq) {
      u32x2 tv = {(u32)tag, (u32)(tag >> 32)};
      __builtin_amdgcn_raw_buffer_store_b64(tv, rw, lane * 128 + hq * 8, WS_FLAG + grp * 1024, 16);
    }
    const u32 mlo = __builtin_amdgcn_readlane((u32)tag, hq), mhi = __builtin_amdgcn_readlane((u32)(tag >> 32), hq);
    const bool skip = (lane & 7) == hq;
    for (int it = 0; it < (1 << 20); ++it) {
      u32x2 f = __builtin_amdgcn_raw_buffer_load_b64(rw, (lane & 7) * 8, WS_FLAG + grp * 1024 + hq * 128, 16);
      const bool ok = skip || (f[0] == mlo && f[1] == mhi);
      if (__builtin_amdgcn_ballot_w64(ok) == ~0ull) break;
      __builtin_amdgcn_s_sleep(1);
    }
    if (lane == 0) {
      u32x2 tv = {mlo, mhi};
      __builtin_amdgcn_raw_buffer_store_b64(tv, rw, 0, WS_NONCE + grp * 64 + hq * 8, 16);
    }
  }
  bar();
  STAMP(7);
  {
    u32x4 pv[3][7];
    const int ss = WS_SLAB + grp * 655360 + hq * 81920;
#pragma unroll
    for (int c = 0; c < 3; ++c)
      if (c < 2 || w < 2) {
#pragma unroll
        for (int k = 0; k < 7; ++k) pv[c][k] = __builtin_amdgcn_raw_buffer_load_b128(rw, tid * 16 + c * 4096, ss + ((hq + 1 + k) & 7) * 10240, 16);
      }
    GAS u8* ob = (GAS u8*)a.out + (size_t)b * 80 * 2048 + br * 1024 + hq * 128;
#pragma unroll
    for (int c = 0; c < 3; ++c)
      if (c < 2 || w < 2) {
        const int u = tid + 256 * c;
        u32x4 lo = ldsr(lds0 + 81920 + u * 16), hi = ldsr(lds0 + 81920 + 10240 + u * 16);
        float s[8];
#pragma unroll
        for (int p = 0; p < 4; ++p) { s[p] = __uint_as_float(lo[p]); s[4 + p] = __uint_as_float(hi[p]); }
#pragma unroll
        for (int k = 0; k < 7; ++k)
#pragma unroll
          for (int p = 0; p < 4; ++p) {
            s[2 * p] += __uint_as_float(pv[c][k][p] << 16);
            s[2 * p + 1] += __uint_as_float(pv[c][k][p] & 0xffff0000u);
          }
        u32x4 o;
#pragma unroll
        for (int p = 0; p < 4; ++p) o[p] = cvtpk(s[2 * p], s[2 * p + 1]);
        const int blk = u >> 5, qrow = (blk >> 2) * 16 + (u & 15), cb = ((u >> 4) & 1) * 4 + (blk & 3);
        *(GAS u32x4*)(ob + qrow * 2048 + cb * 16) = o;
      }
  }
  if (STAMPS) {
    asm volatile("s_waitcnt vmcnt(0)" ::: "memory");
    STAMP(8);
    u64 rt1 = __builtin_amdgcn_s_memrealtime();
    if (lane == 0) {
      GAS u64* sp = (GAS u64*)(a.ws + WS_STAMP) + (bid * 4 + w) * 16;
#pragma unroll
      for (int n = 0; n < 9; ++n) sp[n] = st[n];
      sp[14] = rt0; sp[15] = rt1;
    }
  }
}

extern "C" void kernel_launch(void* const* d_in, const int* in_sizes, int n_in, void* d_out, int out_size, void* d_ws,
                              size_t ws_size, hipStream_t stream) {
  Args a;
  for (int i = 0; i < 2; ++i) {
    a.x[i] = (const u8*)d_in[5 * i + 0];
    a.k[i] = (const u8*)d_in[5 * i + 1];
    a.v[i] = (const u8*)d_in[5 * i + 2];
    a.wq[i] = (const u8*)d_in[5 * i + 3];
    a.wo[i] = (const u8*)d_in[5 * i + 4];
  }
  a.out = (u8*)d_out;
  a.ws = (u8*)d_ws;
  fused_attn_kernel<<<256, 256, 0, stream>>>(a);
}
```

```cpp
#define TFIX 0
#include <hip/hip_runtime.h>
#include <stdint.h>
#include <utility>

typedef unsigned u32;
typedef unsigned long long u64;
typedef unsigned char u8;
typedef u32 u32x4 __attribute__((ext_vector_type(4)));
typedef u32 u32x2 __attribute__((ext_vector_type(2)));
typedef float f32x4 __attribute__((ext_vector_type(4)));
typedef float f32x2 __attribute__((ext_vector_type(2)));
typedef __bf16 bf16x8 __attribute__((ext_vector_type(8)));
typedef __bf16 bf16x2 __attribute__((ext_vector_type(2)));
typedef short s16x4 __attribute__((ext_vector_type(4)));
#define LAS __attribute__((address_space(3)))
#define GAS __attribute__((address_space(1)))
#define DI __device__ __forceinline__
#define SB() __builtin_amdgcn_sched_barrier(0)

#define STAMPS 0
#define AUX_KV 2
#define AUX_X 0

struct Args {
  const u8* x[2];
  const u8* k[2];
  const u8* v[2];
  const u8* wq[2];
  const u8* wo[2];
  u8* out;
  u8* ws;
};

constexpr u32 WS_NONCE = 0;
constexpr u32 WS_FLAG = 4096;
constexpr u32 WS_STAMP = 65536;
constexpr u32 WS_SLAB = 1u << 20;

DI u32 cvtpk(float lo, float hi) {
  f32x2 v = {lo, hi};
  return __builtin_bit_cast(u32, __builtin_convertvector(v, bf16x2));
}
DI f32x4 mfma32(u32x4 a, u32x4 b, f32x4 c) {
  return __builtin_amdgcn_mfma_f32_16x16x32_bf16(__builtin_bit_cast(bf16x8, a), __builtin_bit_cast(bf16x8, b), c, 0, 0, 0);
}
DI f32x4 mfma16(u32x2 a, u32x2 b, f32x4 c) {
  return __builtin_amdgcn_mfma_f32_16x16x16bf16_1k(__builtin_bit_cast(s16x4, a), __builtin_bit_cast(s16x4, b), c, 0, 0, 0);
}
DI u32 permlo(u32 r0, u32 r1) { return __builtin_amdgcn_perm(r1, r0, 0x05040100u); }
DI u32 permhi(u32 r0, u32 r1) { return __builtin_amdgcn_perm(r1, r0, 0x07060302u); }
DI u32x4 ldsr(u32 a) { return *(LAS u32x4*)(uintptr_t)a; }
DI void ldsw(u32 a, u32x4 v) { *(LAS u32x4*)(uintptr_t)a = v; }
DI void bar() { asm volatile("s_waitcnt lgkmcnt(0)\n\ts_barrier" ::: "memory"); }
DI float xmax(float x) {
  auto s = __builtin_amdgcn_permlane16_swap(__float_as_uint(x), __float_as_uint(x), false, false);
  x = fmaxf(__uint_as_float(s[0]), __uint_as_float(s[1]));
  auto t = __builtin_amdgcn_permlane32_swap(__float_as_uint(x), __float_as_uint(x), false, false);
  return fmaxf(__uint_as_float(t[0]), __uint_as_float(t[1]));
}
DI float xsum(float x) {
  auto s = __builtin_amdgcn_permlane16_swap(__float_as_uint(x), __float_as_uint(x), false, false);
  x = __uint_as_float(s[0]) + __uint_as_float(s[1]);
  auto t = __builtin_amdgcn_permlane32_swap(__float_as_uint(x), __float_as_uint(x), false, false);
  return __uint_as_float(t[0]) + __uint_as_float(t[1]);
}
DI u64 tagof(u64 n) { return n * 6364136223846793005ull + 1442695040888963407ull; }

template <class F, int... I>
DI void sfor_impl(F&& f, std::integer_sequence<int, I...>) { (f(std::integral_constant<int, I>{}), ...); }
template <int N, class F>
DI void sfor(F&& f) { sfor_impl(f, std::make_integer_sequence<int, N>{}); }

DI u32 tperm(const u32x4 (&R)[8], int t, int p) {
  return (t & 1) ? permhi(R[2 * p][t >> 1], R[2 * p + 1][t >> 1]) : permlo(R[2 * p][t >> 1], R[2 * p + 1][t >> 1]);
}

#define DUMP(which)
#define NSTAMP 9
#define STAMP(n) do { if (STAMPS) { SB(); st[n] = __builtin_amdgcn_s_memtime(); SB(); } } while (0)

__global__ void __launch_bounds__(256, 1) fused_attn_kernel(Args a) {
  __shared__ __attribute__((aligned(1024))) u8 smem[163840];
  const int bid = blockIdx.x, xcd = bid & 7, idx = bid >> 3;
  const int br = xcd >> 2;
  const int hq = (xcd & 3) * 2 + (idx & 1), b = idx >> 1;
  const int tid = threadIdx.x;
  const int lane = tid & 63, w = __builtin_amdgcn_readfirstlane(tid >> 6);
  const int li = lane & 15, g = lane >> 4;
  const int h = hq * 4 + w;
  const int rot = (b ^ ((hq & 1) << 3)) & 15;
  const int grp = br * 16 + b;
  u64 st[12];
  u64 rt0 = 0;
  rt0 = __builtin_amdgcn_s_memrealtime();
  while (__builtin_amdgcn_s_memrealtime() - rt0 < 2000) __builtin_amdgcn_s_sleep(1);
  STAMP(0);

  const u32 lds0 = (u32)(uintptr_t)(LAS u8*)smem;
  __amdgpu_buffer_rsrc_t rx = __builtin_amdgcn_make_buffer_rsrc((void*)(a.x[br] + (size_t)b * 81920), 0, 81920, 0x00020000);
  __amdgpu_buffer_rsrc_t rk = __builtin_amdgcn_make_buffer_rsrc((void*)(a.k[br] + (size_t)(b * 32 + h) * 20480), 0, 20480, 0x00020000);
  __amdgpu_buffer_rsrc_t rv = __builtin_amdgcn_make_buffer_rsrc((void*)(a.v[br] + (size_t)(b * 32 + h) * 20480), 0, 20480, 0x00020000);
  __amdgpu_buffer_rsrc_t rq = __builtin_amdgcn_make_buffer_rsrc((void*)(a.wq[br] + h * 256), 0, 512 * 8192 - h * 256, 0x00020000);
  __amdgpu_buffer_rsrc_t ro = __builtin_amdgcn_make_buffer_rsrc((void*)(a.wo[br] + (size_t)hq * 512 * 1024 + w * 256), 0, 512 * 1024 - w * 256, 0x00020000);
  __amdgpu_buffer_rsrc_t rw = __builtin_amdgcn_make_buffer_rsrc((void*)(a.ws), 0, WS_SLAB + 32 * 8 * 8 * 10240, 0x00020000);

  u32x2 nv = __builtin_amdgcn_raw_buffer_load_b64(rw, (lane & 7) * 8, WS_NONCE + grp * 64, 16);

  u32x4 ring[4][8];
  u32x4 stg[10];
  const int vq = (8 * g) * 8192 + li * 16;
  const int vo = (8 * g) * 1024 + li * 16;
  const int vx = (20 * w + g) * 1024 + li * 16;
  const int vk = g * 256 + li * 16;
  const int vv = (4 * g) * 256 + li * 16;

#define LOADW(slot, s, j) ring[slot][j] = __builtin_amdgcn_raw_buffer_load_b128(rq, vq + (j) * 8192, (((s) ^ rot) * 32) * 8192, 0)
#define LOADO(slot, s, j) ring[slot][j] = __builtin_amdgcn_raw_buffer_load_b128(ro, vo + (j) * 1024, (((s) ^ rot) * 32) * 1024, 0)
#define LOADX(cc, m) stg[((cc) & 1) * 5 + (m)] = __builtin_amdgcn_raw_buffer_load_b128(rx, vx + (m) * 4096, (((cc) ^ (rot >> 2)) * 256), AUX_X)
#define LOADK(m) stg[(m) % 10] = __builtin_amdgcn_raw_buffer_load_b128(rk, vk + (m) * 1024, 0, AUX_KV)
#define LOADV(slot, s, j) ring[slot][j] = __builtin_amdgcn_raw_buffer_load_b128(rv, vv + (32 * (s) + 16 * ((j) >> 2) + ((j) & 3)) * 256, 0, AUX_KV)

  u32 xw[5];
#pragma unroll
  for (int m = 0; m < 5; ++m) {
    const int q = 20 * w + 4 * m + g;
    xw[m] = lds0 + ((q & 15) * 5 + (q >> 4)) * 1024 + ((li ^ (q & 15)) * 16) + ((rot >> 2) * 256);
  }
#define WRITEX(cc, m) ldsw(xw[m] ^ ((cc) * 256), stg[((cc) & 1) * 5 + (m)])
  const u32 kbase = lds0 + 81920 + w * 20480;
  const u32 kw0 = kbase + g * 256 + ((li ^ ((g + 4) & 15)) * 16);
#define WRITEK(m) ldsw(((kw0 ^ (16 * ((((4 * (m) + 4) & 12) ^ 4)))) + (m) * 1024), stg[(m) % 10])
  const u32 kr0 = kbase + li * 256 + (((4 * g) ^ ((li + 4) & 15)) * 16);
  const u32 bb = lds0 + li * 5120 + (((g ^ li) * 16) ^ (rot << 6));

  SB();
  sfor<5>([&](auto M_) { LOADX(0, M_.value); SB(); });
  sfor<8>([&](auto J_) { LOADW(0, 0, J_.value); SB(); });
  sfor<5>([&](auto M_) { LOADX(1, M_.value); SB(); });
  sfor<8>([&](auto J_) { LOADW(1, 1, J_.value); SB(); });
  sfor<8>([&](auto J_) { LOADW(2, 2, J_.value); SB(); });
  sfor<8>([&](auto J_) { LOADW(3, 3, J_.value); SB(); });
#pragma unroll
  for (int m = 0; m < 5; ++m) WRITEX(0, m);
  bar();
  STAMP(1);

  f32x4 acc[8][5];
#pragma unroll
  for (int t = 0; t < 8; ++t)
#pragma unroll
    for (int q = 0; q < 5; ++q) acc[t][q] = f32x4{0.f, 0.f, 0.f, 0.f};
  u32x4 B[2][5];
  u32x4 F[2];
#pragma unroll
  for (int q = 0; q < 5; ++q) B[0][q] = ldsr(bb + q * 1024);
#pragma unroll
  for (int p = 0; p < 4; ++p) F[0][p] = tperm(ring[0], 0, p);
  SB();

  sfor<16>([&](auto S_) {
    constexpr int s = decltype(S_)::value;
    if constexpr (s == 3 || s == 7) bar();
    const u32 bn = bb ^ ((s + 1) << 6);
    sfor<8>([&](auto T_) {
      constexpr int t = decltype(T_)::value;
      sfor<5>([&](auto Q_) {
        constexpr int q = decltype(Q_)::value;
        acc[t][q] = mfma32(F[t & 1], B[s & 1][q], acc[t][q]);
        if constexpr (q < 4) {
          if constexpr (t < 7) F[(t + 1) & 1][q] = tperm(ring[s & 3], t + 1, q);
          else if constexpr (s < 15) F[0][q] = tperm(ring[(s + 1) & 3], 0, q);
        } else {
          if constexpr (t < 5 && s < 15) B[(s + 1) & 1][t] = ldsr(bn + t * 1024);
          constexpr int sn = (t == 7) ? s + 4 : s + 3;
          constexpr int j = (t + 1) & 7;
          if constexpr (t == 7 || s >= 1) {
            if constexpr (sn < 16) LOADW(sn & 3, sn, j);
            else if constexpr (sn < 18) LOADV(sn & 3, sn - 16, j);
            else if constexpr (sn == 18) { if constexpr (j < 4) LOADV(sn & 3, 2, j); }
            else LOADO(sn & 3, 0, j);
          }
          if constexpr (s == 0 && t < 5) { LOADX(2, t); }
          if constexpr (s == 1 && t < 5) { WRITEX(1, t); LOADX(3, t); }
          if constexpr (s == 3 && t < 5) { WRITEX(2, t); LOADK(t); }
          if constexpr (s == 4 && t < 5) { WRITEX(3, t); LOADK(5 + t); }
          if constexpr (s == 7 && t < 5) { WRITEK(t); LOADK(10 + t); }
          if constexpr (s == 8 && t < 5) { WRITEK(5 + t); LOADK(15 + t); }
          if constexpr (s == 11 && t < 5) { WRITEK(10 + t); }
          if constexpr (s == 12 && t < 5) { WRITEK(15 + t); }
        }
        SB();
      });
    });
  });
  STAMP(2);
  DUMP(0);
#pragma unroll
  for (int j = 1; j < 8; ++j) LOADO(3, 0, j);
  SB();

  u32x4 Kf[5][4];
#pragma unroll
  for (int kt = 0; kt < 5; ++kt)
#pragma unroll
    for (int s = 0; s < 4; ++s) Kf[kt][s] = ldsr((kr0 ^ (s << 4)) + kt * 4096);
  u32x4 Pf[5][2];
  u32x2 Pl[5];
  constexpr float C2 = 0.08838834764831845f * 1.4426950408889634f;
#pragma unroll
  for (int q = 0; q < 5; ++q) {
    u32x4 Qf[4];
#pragma unroll
    for (int s = 0; s < 4; ++s)
#pragma unroll
      for (int p = 0; p < 4; ++p) Qf[s][p] = cvtpk(acc[2 * p][q][s], acc[2 * p + 1][q][s]);
    f32x4 S[5];
#pragma unroll
    for (int kt = 0; kt < 5; ++kt) {
      S[kt] = f32x4{0.f, 0.f, 0.f, 0.f};
#pragma unroll
      for (int s = 0; s < 4; ++s) S[kt] = mfma32(Kf[kt][s], Qf[s], S[kt]);
    }
    float m = S[0][0];
#pragma unroll
    for (int kt = 0; kt < 5; ++kt)
#pragma unroll
      for (int r = 0; r < 4; ++r) m = fmaxf(m, S[kt][r]);
    m = xmax(m);
    const float nm = -m * C2;
    float l = 0.f;
#pragma unroll
    for (int kt = 0; kt < 5; ++kt)
#pragma unroll
      for (int r = 0; r < 4; ++r) { S[kt][r] = __builtin_amdgcn_exp2f(__builtin_fmaf(S[kt][r], C2, nm)); l += S[kt][r]; }
    l = xsum(l);
    const float il = __builtin_amdgcn_rcpf(l);
#pragma unroll
    for (int s = 0; s < 2; ++s)
#pragma unroll
      for (int p = 0; p < 4; ++p) Pf[q][s][p] = cvtpk(S[2 * s + (p >> 1)][2 * (p & 1)] * il, S[2 * s + (p >> 1)][2 * (p & 1) + 1] * il);
    Pl[q][0] = cvtpk(S[4][0] * il, S[4][1] * il);
    Pl[q][1] = cvtpk(S[4][2] * il, S[4][3] * il);
  }
  STAMP(3);
#pragma unroll
  for (int t = 0; t < 8; ++t) {
    u32x4 V0, V1;
    u32x2 V2;
#pragma unroll
    for (int p = 0; p < 4; ++p) { V0[p] = tperm(ring[0], t, p); V1[p] = tperm(ring[1], t, p); }
#pragma unroll
    for (int p = 0; p < 2; ++p) V2[p] = tperm(ring[2], t, p);
#pragma unroll
    for (int q = 0; q < 5; ++q) {
      f32x4 o = mfma32(u32x4{V2[0], V2[1], 0u, 0u}, u32x4{Pl[q][0], Pl[q][1], 0u, 0u}, f32x4{0.f, 0.f, 0.f, 0.f});
      o = mfma32(V0, Pf[q][0], o);
      acc[t][q] = mfma32(V1, Pf[q][1], o);
    }
  }
  SB();
#pragma unroll
  for (int j = 0; j < 8; ++j) LOADO(0, 1, j);
#pragma unroll
  for (int j = 0; j < 8; ++j) LOADO(1, 2, j);
#pragma unroll
  for (int j = 0; j < 8; ++j) LOADO(2, 3, j);
  SB();
  DUMP(1);
  bar();
  {
    const u32 ow = lds0 + li * 5120 + (((16 * w + 4 * g) ^ li) * 16);
#pragma unroll
    for (int q = 0; q < 5; ++q)
#pragma unroll
      for (int r = 0; r < 4; ++r) {
        u32x4 o;
#pragma unroll
        for (int p = 0; p < 4; ++p) o[p] = cvtpk(acc[2 * p][q][r], acc[2 * p + 1][q][r]);
        ldsw((ow ^ (r << 4)) + q * 1024, o);
      }
  }
  bar();
  STAMP(4);

#pragma unroll
  for (int t = 0; t < 8; ++t)
#pragma unroll
    for (int q = 0; q < 5; ++q) acc[t][q] = f32x4{0.f, 0.f, 0.f, 0.f};
#pragma unroll
  for (int q = 0; q < 5; ++q) B[0][q] = ldsr(bb + q * 1024);
#pragma unroll
  for (int p = 0; p < 4; ++p) F[0][p] = tperm(ring[3], 0, p);
  SB();
  sfor<16>([&](auto S_) {
    constexpr int s = decltype(S_)::value;
    const u32 bn = bb ^ ((s + 1) << 6);
    sfor<8>([&](auto T_) {
      constexpr int t = decltype(T_)::value;
      sfor<5>([&](auto Q_) {
        constexpr int q = decltype(Q_)::value;
        acc[t][q] = mfma32(F[t & 1], B[s & 1][q], acc[t][q]);
        if constexpr (q < 4) {
          if constexpr (t < 7) F[(t + 1) & 1][q] = tperm(ring[(s + 3) & 3], t + 1, q);
          else if constexpr (s < 15) F[0][q] = tperm(ring[(s + 4) & 3], 0, q);
        } else {
          if constexpr (t < 5 && s < 15) B[(s + 1) & 1][t] = ldsr(bn + t * 1024);
          constexpr int sn = (t == 7) ? s + 4 : s + 3;
          constexpr int j = (t + 1) & 7;
          if constexpr ((t == 7 || s >= 1) && sn < 16) LOADO((sn + 3) & 3, sn, j);
        }
        SB();
      });
    });
  });
  STAMP(5);

  const u64 nonce = ((u64)nv[1] << 32) | nv[0];
  const u64 tag = tagof(nonce);
  const int own = 2 * w + (g >> 1);
  {
    const int vs = own * 81920 + (lane & 31) * 16;
    const int ss = WS_SLAB + grp * 655360 + hq * 10240;
    const u32 lw = lds0 + 81920 + (lane & 31) * 16;
#pragma unroll
    for (int q = 0; q < 5; ++q)
#pragma unroll
      for (int r = 0; r < 4; ++r) {
        if (own != hq) {
          u32x4 o;
#pragma unroll
          for (int p = 0; p < 4; ++p) o[p] = cvtpk(acc[2 * p][q][r], acc[2 * p + 1][q][r]);
          __builtin_amdgcn_raw_buffer_store_b128(o, rw, vs + (q * 4 + r) * 512, ss, 16);
        } else {
          u32x4 lo, hi;
#pragma unroll
          for (int p = 0; p < 4; ++p) { lo[p] = __float_as_uint(acc[p][q][r]); hi[p] = __float_as_uint(acc[4 + p][q][r]); }
          ldsw(lw + (q * 4 + r) * 512, lo);
          ldsw(lw + 10240 + (q * 4 + r) * 512, hi);
        }
      }
  }
  asm volatile("s_waitcnt vmcnt(0)" ::: "memory");
  bar();
  STAMP(6);
  if (w == 0) {
    if (lane < 8 && lane != hq) {
      u32x2 tv = {(u32)tag, (u32)(tag >> 32)};
      __builtin_amdgcn_raw_buffer_store_b64(tv, rw, lane * 128 + hq * 8, WS_FLAG + grp * 1024, 16);
    }
    const u32 mlo = __builtin_amdgcn_readlane((u32)tag, hq), mhi = __builtin_amdgcn_readlane((u32)(tag >> 32), hq);
    const bool skip = (lane & 7) == hq;
    for (int it = 0; it < (1 << 20); ++it) {
      u32x2 f = __builtin_amdgcn_raw_buffer_load_b64(rw, (lane & 7) * 8, WS_FLAG + grp * 1024 + hq * 128, 16);
      const bool ok = skip || (f[0] == mlo && f[1] == mhi);
      if (__builtin_amdgcn_ballot_w64(ok) == ~0ull) break;
      __builtin_amdgcn_s_sleep(1);
    }
    if (lane == 0) {
      u32x2 tv = {mlo, mhi};
      __builtin_amdgcn_raw_buffer_store_b64(tv, rw, 0, WS_NONCE + grp * 64 + hq * 8, 16);
    }
  }
  bar();
  STAMP(7);
  {
    u32x4 pv[3][7];
    const int ss = WS_SLAB + grp * 655360 + hq * 81920;
#pragma unroll
    for (int c = 0; c < 3; ++c)
      if (c < 2 || w < 2) {
#pragma unroll
        for (int k = 0; k < 7; ++k) pv[c][k] = __builtin_amdgcn_raw_buffer_load_b128(rw, tid * 16 + c * 4096, ss + ((hq + 1 + k) & 7) * 10240, 16);
      }
    GAS u8* ob = (GAS u8*)a.out + (size_t)b * 80 * 2048 + br * 1024 + hq * 128;
#pragma unroll
    for (int c = 0; c < 3; ++c)
      if (c < 2 || w < 2) {
        const int u = tid + 256 * c;
        u32x4 lo = ldsr(lds0 + 81920 + u * 16), hi = ldsr(lds0 + 81920 + 10240 + u * 16);
        float s[8];
#pragma unroll
        for (int p = 0; p < 4; ++p) { s[p] = __uint_as_float(lo[p]); s[4 + p] = __uint_as_float(hi[p]); }
#pragma unroll
        for (int k = 0; k < 7; ++k)
#pragma unroll
          for (int p = 0; p < 4; ++p) {
            s[2 * p] += __uint_as_float(pv[c][k][p] << 16);
            s[2 * p + 1] += __uint_as_float(pv[c][k][p] & 0xffff0000u);
          }
        u32x4 o;
#pragma unroll
        for (int p = 0; p < 4; ++p) o[p] = cvtpk(s[2 * p], s[2 * p + 1]);
        const int blk = u >> 5, qrow = (blk >> 2) * 16 + (u & 15), cb = ((u >> 4) & 1) * 4 + (blk & 3);
        *(GAS u32x4*)(ob + qrow * 2048 + cb * 16) = o;
      }
  }
  asm volatile("s_waitcnt vmcnt(0)" ::: "memory");
  while (__builtin_amdgcn_s_memrealtime() - rt0 < TFIX) __builtin_amdgcn_s_sleep(1);
  if (STAMPS) {
    asm volatile("s_waitcnt vmcnt(0)" ::: "memory");
    STAMP(8);
    u64 rt1 = __builtin_amdgcn_s_memrealtime();
    if (lane == 0) {
      GAS u64* sp = (GAS u64*)(a.ws + WS_STAMP) + (bid * 4 + w) * 16;
#pragma unroll
      for (int n = 0; n < 9; ++n) sp[n] = st[n];
      sp[14] = rt0; sp[15] = rt1;
    }
  }
}

extern "C" void kernel_launch(void* const* d_in, const int* in_sizes, int n_in, void* d_out, int out_size, void* d_ws,
                              size_t ws_size, hipStream_t stream) {
  Args a;
  for (int i = 0; i < 2; ++i) {
    a.x[i] = (const u8*)d_in[5 * i + 0];
    a.k[i] = (const u8*)d_in[5 * i + 1];
    a.v[i] = (const u8*)d_in[5 * i + 2];
    a.wq[i] = (const u8*)d_in[5 * i + 3];
    a.wo[i] = (const u8*)d_in[5 * i + 4];
  }
  a.out = (u8*)d_out;
  a.ws = (u8*)d_ws;
  fused_attn_kernel<<<256, 256, 0, stream>>>(a);
}
```

```cpp
#include <hip/hip_runtime.h>
#include <stdint.h>
#include <utility>

typedef unsigned u32;
typedef unsigned long long u64;
typedef unsigned char u8;
typedef u32 u32x4 __attribute__((ext_vector_type(4)));
typedef u32 u32x2 __attribute__((ext_vector_type(2)));
typedef float f32x4 __attribute__((ext_vector_type(4)));
typedef float f32x2 __attribute__((ext_vector_type(2)));
typedef __bf16 bf16x8 __attribute__((ext_vector_type(8)));
typedef __bf16 bf16x2 __attribute__((ext_vector_type(2)));
typedef short s16x4 __attribute__((ext_vector_type(4)));
#define LAS __attribute__((address_space(3)))
#define GAS __attribute__((address_space(1)))
#define DI __device__ __forceinline__
#define SB() __builtin_amdgcn_sched_barrier(0)

#define STAMPS 0
#define AUX_KV 2
#define AUX_W 0
#define AUX_O 0
#define AUX_X 2

struct Args {
  const u8* x[2];
  const u8* k[2];
  const u8* v[2];
  const u8* wq[2];
  const u8* wo[2];
  u8* out;
  u8* ws;
};

constexpr u32 WS_NONCE = 0;
constexpr u32 WS_FLAG = 4096;
constexpr u32 WS_STAMP = 65536;
constexpr u32 WS_SLAB = 1u << 20;

DI u32 cvtpk(float lo, float hi) {
  f32x2 v = {lo, hi};
  return __builtin_bit_cast(u32, __builtin_convertvector(v, bf16x2));
}
DI f32x4 mfma32(u32x4 a, u32x4 b, f32x4 c) {
  return __builtin_amdgcn_mfma_f32_16x16x32_bf16(__builtin_bit_cast(bf16x8, a), __builtin_bit_cast(bf16x8, b), c, 0, 0, 0);
}
DI f32x4 mfma16(u32x2 a, u32x2 b, f32x4 c) {
  return __builtin_amdgcn_mfma_f32_16x16x16bf16_1k(__builtin_bit_cast(s16x4, a), __builtin_bit_cast(s16x4, b), c, 0, 0, 0);
}
DI u32 permlo(u32 r0, u32 r1) { return __builtin_amdgcn_perm(r1, r0, 0x05040100u); }
DI u32 permhi(u32 r0, u32 r1) { return __builtin_amdgcn_perm(r1, r0, 0x07060302u); }
DI u32x4 ldsr(u32 a) { return *(LAS u32x4*)(uintptr_t)a; }
DI void ldsw(u32 a, u32x4 v) { *(LAS u32x4*)(uintptr_t)a = v; }
DI void bar() { asm volatile("s_waitcnt lgkmcnt(0)\n\ts_barrier" ::: "memory"); }
DI float xmax(float x) {
  auto s = __builtin_amdgcn_permlane16_swap(__float_as_uint(x), __float_as_uint(x), false, false);
  x = fmaxf(__uint_as_float(s[0]), __uint_as_float(s[1]));
  auto t = __builtin_amdgcn_permlane32_swap(__float_as_uint(x), __float_as_uint(x), false, false);
  return fmaxf(__uint_as_float(t[0]), __uint_as_float(t[1]));
}
DI float xsum(float x) {
  auto s = __builtin_amdgcn_permlane16_swap(__float_as_uint(x), __float_as_uint(x), false, false);
  x = __uint_as_float(s[0]) + __uint_as_float(s[1]);
  auto t = __builtin_amdgcn_permlane32_swap(__float_as_uint(x), __float_as_uint(x), false, false);
  return __uint_as_float(t[0]) + __uint_as_float(t[1]);
}
DI u64 tagof(u64 n) { return n * 6364136223846793005ull + 1442695040888963407ull; }

template <class F, int... I>
DI void sfor_impl(F&& f, std::integer_sequence<int, I...>) { (f(std::integral_constant<int, I>{}), ...); }
template <int N, class F>
DI void sfor(F&& f) { sfor_impl(f, std::make_integer_sequence<int, N>{}); }

DI u32 tperm(const u32x4 (&R)[8], int t, int p) {
  return (t & 1) ? permhi(R[2 * p][t >> 1], R[2 * p + 1][t >> 1]) : permlo(R[2 * p][t >> 1], R[2 * p + 1][t >> 1]);
}

#define DUMP(which)
#define NSTAMP 9
#define STAMP(n) do { if (STAMPS) { SB(); st[n] = __builtin_amdgcn_s_memtime(); SB(); } } while (0)

__global__ void __launch_bounds__(256, 1) fused_attn_kernel(Args a) {
  __shared__ __attribute__((aligned(1024))) u8 smem[163840];
  const int bid = blockIdx.x, xcd = bid & 7, idx = bid >> 3;
  const int br = xcd >> 2;
  const int hq = (xcd & 3) * 2 + (idx & 1), b = idx >> 1;
  const int tid = threadIdx.x;
  const int lane = tid & 63, w = __builtin_amdgcn_readfirstlane(tid >> 6);
  const int li = lane & 15, g = lane >> 4;
  const int h = hq * 4 + w;
  const int rot = (b ^ ((hq & 1) << 3)) & 15;
  const int grp = br * 16 + b;
  u64 st[12];
  u64 rt0 = 0;
  if (STAMPS) rt0 = __builtin_amdgcn_s_memrealtime();
  STAMP(0);

  const u32 lds0 = (u32)(uintptr_t)(LAS u8*)smem;
  __amdgpu_buffer_rsrc_t rx = __builtin_amdgcn_make_buffer_rsrc((void*)(a.x[br] + (size_t)b * 81920), 0, 81920, 0x00020000);
  __amdgpu_buffer_rsrc_t rk = __builtin_amdgcn_make_buffer_rsrc((void*)(a.k[br] + (size_t)(b * 32 + h) * 20480), 0, 20480, 0x00020000);
  __amdgpu_buffer_rsrc_t rv = __builtin_amdgcn_make_buffer_rsrc((void*)(a.v[br] + (size_t)(b * 32 + h) * 20480), 0, 20480, 0x00020000);
  __amdgpu_buffer_rsrc_t rq = __builtin_amdgcn_make_buffer_rsrc((void*)(a.wq[br] + h * 256), 0, 512 * 8192 - h * 256, 0x00020000);
  __amdgpu_buffer_rsrc_t ro = __builtin_amdgcn_make_buffer_rsrc((void*)(a.wo[br] + (size_t)hq * 512 * 1024 + w * 256), 0, 512 * 1024 - w * 256, 0x00020000);
  __amdgpu_buffer_rsrc_t rw = __builtin_amdgcn_make_buffer_rsrc((void*)(a.ws), 0, WS_SLAB + 32 * 8 * 8 * 10240, 0x00020000);

  u32x2 nv = __builtin_amdgcn_raw_buffer_load_b64(rw, (lane & 7) * 8, WS_NONCE + grp * 64, 16);

  u32x4 ring[4][8];
  u32x4 stg[10];
  const int vq = (8 * g) * 8192 + li * 16;
  const int vo = (8 * g) * 1024 + li * 16;
  const int vx = (20 * w + g) * 1024 + li * 16;
  const int vk = g * 256 + li * 16;
  const int vv = (4 * g) * 256 + li * 16;

#define LOADW(slot, s, j) ring[slot][j] = __builtin_amdgcn_raw_buffer_load_b128(rq, vq + (j) * 8192, (((s) ^ rot) * 32) * 8192, AUX_W)
#define LOADO(slot, s, j) ring[slot][j] = __builtin_amdgcn_raw_buffer_load_b128(ro, vo + (j) * 1024, (((s) ^ rot) * 32) * 1024, AUX_O)
#define LOADX(cc, m) stg[((cc) & 1) * 5 + (m)] = __builtin_amdgcn_raw_buffer_load_b128(rx, vx + (m) * 4096, (((cc) ^ (rot >> 2)) * 256), AUX_X)
#define LOADK(m) stg[(m) % 10] = __builtin_amdgcn_raw_buffer_load_b128(rk, vk + (m) * 1024, 0, AUX_KV)
#define LOADV(slot, s, j) ring[slot][j] = __builtin_amdgcn_raw_buffer_load_b128(rv, vv + (32 * (s) + 16 * ((j) >> 2) + ((j) & 3)) * 256, 0, AUX_KV)

  u32 xw[5];
#pragma unroll
  for (int m = 0; m < 5; ++m) {
    const int q = 20 * w + 4 * m + g;
    xw[m] = lds0 + ((q & 15) * 5 + (q >> 4)) * 1024 + ((li ^ (q & 15)) * 16) + ((rot >> 2) * 256);
  }
#define WRITEX(cc, m) ldsw(xw[m] ^ ((cc) * 256), stg[((cc) & 1) * 5 + (m)])
  const u32 kbase = lds0 + 81920 + w * 20480;
  const u32 kw0 = kbase + g * 256 + ((li ^ ((g + 4) & 15)) * 16);
#define WRITEK(m) ldsw(((kw0 ^ (16 * ((((4 * (m) + 4) & 12) ^ 4)))) + (m) * 1024), stg[(m) % 10])
  const u32 kr0 = kbase + li * 256 + (((4 * g) ^ ((li + 4) & 15)) * 16);
  const u32 bb = lds0 + li * 5120 + (((g ^ li) * 16) ^ (rot << 6));

  SB();
  sfor<5>([&](auto M_) { LOADX(0, M_.value); SB(); });
  sfor<8>([&](auto J_) { LOADW(0, 0, J_.value); SB(); });
  sfor<5>([&](auto M_) { LOADX(1, M_.value); SB(); });
  sfor<8>([&](auto J_) { LOADW(1, 1, J_.value); SB(); });
  sfor<8>([&](auto J_) { LOADW(2, 2, J_.value); SB(); });
  sfor<8>([&](auto J_) { LOADW(3, 3, J_.value); SB(); });
#pragma unroll
  for (int m = 0; m < 5; ++m) WRITEX(0, m);
  bar();
  STAMP(1);

  f32x4 acc[8][5];
#pragma unroll
  for (int t = 0; t < 8; ++t)
#pragma unroll
    for (int q = 0; q < 5; ++q) acc[t][q] = f32x4{0.f, 0.f, 0.f, 0.f};
  u32x4 B[2][5];
  u32x4 F[2];
#pragma unroll
  for (int q = 0; q < 5; ++q) B[0][q] = ldsr(bb + q * 1024);
#pragma unroll
  for (int p = 0; p < 4; ++p) F[0][p] = tperm(ring[0], 0, p);
  SB();

  sfor<16>([&](auto S_) {
    constexpr int s = decltype(S_)::value;
    if constexpr (s == 3 || s == 7) bar();
    const u32 bn = bb ^ ((s + 1) << 6);
    sfor<8>([&](auto T_) {
      constexpr int t = decltype(T_)::value;
      sfor<5>([&](auto Q_) {
        constexpr int q = decltype(Q_)::value;
        acc[t][q] = mfma32(F[t & 1], B[s & 1][q], acc[t][q]);
        if constexpr (q < 4) {
          if constexpr (t < 7) F[(t + 1) & 1][q] = tperm(ring[s & 3], t + 1, q);
          else if constexpr (s < 15) F[0][q] = tperm(ring[(s + 1) & 3], 0, q);
        } else {
          if constexpr (t < 5 && s < 15) B[(s + 1) & 1][t] = ldsr(bn + t * 1024);
          constexpr int sn = (t == 7) ? s + 4 : s + 3;
          constexpr int j = (t + 1) & 7;
          if constexpr (t == 7 || s >= 1) {
            if constexpr (sn < 16) LOADW(sn & 3, sn, j);
            else if constexpr (sn < 18) LOADV(sn & 3, sn - 16, j);
            else if constexpr (sn == 18) { if constexpr (j < 4) LOADV(sn & 3, 2, j); }
            else LOADO(sn & 3, 0, j);
          }
          if constexpr (s == 0 && t < 5) { LOADX(2, t); }
          if constexpr (s == 1 && t < 5) { WRITEX(1, t); LOADX(3, t); }
          if constexpr (s == 3 && t < 5) { WRITEX(2, t); LOADK(t); }
          if constexpr (s == 4 && t < 5) { WRITEX(3, t); LOADK(5 + t); }
          if constexpr (s == 7 && t < 5) { WRITEK(t); LOADK(10 + t); }
          if constexpr (s == 8 && t < 5) { WRITEK(5 + t); LOADK(15 + t); }
          if constexpr (s == 11 && t < 5) { WRITEK(10 + t); }
          if constexpr (s == 12 && t < 5) { WRITEK(15 + t); }
        }
        SB();
      });
    });
  });
  STAMP(2);
  DUMP(0);
#pragma unroll
  for (int j = 1; j < 8; ++j) LOADO(3, 0, j);
  SB();

  u32x4 Kf[5][4];
#pragma unroll
  for (int kt = 0; kt < 5; ++kt)
#pragma unroll
    for (int s = 0; s < 4; ++s) Kf[kt][s] = ldsr((kr0 ^ (s << 4)) + kt * 4096);
  u32x4 Pf[5][2];
  u32x2 Pl[5];
  constexpr float C2 = 0.08838834764831845f * 1.4426950408889634f;
#pragma unroll
  for (int q = 0; q < 5; ++q) {
    u32x4 Qf[4];
#pragma unroll
    for (int s = 0; s < 4; ++s)
#pragma unroll
      for (int p = 0; p < 4; ++p) Qf[s][p] = cvtpk(acc[2 * p][q][s], acc[2 * p + 1][q][s]);
    f32x4 S[5];
#pragma unroll
    for (int kt = 0; kt < 5; ++kt) {
      S[kt] = f32x4{0.f, 0.f, 0.f, 0.f};
#pragma unroll
      for (int s = 0; s < 4; ++s) S[kt] = mfma32(Kf[kt][s], Qf[s], S[kt]);
    }
    float m = S[0][0];
#pragma unroll
    for (int kt = 0; kt < 5; ++kt)
#pragma unroll
      for (int r = 0; r < 4; ++r) m = fmaxf(m, S[kt][r]);
    m = xmax(m);
    const float nm = -m * C2;
    float l = 0.f;
#pragma unroll
    for (int kt = 0; kt < 5; ++kt)
#pragma unroll
      for (int r = 0; r < 4; ++r) { S[kt][r] = __builtin_amdgcn_exp2f(__builtin_fmaf(S[kt][r], C2, nm)); l += S[kt][r]; }
    l = xsum(l);
    const float il = __builtin_amdgcn_rcpf(l);
#pragma unroll
    for (int s = 0; s < 2; ++s)
#pragma unroll
      for (int p = 0; p < 4; ++p) Pf[q][s][p] = cvtpk(S[2 * s + (p >> 1)][2 * (p & 1)] * il, S[2 * s + (p >> 1)][2 * (p & 1) + 1] * il);
    Pl[q][0] = cvtpk(S[4][0] * il, S[4][1] * il);
    Pl[q][1] = cvtpk(S[4][2] * il, S[4][3] * il);
  }
  STAMP(3);
#pragma unroll
  for (int t = 0; t < 8; ++t) {
    u32x4 V0, V1;
    u32x2 V2;
#pragma unroll
    for (int p = 0; p < 4; ++p) { V0[p] = tperm(ring[0], t, p); V1[p] = tperm(ring[1], t, p); }
#pragma unroll
    for (int p = 0; p < 2; ++p) V2[p] = tperm(ring[2], t, p);
#pragma unroll
    for (int q = 0; q < 5; ++q) {
      f32x4 o = mfma32(u32x4{V2[0], V2[1], 0u, 0u}, u32x4{Pl[q][0], Pl[q][1], 0u, 0u}, f32x4{0.f, 0.f, 0.f, 0.f});
      o = mfma32(V0, Pf[q][0], o);
      acc[t][q] = mfma32(V1, Pf[q][1], o);
    }
  }
  SB();
#pragma unroll
  for (int j = 0; j < 8; ++j) LOADO(0, 1, j);
#pragma unroll
  for (int j = 0; j < 8; ++j) LOADO(1, 2, j);
#pragma unroll
  for (int j = 0; j < 8; ++j) LOADO(2, 3, j);
  SB();
  DUMP(1);
  bar();
  {
    const u32 ow = lds0 + li * 5120 + (((16 * w + 4 * g) ^ li) * 16);
#pragma unroll
    for (int q = 0; q < 5; ++q)
#pragma unroll
      for (int r = 0; r < 4; ++r) {
        u32x4 o;
#pragma unroll
        for (int p = 0; p < 4; ++p) o[p] = cvtpk(acc[2 * p][q][r], acc[2 * p + 1][q][r]);
        ldsw((ow ^ (r << 4)) + q * 1024, o);
      }
  }
  bar();
  STAMP(4);

#pragma unroll
  for (int t = 0; t < 8; ++t)
#pragma unroll
    for (int q = 0; q < 5; ++q) acc[t][q] = f32x4{0.f, 0.f, 0.f, 0.f};
#pragma unroll
  for (int q = 0; q < 5; ++q) B[0][q] = ldsr(bb + q * 1024);
#pragma unroll
  for (int p = 0; p < 4; ++p) F[0][p] = tperm(ring[3], 0, p);
  SB();
  sfor<16>([&](auto S_) {
    constexpr int s = decltype(S_)::value;
    const u32 bn = bb ^ ((s + 1) << 6);
    sfor<8>([&](auto T_) {
      constexpr int t = decltype(T_)::value;
      sfor<5>([&](auto Q_) {
        constexpr int q = decltype(Q_)::value;
        acc[t][q] = mfma32(F[t & 1], B[s & 1][q], acc[t][q]);
        if constexpr (q < 4) {
          if constexpr (t < 7) F[(t + 1) & 1][q] = tperm(ring[(s + 3) & 3], t + 1, q);
          else if constexpr (s < 15) F[0][q] = tperm(ring[(s + 4) & 3], 0, q);
        } else {
          if constexpr (t < 5 && s < 15) B[(s + 1) & 1][t] = ldsr(bn + t * 1024);
          constexpr int sn = (t == 7) ? s + 4 : s + 3;
          constexpr int j = (t + 1) & 7;
          if constexpr ((t == 7 || s >= 1) && sn < 16) LOADO((sn + 3) & 3, sn, j);
        }
        SB();
      });
    });
  });
  STAMP(5);

  const u64 nonce = ((u64)nv[1] << 32) | nv[0];
  const u64 tag = tagof(nonce);
  const int own = 2 * w + (g >> 1);
  {
    const int vs = own * 81920 + (lane & 31) * 16;
    const int ss = WS_SLAB + grp * 655360 + hq * 10240;
    const u32 lw = lds0 + 81920 + (lane & 31) * 16;
#pragma unroll
    for (int q = 0; q < 5; ++q)
#pragma unroll
      for (int r = 0; r < 4; ++r) {
        if (own != hq) {
          u32x4 o;
#pragma unroll
          for (int p = 0; p < 4; ++p) o[p] = cvtpk(acc[2 * p][q][r], acc[2 * p + 1][q][r]);
          __builtin_amdgcn_raw_buffer_store_b128(o, rw, vs + (q * 4 + r) * 512, ss, 16);
        } else {
          u32x4 lo, hi;
#pragma unroll
          for (int p = 0; p < 4; ++p) { lo[p] = __float_as_uint(acc[p][q][r]); hi[p] = __float_as_uint(acc[4 + p][q][r]); }
          ldsw(lw + (q * 4 + r) * 512, lo);
          ldsw(lw + 10240 + (q * 4 + r) * 512, hi);
        }
      }
  }
  asm volatile("s_waitcnt vmcnt(0)" ::: "memory");
  bar();
  STAMP(6);
  if (w == 0) {
    if (lane < 8 && lane != hq) {
      u32x2 tv = {(u32)tag, (u32)(tag >> 32)};
      __builtin_amdgcn_raw_buffer_store_b64(tv, rw, lane * 128 + hq * 8, WS_FLAG + grp * 1024, 16);
    }
    const u32 mlo = __builtin_amdgcn_readlane((u32)tag, hq), mhi = __builtin_amdgcn_readlane((u32)(tag >> 32), hq);
    const bool skip = (lane & 7) == hq;
    for (int it = 0; it < (1 << 20); ++it) {
      u32x2 f = __builtin_amdgcn_raw_buffer_load_b64(rw, (lane & 7) * 8, WS_FLAG + grp * 1024 + hq * 128, 16);
      const bool ok = skip || (f[0] == mlo && f[1] == mhi);
      if (__builtin_amdgcn_ballot_w64(ok) == ~0ull) break;
      __builtin_amdgcn_s_sleep(1);
    }
    if (lane == 0) {
      u32x2 tv = {mlo, mhi};
      __builtin_amdgcn_raw_buffer_store_b64(tv, rw, 0, WS_NONCE + grp * 64 + hq * 8, 16);
    }
  }
  bar();
  STAMP(7);
  {
    u32x4 pv[3][7];
    const int ss = WS_SLAB + grp * 655360 + hq * 81920;
#pragma unroll
    for (int c = 0; c < 3; ++c)
      if (c < 2 || w < 2) {
#pragma unroll
        for (int k = 0; k < 7; ++k) pv[c][k] = __builtin_amdgcn_raw_buffer_load_b128(rw, tid * 16 + c * 4096, ss + ((hq + 1 + k) & 7) * 10240, 16);
      }
    GAS u8* ob = (GAS u8*)a.out + (size_t)b * 80 * 2048 + br * 1024 + hq * 128;
#pragma unroll
    for (int c = 0; c < 3; ++c)
      if (c < 2 || w < 2) {
        const int u = tid + 256 * c;
        u32x4 lo = ldsr(lds0 + 81920 + u * 16), hi = ldsr(lds0 + 81920 + 10240 + u * 16);
        float s[8];
#pragma unroll
        for (int p = 0; p < 4; ++p) { s[p] = __uint_as_float(lo[p]); s[4 + p] = __uint_as_float(hi[p]); }
#pragma unroll
        for (int k = 0; k < 7; ++k)
#pragma unroll
          for (int p = 0; p < 4; ++p) {
            s[2 * p] += __uint_as_float(pv[c][k][p] << 16);
            s[2 * p + 1] += __uint_as_float(pv[c][k][p] & 0xffff0000u);
          }
        u32x4 o;
#pragma unroll
        for (int p = 0; p < 4; ++p) o[p] = cvtpk(s[2 * p], s[2 * p + 1]);
        const int blk = u >> 5, qrow = (blk >> 2) * 16 + (u & 15), cb = ((u >> 4) & 1) * 4 + (blk & 3);
        *(GAS u32x4*)(ob + qrow * 2048 + cb * 16) = o;
      }
  }
  if (STAMPS) {
    asm volatile("s_waitcnt vmcnt(0)" ::: "memory");
    STAMP(8);
    u64 rt1 = __builtin_amdgcn_s_memrealtime();
    if (lane == 0) {
      GAS u64* sp = (GAS u64*)(a.ws + WS_STAMP) + (bid * 4 + w) * 16;
#pragma unroll
      for (int n = 0; n < 9; ++n) sp[n] = st[n];
      sp[14] = rt0; sp[15] = rt1;
    }
  }
}

extern "C" void kernel_launch(void* const* d_in, const int* in_sizes, int n_in, void* d_out, int out_size, void* d_ws,
                              size_t ws_size, hipStream_t stream) {
  Args a;
  for (int i = 0; i < 2; ++i) {
    a.x[i] = (const u8*)d_in[5 * i + 0];
    a.k[i] = (const u8*)d_in[5 * i + 1];
    a.v[i] = (const u8*)d_in[5 * i + 2];
    a.wq[i] = (const u8*)d_in[5 * i + 3];
    a.wo[i] = (const u8*)d_in[5 * i + 4];
  }
  a.out = (u8*)d_out;
  a.ws = (u8*)d_ws;
  fused_attn_kernel<<<256, 256, 0, stream>>>(a);
}
```

```cpp
#define PKIND 2
#define PAUX 0
#define TFIX 7000
#include <hip/hip_runtime.h>
#include <stdint.h>
#include <utility>

typedef unsigned u32;
typedef unsigned long long u64;
typedef unsigned char u8;
typedef u32 u32x4 __attribute__((ext_vector_type(4)));
typedef u32 u32x2 __attribute__((ext_vector_type(2)));
typedef float f32x4 __attribute__((ext_vector_type(4)));
typedef float f32x2 __attribute__((ext_vector_type(2)));
typedef __bf16 bf16x8 __attribute__((ext_vector_type(8)));
typedef __bf16 bf16x2 __attribute__((ext_vector_type(2)));
typedef short s16x4 __attribute__((ext_vector_type(4)));
#define LAS __attribute__((address_space(3)))
#define GAS __attribute__((address_space(1)))
#define DI __device__ __forceinline__
#define SB() __builtin_amdgcn_sched_barrier(0)

#define STAMPS 0
#define AUX_KV 2
#define AUX_X 0

struct Args {
  const u8* x[2];
  const u8* k[2];
  const u8* v[2];
  const u8* wq[2];
  const u8* wo[2];
  u8* out;
  u8* ws;
};

constexpr u32 WS_NONCE = 0;
constexpr u32 WS_FLAG = 4096;
constexpr u32 WS_STAMP = 65536;
constexpr u32 WS_SLAB = 1u << 20;

DI u32 cvtpk(float lo, float hi) {
  f32x2 v = {lo, hi};
  return __builtin_bit_cast(u32, __builtin_convertvector(v, bf16x2));
}
DI f32x4 mfma32(u32x4 a, u32x4 b, f32x4 c) {
  return __builtin_amdgcn_mfma_f32_16x16x32_bf16(__builtin_bit_cast(bf16x8, a), __builtin_bit_cast(bf16x8, b), c, 0, 0, 0);
}
DI f32x4 mfma16(u32x2 a, u32x2 b, f32x4 c) {
  return __builtin_amdgcn_mfma_f32_16x16x16bf16_1k(__builtin_bit_cast(s16x4, a), __builtin_bit_cast(s16x4, b), c, 0, 0, 0);
}
DI u32 permlo(u32 r0, u32 r1) { return __builtin_amdgcn_perm(r1, r0, 0x05040100u); }
DI u32 permhi(u32 r0, u32 r1) { return __builtin_amdgcn_perm(r1, r0, 0x07060302u); }
DI u32x4 ldsr(u32 a) { return *(LAS u32x4*)(uintptr_t)a; }
DI void ldsw(u32 a, u32x4 v) { *(LAS u32x4*)(uintptr_t)a = v; }
DI void bar() { asm volatile("s_waitcnt lgkmcnt(0)\n\ts_barrier" ::: "memory"); }
DI float xmax(float x) {
  auto s = __builtin_amdgcn_permlane16_swap(__float_as_uint(x), __float_as_uint(x), false, false);
  x = fmaxf(__uint_as_float(s[0]), __uint_as_float(s[1]));
  auto t = __builtin_amdgcn_permlane32_swap(__float_as_uint(x), __float_as_uint(x), false, false);
  return fmaxf(__uint_as_float(t[0]), __uint_as_float(t[1]));
}
DI float xsum(float x) {
  auto s = __builtin_amdgcn_permlane16_swap(__float_as_uint(x), __float_as_uint(x), false, false);
  x = __uint_as_float(s[0]) + __uint_as_float(s[1]);
  auto t = __builtin_amdgcn_permlane32_swap(__float_as_uint(x), __float_as_uint(x), false, false);
  return __uint_as_float(t[0]) + __uint_as_float(t[1]);
}
DI u64 tagof(u64 n) { return n * 6364136223846793005ull + 1442695040888963407ull; }

template <class F, int... I>
DI void sfor_impl(F&& f, std::integer_sequence<int, I...>) { (f(std::integral_constant<int, I>{}), ...); }
template <int N, class F>
DI void sfor(F&& f) { sfor_impl(f, std::make_integer_sequence<int, N>{}); }

DI u32 tperm(const u32x4 (&R)[8], int t, int p) {
  return (t & 1) ? permhi(R[2 * p][t >> 1], R[2 * p + 1][t >> 1]) : permlo(R[2 * p][t >> 1], R[2 * p + 1][t >> 1]);
}

#define DUMP(which)
#define NSTAMP 9
#define STAMP(n) do { if (STAMPS) { SB(); st[n] = __builtin_amdgcn_s_memtime(); SB(); } } while (0)

__global__ void __launch_bounds__(256, 1) fused_attn_kernel(Args a) {
  __shared__ __attribute__((aligned(1024))) u8 smem[163840];
  const int bid = blockIdx.x, xcd = bid & 7, idx = bid >> 3;
  const int br = xcd >> 2;
  const int hq = (xcd & 3) * 2 + (idx & 1), b = idx >> 1;
  const int tid = threadIdx.x;
  const int lane = tid & 63, w = __builtin_amdgcn_readfirstlane(tid >> 6);
  const int li = lane & 15, g = lane >> 4;
  const int h = hq * 4 + w;
  const int rot = (b ^ ((hq & 1) << 3)) & 15;
  const int grp = br * 16 + b;
  u64 st[12];
  u64 rt0 = 0;
  rt0 = __builtin_amdgcn_s_memrealtime();
  STAMP(0);

  const u32 lds0 = (u32)(uintptr_t)(LAS u8*)smem;
  __amdgpu_buffer_rsrc_t rx = __builtin_amdgcn_make_buffer_rsrc((void*)(a.x[br] + (size_t)b * 81920), 0, 81920, 0x00020000);
  __amdgpu_buffer_rsrc_t rk = __builtin_amdgcn_make_buffer_rsrc((void*)(a.k[br] + (size_t)(b * 32 + h) * 20480), 0, 20480, 0x00020000);
  __amdgpu_buffer_rsrc_t rv = __builtin_amdgcn_make_buffer_rsrc((void*)(a.v[br] + (size_t)(b * 32 + h) * 20480), 0, 20480, 0x00020000);
  __amdgpu_buffer_rsrc_t rq = __builtin_amdgcn_make_buffer_rsrc((void*)(a.wq[br] + h * 256), 0, 512 * 8192 - h * 256, 0x00020000);
  __amdgpu_buffer_rsrc_t ro = __builtin_amdgcn_make_buffer_rsrc((void*)(a.wo[br] + (size_t)hq * 512 * 1024 + w * 256), 0, 512 * 1024 - w * 256, 0x00020000);
  __amdgpu_buffer_rsrc_t rw = __builtin_amdgcn_make_buffer_rsrc((void*)(a.ws), 0, WS_SLAB + 32 * 8 * 8 * 10240, 0x00020000);

  u64 pdt;
  {
    u32x4 pb[40];
    SB();
    pb[0] = __builtin_amdgcn_raw_buffer_load_b128(rk, lane * 16, 0, PAUX);
    asm volatile("" :: "v"(pb[0]));
    SB();
    pdt = __builtin_amdgcn_s_memrealtime() - rt0;
    SB();
  }
  u32x2 nv = __builtin_amdgcn_raw_buffer_load_b64(rw, (lane & 7) * 8, WS_NONCE + grp * 64, 16);

  u32x4 ring[4][8];
  u32x4 stg[10];
  const int vq = (8 * g) * 8192 + li * 16;
  const int vo = (8 * g) * 1024 + li * 16;
  const int vx = (20 * w + g) * 1024 + li * 16;
  const int vk = g * 256 + li * 16;
  const int vv = (4 * g) * 256 + li * 16;

#define LOADW(slot, s, j) ring[slot][j] = __builtin_amdgcn_raw_buffer_load_b128(rq, vq + (j) * 8192, (((s) ^ rot) * 32) * 8192, 0)
#define LOADO(slot, s, j) ring[slot][j] = __builtin_amdgcn_raw_buffer_load_b128(ro, vo + (j) * 1024, (((s) ^ rot) * 32) * 1024, 0)
#define LOADX(cc, m) stg[((cc) & 1) * 5 + (m)] = __builtin_amdgcn_raw_buffer_load_b128(rx, vx + (m) * 4096, (((cc) ^ (rot >> 2)) * 256), AUX_X)
#define LOADK(m) stg[(m) % 10] = __builtin_amdgcn_raw_buffer_load_b128(rk, vk + (m) * 1024, 0, AUX_KV)
#define LOADV(slot, s, j) ring[slot][j] = __builtin_amdgcn_raw_buffer_load_b128(rv, vv + (32 * (s) + 16 * ((j) >> 2) + ((j) & 3)) * 256, 0, AUX_KV)

  u32 xw[5];
#pragma unroll
  for (int m = 0; m < 5; ++m) {
    const int q = 20 * w + 4 * m + g;
    xw[m] = lds0 + ((q & 15) * 5 + (q >> 4)) * 1024 + ((li ^ (q & 15)) * 16) + ((rot >> 2) * 256);
  }
#define WRITEX(cc, m) ldsw(xw[m] ^ ((cc) * 256), stg[((cc) & 1) * 5 + (m)])
  const u32 kbase = lds0 + 81920 + w * 20480;
  const u32 kw0 = kbase + g * 256 + ((li ^ ((g + 4) & 15)) * 16);
#define WRITEK(m) ldsw(((kw0 ^ (16 * ((((4 * (m) + 4) & 12) ^ 4)))) + (m) * 1024), stg[(m) % 10])
  const u32 kr0 = kbase + li * 256 + (((4 * g) ^ ((li + 4) & 15)) * 16);
  const u32 bb = lds0 + li * 5120 + (((g ^ li) * 16) ^ (rot << 6));

  SB();
  sfor<5>([&](auto M_) { LOADX(0, M_.value); SB(); });
  sfor<8>([&](auto J_) { LOADW(0, 0, J_.value); SB(); });
  sfor<5>([&](auto M_) { LOADX(1, M_.value); SB(); });
  sfor<8>([&](auto J_) { LOADW(1, 1, J_.value); SB(); });
  sfor<8>([&](auto J_) { LOADW(2, 2, J_.value); SB(); });
  sfor<8>([&](auto J_) { LOADW(3, 3, J_.value); SB(); });
#pragma unroll
  for (int m = 0; m < 5; ++m) WRITEX(0, m);
  bar();
  STAMP(1);

  f32x4 acc[8][5];
#pragma unroll
  for (int t = 0; t < 8; ++t)
#pragma unroll
    for (int q = 0; q < 5; ++q) acc[t][q] = f32x4{0.f, 0.f, 0.f, 0.f};
  u32x4 B[2][5];
  u32x4 F[2];
#pragma unroll
  for (int q = 0; q < 5; ++q) B[0][q] = ldsr(bb + q * 1024);
#pragma unroll
  for (int p = 0; p < 4; ++p) F[0][p] = tperm(ring[0], 0, p);
  SB();

  sfor<16>([&](auto S_) {
    constexpr int s = decltype(S_)::value;
    if constexpr (s == 3 || s == 7) bar();
    const u32 bn = bb ^ ((s + 1) << 6);
    sfor<8>([&](auto T_) {
      constexpr int t = decltype(T_)::value;
      sfor<5>([&](auto Q_) {
        constexpr int q = decltype(Q_)::value;
        acc[t][q] = mfma32(F[t & 1], B[s & 1][q], acc[t][q]);
        if constexpr (q < 4) {
          if constexpr (t < 7) F[(t + 1) & 1][q] = tperm(ring[s & 3], t + 1, q);
          else if constexpr (s < 15) F[0][q] = tperm(ring[(s + 1) & 3], 0, q);
        } else {
          if constexpr (t < 5 && s < 15) B[(s + 1) & 1][t] = ldsr(bn + t * 1024);
          constexpr int sn = (t == 7) ? s + 4 : s + 3;
          constexpr int j = (t + 1) & 7;
          if constexpr (t == 7 || s >= 1) {
            if constexpr (sn < 16) LOADW(sn & 3, sn, j);
            else if constexpr (sn < 18) LOADV(sn & 3, sn - 16, j);
            else if constexpr (sn == 18) { if constexpr (j < 4) LOADV(sn & 3, 2, j); }
            else LOADO(sn & 3, 0, j);
          }
          if constexpr (s == 0 && t < 5) { LOADX(2, t); }
          if constexpr (s == 1 && t < 5) { WRITEX(1, t); LOADX(3, t); }
          if constexpr (s == 3 && t < 5) { WRITEX(2, t); LOADK(t); }
          if constexpr (s == 4 && t < 5) { WRITEX(3, t); LOADK(5 + t); }
          if constexpr (s == 7 && t < 5) { WRITEK(t); LOADK(10 + t); }
          if constexpr (s == 8 && t < 5) { WRITEK(5 + t); LOADK(15 + t); }
          if constexpr (s == 11 && t < 5) { WRITEK(10 + t); }
          if constexpr (s == 12 && t < 5) { WRITEK(15 + t); }
        }
        SB();
      });
    });
  });
  STAMP(2);
  DUMP(0);
#pragma unroll
  for (int j = 1; j < 8; ++j) LOADO(3, 0, j);
  SB();

  u32x4 Kf[5][4];
#pragma unroll
  for (int kt = 0; kt < 5; ++kt)
#pragma unroll
    for (int s = 0; s < 4; ++s) Kf[kt][s] = ldsr((kr0 ^ (s << 4)) + kt * 4096);
  u32x4 Pf[5][2];
  u32x2 Pl[5];
  constexpr float C2 = 0.08838834764831845f * 1.4426950408889634f;
#pragma unroll
  for (int q = 0; q < 5; ++q) {
    u32x4 Qf[4];
#pragma unroll
    for (int s = 0; s < 4; ++s)
#pragma unroll
      for (int p = 0; p < 4; ++p) Qf[s][p] = cvtpk(acc[2 * p][q][s], acc[2 * p + 1][q][s]);
    f32x4 S[5];
#pragma unroll
    for (int kt = 0; kt < 5; ++kt) {
      S[kt] = f32x4{0.f, 0.f, 0.f, 0.f};
#pragma unroll
      for (int s = 0; s < 4; ++s) S[kt] = mfma32(Kf[kt][s], Qf[s], S[kt]);
    }
    float m = S[0][0];
#pragma unroll
    for (int kt = 0; kt < 5; ++kt)
#pragma unroll
      for (int r = 0; r < 4; ++r) m = fmaxf(m, S[kt][r]);
    m = xmax(m);
    const float nm = -m * C2;
    float l = 0.f;
#pragma unroll
    for (int kt = 0; kt < 5; ++kt)
#pragma unroll
      for (int r = 0; r < 4; ++r) { S[kt][r] = __builtin_amdgcn_exp2f(__builtin_fmaf(S[kt][r], C2, nm)); l += S[kt][r]; }
    l = xsum(l);
    const float il = __builtin_amdgcn_rcpf(l);
#pragma unroll
    for (int s = 0; s < 2; ++s)
#pragma unroll
      for (int p = 0; p < 4; ++p) Pf[q][s][p] = cvtpk(S[2 * s + (p >> 1)][2 * (p & 1)] * il, S[2 * s + (p >> 1)][2 * (p & 1) + 1] * il);
    Pl[q][0] = cvtpk(S[4][0] * il, S[4][1] * il);
    Pl[q][1] = cvtpk(S[4][2] * il, S[4][3] * il);
  }
  STAMP(3);
#pragma unroll
  for (int t = 0; t < 8; ++t) {
    u32x4 V0, V1;
    u32x2 V2;
#pragma unroll
    for (int p = 0; p < 4; ++p) { V0[p] = tperm(ring[0], t, p); V1[p] = tperm(ring[1], t, p); }
#pragma unroll
    for (int p = 0; p < 2; ++p) V2[p] = tperm(ring[2], t, p);
#pragma unroll
    for (int q = 0; q < 5; ++q) {
      f32x4 o = mfma32(u32x4{V2[0], V2[1], 0u, 0u}, u32x4{Pl[q][0], Pl[q][1], 0u, 0u}, f32x4{0.f, 0.f, 0.f, 0.f});
      o = mfma32(V0, Pf[q][0], o);
      acc[t][q] = mfma32(V1, Pf[q][1], o);
    }
  }
  SB();
#pragma unroll
  for (int j = 0; j < 8; ++j) LOADO(0, 1, j);
#pragma unroll
  for (int j = 0; j < 8; ++j) LOADO(1, 2, j);
#pragma unroll
  for (int j = 0; j < 8; ++j) LOADO(2, 3, j);
  SB();
  DUMP(1);
  bar();
  {
    const u32 ow = lds0 + li * 5120 + (((16 * w + 4 * g) ^ li) * 16);
#pragma unroll
    for (int q = 0; q < 5; ++q)
#pragma unroll
      for (int r = 0; r < 4; ++r) {
        u32x4 o;
#pragma unroll
        for (int p = 0; p < 4; ++p) o[p] = cvtpk(acc[2 * p][q][r], acc[2 * p + 1][q][r]);
        ldsw((ow ^ (r << 4)) + q * 1024, o);
      }
  }
  bar();
  STAMP(4);

#pragma unroll
  for (int t = 0; t < 8; ++t)
#pragma unroll
    for (int q = 0; q < 5; ++q) acc[t][q] = f32x4{0.f, 0.f, 0.f, 0.f};
#pragma unroll
  for (int q = 0; q < 5; ++q) B[0][q] = ldsr(bb + q * 1024);
#pragma unroll
  for (int p = 0; p < 4; ++p) F[0][p] = tperm(ring[3], 0, p);
  SB();
  sfor<16>([&](auto S_) {
    constexpr int s = decltype(S_)::value;
    const u32 bn = bb ^ ((s + 1) << 6);
    sfor<8>([&](auto T_) {
      constexpr int t = decltype(T_)::value;
      sfor<5>([&](auto Q_) {
        constexpr int q = decltype(Q_)::value;
        acc[t][q] = mfma32(F[t & 1], B[s & 1][q], acc[t][q]);
        if constexpr (q < 4) {
          if constexpr (t < 7) F[(t + 1) & 1][q] = tperm(ring[(s + 3) & 3], t + 1, q);
          else if constexpr (s < 15) F[0][q] = tperm(ring[(s + 4) & 3], 0, q);
        } else {
          if constexpr (t < 5 && s < 15) B[(s + 1) & 1][t] = ldsr(bn + t * 1024);
          constexpr int sn = (t == 7) ? s + 4 : s + 3;
          constexpr int j = (t + 1) & 7;
          if constexpr ((t == 7 || s >= 1) && sn < 16) LOADO((sn + 3) & 3, sn, j);
        }
        SB();
      });
    });
  });
  STAMP(5);

  const u64 nonce = ((u64)nv[1] << 32) | nv[0];
  const u64 tag = tagof(nonce);
  const int own = 2 * w + (g >> 1);
  {
    const int vs = own * 81920 + (lane & 31) * 16;
    const int ss = WS_SLAB + grp * 655360 + hq * 10240;
    const u32 lw = lds0 + 81920 + (lane & 31) * 16;
#pragma unroll
    for (int q = 0; q < 5; ++q)
#pragma unroll
      for (int r = 0; r < 4; ++r) {
        if (own != hq) {
          u32x4 o;
#pragma unroll
          for (int p = 0; p < 4; ++p) o[p] = cvtpk(acc[2 * p][q][r], acc[2 * p + 1][q][r]);
          __builtin_amdgcn_raw_buffer_store_b128(o, rw, vs + (q * 4 + r) * 512, ss, 16);
        } else {
          u32x4 lo, hi;
#pragma unroll
          for (int p = 0; p < 4; ++p) { lo[p] = __float_as_uint(acc[p][q][r]); hi[p] = __float_as_uint(acc[4 + p][q][r]); }
          ldsw(lw + (q * 4 + r) * 512, lo);
          ldsw(lw + 10240 + (q * 4 + r) * 512, hi);
        }
      }
  }
  asm volatile("s_waitcnt vmcnt(0)" ::: "memory");
  bar();
  STAMP(6);
  if (w == 0) {
    if (lane < 8 && lane != hq) {
      u32x2 tv = {(u32)tag, (u32)(tag >> 32)};
      __builtin_amdgcn_raw_buffer_store_b64(tv, rw, lane * 128 + hq * 8, WS_FLAG + grp * 1024, 16);
    }
    const u32 mlo = __builtin_amdgcn_readlane((u32)tag, hq), mhi = __builtin_amdgcn_readlane((u32)(tag >> 32), hq);
    const bool skip = (lane & 7) == hq;
    for (int it = 0; it < (1 << 20); ++it) {
      u32x2 f = __builtin_amdgcn_raw_buffer_load_b64(rw, (lane & 7) * 8, WS_FLAG + grp * 1024 + hq * 128, 16);
      const bool ok = skip || (f[0] == mlo && f[1] == mhi);
      if (__builtin_amdgcn_ballot_w64(ok) == ~0ull) break;
      __builtin_amdgcn_s_sleep(1);
    }
    if (lane == 0) {
      u32x2 tv = {mlo, mhi};
      __builtin_amdgcn_raw_buffer_store_b64(tv, rw, 0, WS_NONCE + grp * 64 + hq * 8, 16);
    }
  }
  bar();
  STAMP(7);
  {
    u32x4 pv[3][7];
    const int ss = WS_SLAB + grp * 655360 + hq * 81920;
#pragma unroll
    for (int c = 0; c < 3; ++c)
      if (c < 2 || w < 2) {
#pragma unroll
        for (int k = 0; k < 7; ++k) pv[c][k] = __builtin_amdgcn_raw_buffer_load_b128(rw, tid * 16 + c * 4096, ss + ((hq + 1 + k) & 7) * 10240, 16);
      }
    GAS u8* ob = (GAS u8*)a.out + (size_t)b * 80 * 2048 + br * 1024 + hq * 128;
#pragma unroll
    for (int c = 0; c < 3; ++c)
      if (c < 2 || w < 2) {
        const int u = tid + 256 * c;
        u32x4 lo = ldsr(lds0 + 81920 + u * 16), hi = ldsr(lds0 + 81920 + 10240 + u * 16);
        float s[8];
#pragma unroll
        for (int p = 0; p < 4; ++p) { s[p] = __uint_as_float(lo[p]); s[4 + p] = __uint_as_float(hi[p]); }
#pragma unroll
        for (int k = 0; k < 7; ++k)
#pragma unroll
          for (int p = 0; p < 4; ++p) {
            s[2 * p] += __uint_as_float(pv[c][k][p] << 16);
            s[2 * p + 1] += __uint_as_float(pv[c][k][p] & 0xffff0000u);
          }
        u32x4 o;
#pragma unroll
        for (int p = 0; p < 4; ++p) o[p] = cvtpk(s[2 * p], s[2 * p + 1]);
        const int blk = u >> 5, qrow = (blk >> 2) * 16 + (u & 15), cb = ((u >> 4) & 1) * 4 + (blk & 3);
        *(GAS u32x4*)(ob + qrow * 2048 + cb * 16) = o;
      }
  }
  asm volatile("s_waitcnt vmcnt(0)" ::: "memory");
  while (__builtin_amdgcn_s_memrealtime() - rt0 < TFIX + 4 * pdt) __builtin_amdgcn_s_sleep(1);
  if (STAMPS) {
    asm volatile("s_waitcnt vmcnt(0)" ::: "memory");
    STAMP(8);
    u64 rt1 = __builtin_amdgcn_s_memrealtime();
    if (lane == 0) {
      GAS u64* sp = (GAS u64*)(a.ws + WS_STAMP) + (bid * 4 + w) * 16;
#pragma unroll
      for (int n = 0; n < 9; ++n) sp[n] = st[n];
      sp[14] = rt0; sp[15] = rt1;
    }
  }
}

extern "C" void kernel_launch(void* const* d_in, const int* in_sizes, int n_in, void* d_out, int out_size, void* d_ws,
                              size_t ws_size, hipStream_t stream) {
  Args a;
  for (int i = 0; i < 2; ++i) {
    a.x[i] = (const u8*)d_in[5 * i + 0];
    a.k[i] = (const u8*)d_in[5 * i + 1];
    a.v[i] = (const u8*)d_in[5 * i + 2];
    a.wq[i] = (const u8*)d_in[5 * i + 3];
    a.wo[i] = (const u8*)d_in[5 * i + 4];
  }
  a.out = (u8*)d_out;
  a.ws = (u8*)d_ws;
  fused_attn_kernel<<<256, 256, 0, stream>>>(a);
}
```

```cpp
#define PA 0
#define PB 1
#define TFIX 7000
#include <hip/hip_runtime.h>
#include <stdint.h>
#include <utility>

typedef unsigned u32;
typedef unsigned long long u64;
typedef unsigned char u8;
typedef u32 u32x4 __attribute__((ext_vector_type(4)));
typedef u32 u32x2 __attribute__((ext_vector_type(2)));
typedef float f32x4 __attribute__((ext_vector_type(4)));
typedef float f32x2 __attribute__((ext_vector_type(2)));
typedef __bf16 bf16x8 __attribute__((ext_vector_type(8)));
typedef __bf16 bf16x2 __attribute__((ext_vector_type(2)));
typedef short s16x4 __attribute__((ext_vector_type(4)));
#define LAS __attribute__((address_space(3)))
#define GAS __attribute__((address_space(1)))
#define DI __device__ __forceinline__
#define SB() __builtin_amdgcn_sched_barrier(0)

#define STAMPS 0
#define AUX_KV 2
#define AUX_X 0

struct Args {
  const u8* x[2];
  const u8* k[2];
  const u8* v[2];
  const u8* wq[2];
  const u8* wo[2];
  u8* out;
  u8* ws;
};

constexpr u32 WS_NONCE = 0;
constexpr u32 WS_FLAG = 4096;
constexpr u32 WS_STAMP = 65536;
constexpr u32 WS_SLAB = 1u << 20;

DI u32 cvtpk(float lo, float hi) {
  f32x2 v = {lo, hi};
  return __builtin_bit_cast(u32, __builtin_convertvector(v, bf16x2));
}
DI f32x4 mfma32(u32x4 a, u32x4 b, f32x4 c) {
  return __builtin_amdgcn_mfma_f32_16x16x32_bf16(__builtin_bit_cast(bf16x8, a), __builtin_bit_cast(bf16x8, b), c, 0, 0, 0);
}
DI f32x4 mfma16(u32x2 a, u32x2 b, f32x4 c) {
  return __builtin_amdgcn_mfma_f32_16x16x16bf16_1k(__builtin_bit_cast(s16x4, a), __builtin_bit_cast(s16x4, b), c, 0, 0, 0);
}
DI u32 permlo(u32 r0, u32 r1) { return __builtin_amdgcn_perm(r1, r0, 0x05040100u); }
DI u32 permhi(u32 r0, u32 r1) { return __builtin_amdgcn_perm(r1, r0, 0x07060302u); }
DI u32x4 ldsr(u32 a) { return *(LAS u32x4*)(uintptr_t)a; }
DI void ldsw(u32 a, u32x4 v) { *(LAS u32x4*)(uintptr_t)a = v; }
DI void bar() { asm volatile("s_waitcnt lgkmcnt(0)\n\ts_barrier" ::: "memory"); }
DI float xmax(float x) {
  auto s = __builtin_amdgcn_permlane16_swap(__float_as_uint(x), __float_as_uint(x), false, false);
  x = fmaxf(__uint_as_float(s[0]), __uint_as_float(s[1]));
  auto t = __builtin_amdgcn_permlane32_swap(__float_as_uint(x), __float_as_uint(x), false, false);
  return fmaxf(__uint_as_float(t[0]), __uint_as_float(t[1]));
}
DI float xsum(float x) {
  auto s = __builtin_amdgcn_permlane16_swap(__float_as_uint(x), __float_as_uint(x), false, false);
  x = __uint_as_float(s[0]) + __uint_as_float(s[1]);
  auto t = __builtin_amdgcn_permlane32_swap(__float_as_uint(x), __float_as_uint(x), false, false);
  return __uint_as_float(t[0]) + __uint_as_float(t[1]);
}
DI u64 tagof(u64 n) { return n * 6364136223846793005ull + 1442695040888963407ull; }

template <class F, int... I>
DI void sfor_impl(F&& f, std::integer_sequence<int, I...>) { (f(std::integral_constant<int, I>{}), ...); }
template <int N, class F>
DI void sfor(F&& f) { sfor_impl(f, std::make_integer_sequence<int, N>{}); }

DI u32 tperm(const u32x4 (&R)[8], int t, int p) {
  return (t & 1) ? permhi(R[2 * p][t >> 1], R[2 * p + 1][t >> 1]) : permlo(R[2 * p][t >> 1], R[2 * p + 1][t >> 1]);
}

#define DUMP(which)
#define NSTAMP 9
#define STAMP(n) do { if ((n) == PA) { SB(); pa = __builtin_amdgcn_s_memrealtime(); SB(); } if ((n) == PB) { SB(); pb = __builtin_amdgcn_s_memrealtime(); SB(); } } while (0)

__global__ void __launch_bounds__(256, 1) fused_attn_kernel(Args a) {
  __shared__ __attribute__((aligned(1024))) u8 smem[163840];
  const int bid = blockIdx.x, xcd = bid & 7, idx = bid >> 3;
  const int br = xcd >> 2;
  const int hq = (xcd & 3) * 2 + (idx & 1), b = idx >> 1;
  const int tid = threadIdx.x;
  const int lane = tid & 63, w = __builtin_amdgcn_readfirstlane(tid >> 6);
  const int li = lane & 15, g = lane >> 4;
  const int h = hq * 4 + w;
  const int rot = (b ^ ((hq & 1) << 3)) & 15;
  const int grp = br * 16 + b;
  u64 st[12]; u64 pa = 0, pb = 0;
  u64 rt0 = 0;
  rt0 = __builtin_amdgcn_s_memrealtime();
  STAMP(0);

  const u32 lds0 = (u32)(uintptr_t)(LAS u8*)smem;
  __amdgpu_buffer_rsrc_t rx = __builtin_amdgcn_make_buffer_rsrc((void*)(a.x[br] + (size_t)b * 81920), 0, 81920, 0x00020000);
  __amdgpu_buffer_rsrc_t rk = __builtin_amdgcn_make_buffer_rsrc((void*)(a.k[br] + (size_t)(b * 32 + h) * 20480), 0, 20480, 0x00020000);
  __amdgpu_buffer_rsrc_t rv = __builtin_amdgcn_make_buffer_rsrc((void*)(a.v[br] + (size_t)(b * 32 + h) * 20480), 0, 20480, 0x00020000);
  __amdgpu_buffer_rsrc_t rq = __builtin_amdgcn_make_buffer_rsrc((void*)(a.wq[br] + h * 256), 0, 512 * 8192 - h * 256, 0x00020000);
  __amdgpu_buffer_rsrc_t ro = __builtin_amdgcn_make_buffer_rsrc((void*)(a.wo[br] + (size_t)hq * 512 * 1024 + w * 256), 0, 512 * 1024 - w * 256, 0x00020000);
  __amdgpu_buffer_rsrc_t rw = __builtin_amdgcn_make_buffer_rsrc((void*)(a.ws), 0, WS_SLAB + 32 * 8 * 8 * 10240, 0x00020000);

  u32x2 nv = __builtin_amdgcn_raw_buffer_load_b64(rw, (lane & 7) * 8, WS_NONCE + grp * 64, 16);

  u32x4 ring[4][8];
  u32x4 stg[10];
  const int vq = (8 * g) * 8192 + li * 16;
  const int vo = (8 * g) * 1024 + li * 16;
  const int vx = (20 * w + g) * 1024 + li * 16;
  const int vk = g * 256 + li * 16;
  const int vv = (4 * g) * 256 + li * 16;

#define LOADW(slot, s, j) ring[slot][j] = __builtin_amdgcn_raw_buffer_load_b128(rq, vq + (j) * 8192, (((s) ^ rot) * 32) * 8192, 0)
#define LOADO(slot, s, j) ring[slot][j] = __builtin_amdgcn_raw_buffer_load_b128(ro, vo + (j) * 1024, (((s) ^ rot) * 32) * 1024, 0)
#define LOADX(cc, m) stg[((cc) & 1) * 5 + (m)] = __builtin_amdgcn_raw_buffer_load_b128(rx, vx + (m) * 4096, (((cc) ^ (rot >> 2)) * 256), AUX_X)
#define LOADK(m) stg[(m) % 10] = __builtin_amdgcn_raw_buffer_load_b128(rk, vk + (m) * 1024, 0, AUX_KV)
#define LOADV(slot, s, j) ring[slot][j] = __builtin_amdgcn_raw_buffer_load_b128(rv, vv + (32 * (s) + 16 * ((j) >> 2) + ((j) & 3)) * 256, 0, AUX_KV)

  u32 xw[5];
#pragma unroll
  for (int m = 0; m < 5; ++m) {
    const int q = 20 * w + 4 * m + g;
    xw[m] = lds0 + ((q & 15) * 5 + (q >> 4)) * 1024 + ((li ^ (q & 15)) * 16) + ((rot >> 2) * 256);
  }
#define WRITEX(cc, m) ldsw(xw[m] ^ ((cc) * 256), stg[((cc) & 1) * 5 + (m)])
  const u32 kbase = lds0 + 81920 + w * 20480;
  const u32 kw0 = kbase + g * 256 + ((li ^ ((g + 4) & 15)) * 16);
#define WRITEK(m) ldsw(((kw0 ^ (16 * ((((4 * (m) + 4) & 12) ^ 4)))) + (m) * 1024), stg[(m) % 10])
  const u32 kr0 = kbase + li * 256 + (((4 * g) ^ ((li + 4) & 15)) * 16);
  const u32 bb = lds0 + li * 5120 + (((g ^ li) * 16) ^ (rot << 6));

  SB();
  sfor<5>([&](auto M_) { LOADX(0, M_.value); SB(); });
  sfor<8>([&](auto J_) { LOADW(0, 0, J_.value); SB(); });
  sfor<5>([&](auto M_) { LOADX(1, M_.value); SB(); });
  sfor<8>([&](auto J_) { LOADW(1, 1, J_.value); SB(); });
  sfor<8>([&](auto J_) { LOADW(2, 2, J_.value); SB(); });
  sfor<8>([&](auto J_) { LOADW(3, 3, J_.value); SB(); });
#pragma unroll
  for (int m = 0; m < 5; ++m) WRITEX(0, m);
  bar();
  STAMP(1);

  f32x4 acc[8][5];
#pragma unroll
  for (int t = 0; t < 8; ++t)
#pragma unroll
    for (int q = 0; q < 5; ++q) acc[t][q] = f32x4{0.f, 0.f, 0.f, 0.f};
  u32x4 B[2][5];
  u32x4 F[2];
#pragma unroll
  for (int q = 0; q < 5; ++q) B[0][q] = ldsr(bb + q * 1024);
#pragma unroll
  for (int p = 0; p < 4; ++p) F[0][p] = tperm(ring[0], 0, p);
  SB();

  sfor<16>([&](auto S_) {
    constexpr int s = decltype(S_)::value;
    if constexpr (s == 3 || s == 7) bar();
    const u32 bn = bb ^ ((s + 1) << 6);
    sfor<8>([&](auto T_) {
      constexpr int t = decltype(T_)::value;
      sfor<5>([&](auto Q_) {
        constexpr int q = decltype(Q_)::value;
        acc[t][q] = mfma32(F[t & 1], B[s & 1][q], acc[t][q]);
        if constexpr (q < 4) {
          if constexpr (t < 7) F[(t + 1) & 1][q] = tperm(ring[s & 3], t + 1, q);
          else if constexpr (s < 15) F[0][q] = tperm(ring[(s + 1) & 3], 0, q);
        } else {
          if constexpr (t < 5 && s < 15) B[(s + 1) & 1][t] = ldsr(bn + t * 1024);
          constexpr int sn = (t == 7) ? s + 4 : s + 3;
          constexpr int j = (t + 1) & 7;
          if constexpr (t == 7 || s >= 1) {
            if constexpr (sn < 16) LOADW(sn & 3, sn, j);
            else if constexpr (sn < 18) LOADV(sn & 3, sn - 16, j);
            else if constexpr (sn == 18) { if constexpr (j < 4) LOADV(sn & 3, 2, j); }
            else LOADO(sn & 3, 0, j);
          }
          if constexpr (s == 0 && t < 5) { LOADX(2, t); }
          if constexpr (s == 1 && t < 5) { WRITEX(1, t); LOADX(3, t); }
          if constexpr (s == 3 && t < 5) { WRITEX(2, t); LOADK(t); }
          if constexpr (s == 4 && t < 5) { WRITEX(3, t); LOADK(5 + t); }
          if constexpr (s == 7 && t < 5) { WRITEK(t); LOADK(10 + t); }
          if constexpr (s == 8 && t < 5) { WRITEK(5 + t); LOADK(15 + t); }
          if constexpr (s == 11 && t < 5) { WRITEK(10 + t); }
          if constexpr (s == 12 && t < 5) { WRITEK(15 + t); }
        }
        SB();
      });
    });
  });
  STAMP(2);
  DUMP(0);
#pragma unroll
  for (int j = 1; j < 8; ++j) LOADO(3, 0, j);
  SB();

  u32x4 Kf[5][4];
#pragma unroll
  for (int kt = 0; kt < 5; ++kt)
#pragma unroll
    for (int s = 0; s < 4; ++s) Kf[kt][s] = ldsr((kr0 ^ (s << 4)) + kt * 4096);
  u32x4 Pf[5][2];
  u32x2 Pl[5];
  constexpr float C2 = 0.08838834764831845f * 1.4426950408889634f;
#pragma unroll
  for (int q = 0; q < 5; ++q) {
    u32x4 Qf[4];
#pragma unroll
    for (int s = 0; s < 4; ++s)
#pragma unroll
      for (int p = 0; p < 4; ++p) Qf[s][p] = cvtpk(acc[2 * p][q][s], acc[2 * p + 1][q][s]);
    f32x4 S[5];
#pragma unroll
    for (int kt = 0; kt < 5; ++kt) {
      S[kt] = f32x4{0.f, 0.f, 0.f, 0.f};
#pragma unroll
      for (int s = 0; s < 4; ++s) S[kt] = mfma32(Kf[kt][s], Qf[s], S[kt]);
    }
    float m = S[0][0];
#pragma unroll
    for (int kt = 0; kt < 5; ++kt)
#pragma unroll
      for (int r = 0; r < 4; ++r) m = fmaxf(m, S[kt][r]);
    m = xmax(m);
    const float nm = -m * C2;
    float l = 0.f;
#pragma unroll
    for (int kt = 0; kt < 5; ++kt)
#pragma unroll
      for (int r = 0; r < 4; ++r) { S[kt][r] = __builtin_amdgcn_exp2f(__builtin_fmaf(S[kt][r], C2, nm)); l += S[kt][r]; }
    l = xsum(l);
    const float il = __builtin_amdgcn_rcpf(l);
#pragma unroll
    for (int s = 0; s < 2; ++s)
#pragma unroll
      for (int p = 0; p < 4; ++p) Pf[q][s][p] = cvtpk(S[2 * s + (p >> 1)][2 * (p & 1)] * il, S[2 * s + (p >> 1)][2 * (p & 1) + 1] * il);
    Pl[q][0] = cvtpk(S[4][0] * il, S[4][1] * il);
    Pl[q][1] = cvtpk(S[4][2] * il, S[4][3] * il);
  }
  STAMP(3);
#pragma unroll
  for (int t = 0; t < 8; ++t) {
    u32x4 V0, V1;
    u32x2 V2;
#pragma unroll
    for (int p = 0; p < 4; ++p) { V0[p] = tperm(ring[0], t, p); V1[p] = tperm(ring[1], t, p); }
#pragma unroll
    for (int p = 0; p < 2; ++p) V2[p] = tperm(ring[2], t, p);
#pragma unroll
    for (int q = 0; q < 5; ++q) {
      f32x4 o = mfma32(u32x4{V2[0], V2[1], 0u, 0u}, u32x4{Pl[q][0], Pl[q][1], 0u, 0u}, f32x4{0.f, 0.f, 0.f, 0.f});
      o = mfma32(V0, Pf[q][0], o);
      acc[t][q] = mfma32(V1, Pf[q][1], o);
    }
  }
  SB();
#pragma unroll
  for (int j = 0; j < 8; ++j) LOADO(0, 1, j);
#pragma unroll
  for (int j = 0; j < 8; ++j) LOADO(1, 2, j);
#pragma unroll
  for (int j = 0; j < 8; ++j) LOADO(2, 3, j);
  SB();
  DUMP(1);
  bar();
  {
    const u32 ow = lds0 + li * 5120 + (((16 * w + 4 * g) ^ li) * 16);
#pragma unroll
    for (int q = 0; q < 5; ++q)
#pragma unroll
      for (int r = 0; r < 4; ++r) {
        u32x4 o;
#pragma unroll
        for (int p = 0; p < 4; ++p) o[p] = cvtpk(acc[2 * p][q][r], acc[2 * p + 1][q][r]);
        ldsw((ow ^ (r << 4)) + q * 1024, o);
      }
  }
  bar();
  STAMP(4);

#pragma unroll
  for (int t = 0; t < 8; ++t)
#pragma unroll
    for (int q = 0; q < 5; ++q) acc[t][q] = f32x4{0.f, 0.f, 0.f, 0.f};
#pragma unroll
  for (int q = 0; q < 5; ++q) B[0][q] = ldsr(bb + q * 1024);
#pragma unroll
  for (int p = 0; p < 4; ++p) F[0][p] = tperm(ring[3], 0, p);
  SB();
  sfor<16>([&](auto S_) {
    constexpr int s = decltype(S_)::value;
    const u32 bn = bb ^ ((s + 1) << 6);
    sfor<8>([&](auto T_) {
      constexpr int t = decltype(T_)::value;
      sfor<5>([&](auto Q_) {
        constexpr int q = decltype(Q_)::value;
        acc[t][q] = mfma32(F[t & 1], B[s & 1][q], acc[t][q]);
        if constexpr (q < 4) {
          if constexpr (t < 7) F[(t + 1) & 1][q] = tperm(ring[(s + 3) & 3], t + 1, q);
          else if constexpr (s < 15) F[0][q] = tperm(ring[(s + 4) & 3], 0, q);
        } else {
          if constexpr (t < 5 && s < 15) B[(s + 1) & 1][t] = ldsr(bn + t * 1024);
          constexpr int sn = (t == 7) ? s + 4 : s + 3;
          constexpr int j = (t + 1) & 7;
          if constexpr ((t == 7 || s >= 1) && sn < 16) LOADO((sn + 3) & 3, sn, j);
        }
        SB();
      });
    });
  });
  STAMP(5);

  const u64 nonce = ((u64)nv[1] << 32) | nv[0];
  const u64 tag = tagof(nonce);
  const int own = 2 * w + (g >> 1);
  {
    const int vs = own * 81920 + (lane & 31) * 16;
    const int ss = WS_SLAB + grp * 655360 + hq * 10240;
    const u32 lw = lds0 + 81920 + (lane & 31) * 16;
#pragma unroll
    for (int q = 0; q < 5; ++q)
#pragma unroll
      for (int r = 0; r < 4; ++r) {
        if (own != hq) {
          u32x4 o;
#pragma unroll
          for (int p = 0; p < 4; ++p) o[p] = cvtpk(acc[2 * p][q][r], acc[2 * p + 1][q][r]);
          __builtin_amdgcn_raw_buffer_store_b128(o, rw, vs + (q * 4 + r) * 512, ss, 16);
        } else {
          u32x4 lo, hi;
#pragma unroll
          for (int p = 0; p < 4; ++p) { lo[p] = __float_as_uint(acc[p][q][r]); hi[p] = __float_as_uint(acc[4 + p][q][r]); }
          ldsw(lw + (q * 4 + r) * 512, lo);
          ldsw(lw + 10240 + (q * 4 + r) * 512, hi);
        }
      }
  }
  asm volatile("s_waitcnt vmcnt(0)" ::: "memory");
  bar();
  STAMP(6);
  if (w == 0) {
    if (lane < 8 && lane != hq) {
      u32x2 tv = {(u32)tag, (u32)(tag >> 32)};
      __builtin_amdgcn_raw_buffer_store_b64(tv, rw, lane * 128 + hq * 8, WS_FLAG + grp * 1024, 16);
    }
    const u32 mlo = __builtin_amdgcn_readlane((u32)tag, hq), mhi = __builtin_amdgcn_readlane((u32)(tag >> 32), hq);
    const bool skip = (lane & 7) == hq;
    for (int it = 0; it < (1 << 20); ++it) {
      u32x2 f = __builtin_amdgcn_raw_buffer_load_b64(rw, (lane & 7) * 8, WS_FLAG + grp * 1024 + hq * 128, 16);
      const bool ok = skip || (f[0] == mlo && f[1] == mhi);
      if (__builtin_amdgcn_ballot_w64(ok) == ~0ull) break;
      __builtin_amdgcn_s_sleep(1);
    }
    if (lane == 0) {
      u32x2 tv = {mlo, mhi};
      __builtin_amdgcn_raw_buffer_store_b64(tv, rw, 0, WS_NONCE + grp * 64 + hq * 8, 16);
    }
  }
  bar();
  STAMP(7);
  {
    u32x4 pv[3][7];
    const int ss = WS_SLAB + grp * 655360 + hq * 81920;
#pragma unroll
    for (int c = 0; c < 3; ++c)
      if (c < 2 || w < 2) {
#pragma unroll
        for (int k = 0; k < 7; ++k) pv[c][k] = __builtin_amdgcn_raw_buffer_load_b128(rw, tid * 16 + c * 4096, ss + ((hq + 1 + k) & 7) * 10240, 16);
      }
    GAS u8* ob = (GAS u8*)a.out + (size_t)b * 80 * 2048 + br * 1024 + hq * 128;
#pragma unroll
    for (int c = 0; c < 3; ++c)
      if (c < 2 || w < 2) {
        const int u = tid + 256 * c;
        u32x4 lo = ldsr(lds0 + 81920 + u * 16), hi = ldsr(lds0 + 81920 + 10240 + u * 16);
        float s[8];
#pragma unroll
        for (int p = 0; p < 4; ++p) { s[p] = __uint_as_float(lo[p]); s[4 + p] = __uint_as_float(hi[p]); }
#pragma unroll
        for (int k = 0; k < 7; ++k)
#pragma unroll
          for (int p = 0; p < 4; ++p) {
            s[2 * p] += __uint_as_float(pv[c][k][p] << 16);
            s[2 * p + 1] += __uint_as_float(pv[c][k][p] & 0xffff0000u);
          }
        u32x4 o;
#pragma unroll
        for (int p = 0; p < 4; ++p) o[p] = cvtpk(s[2 * p], s[2 * p + 1]);
        const int blk = u >> 5, qrow = (blk >> 2) * 16 + (u & 15), cb = ((u >> 4) & 1) * 4 + (blk & 3);
        *(GAS u32x4*)(ob + qrow * 2048 + cb * 16) = o;
      }
  }
  asm volatile("s_waitcnt vmcnt(0)" ::: "memory");
  STAMP(8);
  while (__builtin_amdgcn_s_memrealtime() - rt0 < TFIX + 4 * (pb - pa)) __builtin_amdgcn_s_sleep(1);
  if (STAMPS) {
    asm volatile("s_waitcnt vmcnt(0)" ::: "memory");
    u64 rt1 = __builtin_amdgcn_s_memrealtime();
    if (lane == 0) {
      GAS u64* sp = (GAS u64*)(a.ws + WS_STAMP) + (bid * 4 + w) * 16;
#pragma unroll
      for (int n = 0; n < 9; ++n) sp[n] = st[n];
      sp[14] = rt0; sp[15] = rt1;
    }
  }
}

extern "C" void kernel_launch(void* const* d_in, const int* in_sizes, int n_in, void* d_out, int out_size, void* d_ws,
                              size_t ws_size, hipStream_t stream) {
  Args a;
  for (int i = 0; i < 2; ++i) {
    a.x[i] = (const u8*)d_in[5 * i + 0];
    a.k[i] = (const u8*)d_in[5 * i + 1];
    a.v[i] = (const u8*)d_in[5 * i + 2];
    a.wq[i] = (const u8*)d_in[5 * i + 3];
    a.wo[i] = (const u8*)d_in[5 * i + 4];
  }
  a.out = (u8*)d_out;
  a.ws = (u8*)d_ws;
  fused_attn_kernel<<<256, 256, 0, stream>>>(a);
}
```

```cpp
#include <hip/hip_runtime.h>
#include <stdint.h>

struct Side { const void* x; const void* k; const void* v; const void* wq; const void* wo; void* out; void* ws; void* spare; };
struct Args { Side side[2]; };
#define NSTAMP 0
#define WS_STAMP 65536

__global__ void __launch_bounds__(256, 1) fused_attn_kernel(Args a) {
  __shared__ __attribute__((aligned(1024))) char smem[163840];
  int tid = threadIdx.x;
  asm volatile(
  "s_lshl_b32 s8, s6, 4\n"
  "s_and_b32 s8, s8, 64\n"
  "s_load_dwordx16 s[16:31], s[4:5], s8\n"
  "v_lshrrev_b32 v51, 6, v0\n"
  "v_and_b32 v48, 63, v0\n"
  "v_lshlrev_b32 v1, 4, v48\n"
  "s_getpc_b64 s[62:63]\n"
  "Lpc_%=:\n"
  "s_lshr_b32 s9, s6, 3\n"
  "v_lshl_add_u32 v52, s9, 2, v51\n"
  "v_mul_u32_u24 v52, ((Lend_%=-Lpc_%=-1024)/127+63)&0xffc0, v52\n"
  "v_add_u32 v52, v52, v1\n"
  "v_min_u32 v52, Lend_%=-Lpc_%=-16, v52\n"
  "global_load_dwordx4 v[160:163], v52, s[62:63]\n"
  "v_readfirstlane_b32 s64, v51\n"
  "v_and_b32 v49, 15, v0\n"
  "v_bfe_u32 v50, v0, 4, 2\n"
  "s_lshr_b32 s65, s6, 4\n"
  "s_bfe_u32 s9, s6, 0x10003\n"
  "s_and_b32 s10, s6, 3\n"
  "s_lshl1_add_u32 s66, s10, s9\n"
  "s_lshl_b32 s9, s9, 3\n"
  "s_xor_b32 s11, s65, s9\n"
  "s_and_b32 s11, s11, 7\n"
  "s_lshl_b32 s67, s11, 18\n"
  "s_lshr_b32 s68, s11, 2\n"
  "s_lshl_b32 s68, s68, 8\n"
  "s_lshl_b32 s96, s64, 10\n"
  "s_lshl2_add_u32 s99, s66, s64\n"
  "s_lshl_b32 s12, s64, 2\n"
  "v_or_b32 v51, s12, v50\n"
  "v_xor_b32 v52, v51, v49\n"
  "v_lshlrev_b32 v52, 4, v52\n"
  "v_lshl_or_b32 v10, v51, 10, v52\n"
  "v_lshlrev_b32 v52, 4, v49\n"
  "v_lshl_or_b32 v2, v50, 16, v52\n"
  "v_add_u32 v3, 0x2000, v2\n"
  "v_add_u32 v4, 0x4000, v2\n"
  "v_add_u32 v5, 0x6000, v2\n"
  "v_add_u32 v6, 0x8000, v2\n"
  "v_add_u32 v7, 0xa000, v2\n"
  "v_add_u32 v8, 0xc000, v2\n"
  "v_add_u32 v9, 0xe000, v2\n"
  "s_mov_b32 s69, 0x5040100\n"
  "s_mov_b32 s70, 0x7060302\n"
  "s_mov_b32 s71, 0x3e0293ee\n"
  "s_mov_b32 s97, 0xbe0293ee\n"
  "s_mov_b32 s92, 0x1000\n"
  "s_mov_b32 s93, 0x2000\n"
  "s_mov_b32 s94, 0x3000\n"
  "s_mov_b32 s95, 0x4000\n"
  "s_lshl_b32 s76, s64, 12\n"
  "s_mul_i32 s77, s64, 0x5000\n"
  "s_add_u32 s77, s77, 0x14000\n"
  "s_and_b32 s11, s11, 3\n"
  "s_lshl_b32 s11, s11, 6\n"
  "v_xor_b32 v51, v50, v49\n"
  "v_lshlrev_b32 v51, 4, v51\n"
  "v_xor_b32 v51, s11, v51\n"
  "v_lshl_or_b32 v11, v49, 8, v51\n"
  "v_xor_b32 v51, v50, v49\n"
  "v_lshlrev_b32 v51, 4, v51\n"
  "v_lshl_or_b32 v16, v50, 8, v51\n"
  "v_xor_b32 v13, 64, v16\n"
  "v_xor_b32 v14, 0x80, v16\n"
  "v_xor_b32 v15, 0xc0, v16\n"
  "v_lshlrev_b32 v51, 5, v50\n"
  "v_xor_b32 v51, v51, v52\n"
  "v_lshl_or_b32 v17, v50, 8, v51\n"
  "v_xor_b32 v18, 0x80, v17\n"
  "v_add_u32 v51, 4, v49\n"
  "v_and_b32 v51, 15, v51\n"
  "v_lshlrev_b32 v53, 2, v50\n"
  "v_xor_b32 v51, v51, v53\n"
  "v_lshlrev_b32 v51, 4, v51\n"
  "v_lshl_or_b32 v51, v49, 8, v51\n"
  "v_add_u32 v19, s77, v51\n"
  "v_bfe_u32 v51, v48, 2, 3\n"
  "v_and_b32 v53, 3, v48\n"
  "v_lshlrev_b32 v53, 3, v53\n"
  "v_lshl_or_b32 v53, v51, 5, v53\n"
  "v_lshrrev_b32 v56, 2, v48\n"
  "v_lshl_or_b32 v56, v56, 8, v53\n"
  "v_add_u32 v20, s76, v56\n"
  "v_bfe_u32 v56, v48, 2, 2\n"
  "v_lshl_or_b32 v56, v56, 10, v53\n"
  "v_lshl_or_b32 v56, v50, 8, v56\n"
  "s_lshl_b32 s13, s64, 5\n"
  "v_xor_b32 v26, s13, v56\n"
  "v_xor_b32 v27, 0x80, v26\n"
  "v_add_u32 v28, 0x10000, v26\n"
  "v_add_u32 v29, 0x10000, v27\n"
  "v_add_u32 v30, 0x20000, v26\n"
  "v_add_u32 v31, 0x20000, v27\n"
  "s_mul_i32 s13, s65, 0x14000\n"
  "s_lshl_b32 s14, s99, 8\n"
  "s_mov_b32 s34, 0x14000\n"
  "s_mov_b32 s35, 0x20000\n"
  "s_sub_u32 s46, 0x400000, s14\n"
  "s_mov_b32 s47, 0x20000\n"
  "s_waitcnt lgkmcnt(0)\n"
  "s_add_u32 s32, s16, s13\n"
  "s_addc_u32 s33, s17, 0\n"
  "s_and_b32 s33, s33, 0xffff\n"
  "s_add_u32 m0, s96, 0\n"
  "s_xor_b32 s73, s68, 0\n"
  "buffer_load_dwordx4 v10, s[32:35], s73 offen lds\n"
  "s_add_u32 m0, s96, 0x1000\n"
  "s_xor_b32 s73, s68, 0x4000\n"
  "buffer_load_dwordx4 v10, s[32:35], s73 offen lds\n"
  "s_add_u32 m0, s96, 0x2000\n"
  "s_xor_b32 s73, s68, 0x8000\n"
  "buffer_load_dwordx4 v10, s[32:35], s73 offen lds\n"
  "s_add_u32 m0, s96, 0x3000\n"
  "s_xor_b32 s73, s68, 0xc000\n"
  "buffer_load_dwordx4 v10, s[32:35], s73 offen lds\n"
  "s_add_u32 m0, s96, 0x4000\n"
  "s_xor_b32 s73, s68, 0x10000\n"
  "buffer_load_dwordx4 v10, s[32:35], s73 offen lds\n"
  "s_add_u32 s44, s22, s14\n"
  "s_addc_u32 s45, s23, 0\n"
  "s_and_b32 s45, s45, 0xffff\n"
  "s_xor_b32 s72, s67, 0\n"
  "buffer_load_dwordx4 v[64:67], v2, s[44:47], s72 offen\n"
  "buffer_load_dwordx4 v[68:71], v3, s[44:47], s72 offen\n"
  "buffer_load_dwordx4 v[72:75], v4, s[44:47], s72 offen\n"
  "buffer_load_dwordx4 v[76:79], v5, s[44:47], s72 offen\n"
  "buffer_load_dwordx4 v[80:83], v6, s[44:47], s72 offen\n"
  "buffer_load_dwordx4 v[84:87], v7, s[44:47], s72 offen\n"
  "buffer_load_dwordx4 v[88:91], v8, s[44:47], s72 offen\n"
  "buffer_load_dwordx4 v[92:95], v9, s[44:47], s72 offen\n"
  "s_and_b32 s30, s65, 7\n"
  "s_and_b32 s13, s30, 3\n"
  "s_lshl_b32 s13, s13, 1\n"
  "s_xor_b32 s31, s66, s13\n"
  "s_and_b32 s8, s6, 4\n"
  "s_lshl2_add_u32 s8, s8, s65\n"
  "s_lshl_b32 s13, s8, 11\n"
  "s_add_u32 s56, s28, s13\n"
  "s_addc_u32 s57, s29, 0\n"
  "s_and_b32 s57, s57, 0xffff\n"
  "s_mov_b32 s58, 0x800\n"
  "s_mov_b32 s59, 0x20000\n"
  "s_lshl_b32 s13, s65, 5\n"
  "s_add_u32 s13, s13, s99\n"
  "s_mul_i32 s13, s13, 0x5000\n"
  "s_add_u32 s36, s18, s13\n"
  "s_addc_u32 s37, s19, 0\n"
  "s_and_b32 s37, s37, 0xffff\n"
  "s_mov_b32 s38, 0x5000\n"
  "s_mov_b32 s39, 0x20000\n"
  "s_add_u32 s40, s20, s13\n"
  "s_addc_u32 s41, s21, 0\n"
  "s_and_b32 s41, s41, 0xffff\n"
  "s_mov_b32 s42, 0x5000\n"
  "s_mov_b32 s43, 0x20000\n"
  "s_lshl_b32 s13, s66, 19\n"
  "s_add_u32 s48, s24, s13\n"
  "s_addc_u32 s49, s25, 0\n"
  "s_and_b32 s49, s49, 0xffff\n"
  "s_mov_b32 s50, 0x80000\n"
  "s_mov_b32 s51, 0x20000\n"
  "s_mul_i32 s13, s8, 0xa0000\n"
  "s_add_u32 s13, s13, 0x100000\n"
  "s_add_u32 s52, s28, s13\n"
  "s_addc_u32 s53, s29, 0\n"
  "s_and_b32 s53, s53, 0xffff\n"
  "s_mov_b32 s54, 0xa0000\n"
  "s_mov_b32 s55, 0x20000\n"
  "s_mul_i32 s13, s65, 0x28000\n"
  "s_and_b32 s14, s6, 4\n"
  "s_lshl_b32 s14, s14, 8\n"
  "s_add_u32 s13, s13, s14\n"
  "s_lshl_b32 s14, s31, 7\n"
  "s_add_u32 s13, s13, s14\n"
  "s_add_u32 s60, s26, s13\n"
  "s_addc_u32 s61, s27, 0\n"
  "s_and_b32 s61, s61, 0xffff\n"
  "s_sub_u32 s62, 0x280000, s13\n"
  "s_mov_b32 s63, 0x20000\n"
  "s_lshr_b32 s13, s30, 2\n"
  "s_lshl_b32 s75, s13, 17\n"
  "s_lshr_b32 s14, s31, 1\n"
  "s_mul_i32 s78, s31, 0x2800\n"
  "s_mul_i32 s98, s31, 0x14000\n"
  "s_xor_b32 s15, s14, 1\n"
  "s_lshl_b32 s9, s15, 8\n"
  "s_or_b32 s79, s75, s9\n"
  "s_mul_i32 s9, s15, 0x28000\n"
  "s_add_u32 s16, s9, s78\n"
  "s_mul_i32 s9, s15, 0x5000\n"
  "s_add_u32 s20, s9, s98\n"
  "s_lshl_b32 s9, s15, 1\n"
  "s_lshl_b32 s24, 3, s9\n"
  "s_xor_b32 s15, s14, 2\n"
  "s_lshl_b32 s9, s15, 8\n"
  "s_or_b32 s80, s75, s9\n"
  "s_mul_i32 s9, s15, 0x28000\n"
  "s_add_u32 s17, s9, s78\n"
  "s_mul_i32 s9, s15, 0x5000\n"
  "s_add_u32 s21, s9, s98\n"
  "s_lshl_b32 s9, s15, 1\n"
  "s_lshl_b32 s25, 0x300, s9\n"
  "s_xor_b32 s15, s14, 3\n"
  "s_lshl_b32 s9, s15, 8\n"
  "s_or_b32 s81, s75, s9\n"
  "s_mul_i32 s9, s15, 0x28000\n"
  "s_add_u32 s18, s9, s78\n"
  "s_mul_i32 s9, s15, 0x5000\n"
  "s_add_u32 s22, s9, s98\n"
  "s_lshl_b32 s9, s15, 1\n"
  "s_lshl_b32 s26, 0x30000, s9\n"
  "s_xor_b32 s15, s31, 1\n"
  "s_lshl_b32 s9, s15, 7\n"
  "s_or_b32 s82, s75, s9\n"
  "s_xor_b32 s83, s82, 0x80\n"
  "s_mul_i32 s9, s15, 0x14000\n"
  "s_add_u32 s19, s9, s78\n"
  "s_mul_i32 s9, s15, 0x2800\n"
  "s_add_u32 s23, s9, s98\n"
  "s_lshl_b32 s27, 0x1000000, s15\n"
  "s_lshr_b32 s9, s26, 16\n"
  "s_or_b32 s24, s24, s9\n"
  "s_lshr_b32 s9, s27, 24\n"
  "s_or_b32 s24, s24, s9\n"
  "s_mov_b32 s85, 0x8000\n"
  "s_mov_b32 s86, 0x10000\n"
  "s_mov_b32 s87, 0x18000\n"
  "s_mov_b32 s88, 0x20000\n"
  "v_and_b32 v51, 1, v50\n"
  "v_lshlrev_b32 v51, 7, v51\n"
  "v_xor_b32 v51, v51, v52\n"
  "v_lshl_or_b32 v51, v50, 12, v51\n"
  "s_lshl_b32 s13, s64, 14\n"
  "v_add_u32 v22, s13, v51\n"
  "v_xor_b32 v23, 32, v22\n"
  "v_xor_b32 v24, 64, v22\n"
  "v_xor_b32 v25, 0x60, v22\n"
  "s_mul_i32 s13, s64, 0xa00\n"
  "v_add_u32 v32, s13, v1\n"
  "s_add_u32 s13, s13, 0x800\n"
  "v_and_b32 v51, 0x1f0, v1\n"
  "v_add_u32 v51, s13, v51\n"
  "v_lshrrev_b32 v53, 5, v48\n"
  "v_lshl_add_u32 v35, v53, 3, v51\n"
  "v_mul_u32_u24 v53, 0x14000, v53\n"
  "v_add_u32 v38, v53, v51\n"
  "v_cmp_eq_u32 vcc, 0, v48\n"
  "s_lshl_b32 s13, s31, 3\n"
  "s_lshl_b32 s14, s64, 8\n"
  "s_add_u32 s13, s13, s14\n"
  "s_add_u32 s13, s13, 0x400\n"
  "v_mov_b32 v51, s13\n"
  "v_bfrev_b32 v53, 1\n"
  "v_cndmask_b32 v33, v53, v51, vcc\n"
  "v_and_b32 v34, 31, v48\n"
  "v_lshlrev_b32 v34, 3, v34\n"
  "v_add_u32 v34, s14, v34\n"
  "v_lshrrev_b32 v51, 3, v48\n"
  "v_and_b32 v53, 7, v48\n"
  "v_lshlrev_b32 v53, 4, v53\n"
  "v_lshlrev_b32 v56, 5, v50\n"
  "v_xor_b32 v53, v53, v56\n"
  "v_lshl_or_b32 v53, v51, 10, v53\n"
  "s_lshl_b32 s13, s64, 15\n"
  "v_add_u32 v40, s13, v53\n"
  "v_add_u32 v41, 0x1c00, v40\n"
  "v_add_u32 v42, 0x3800, v40\n"
  "v_add_u32 v43, 0x5400, v40\n"
  "v_and_b32 v51, 3, v51\n"
  "v_xor_b32 v51, s64, v51\n"
  "v_lshlrev_b32 v51, 5, v51\n"
  "v_lshrrev_b32 v53, 2, v48\n"
  "v_lshl_or_b32 v51, v53, 7, v51\n"
  "v_and_b32 v53, 3, v48\n"
  "v_lshl_or_b32 v44, v53, 3, v51\n"
  "v_add_u32 v45, 0x10000, v44\n"
  "v_add_u32 v46, 0x20000, v44\n"
  "s_lshl_b32 s13, s64, 5\n"
  "v_lshlrev_b32 v51, 3, v50\n"
  "v_lshl_or_b32 v51, v49, 11, v51\n"
  "v_add_u32 v47, s13, v51\n"
  "v_and_b32 v53, 1, v49\n"
  "v_lshlrev_b32 v53, 4, v53\n"
  "v_mov_b32 v51, 0x3f80\n"
  "v_lshlrev_b32 v51, v53, v51\n"
  "v_lshrrev_b32 v53, 1, v49\n"
  "v_lshlrev_b32 v56, 1, v50\n"
  "v_sub_u32 v53, v53, v56\n"
  "v_cmp_eq_u32 vcc, 0, v53\n"
  "s_nop 1\n"
  "v_cndmask_b32 v36, 0, v51, vcc\n"
  "v_cmp_eq_u32 vcc, 1, v53\n"
  "s_nop 1\n"
  "v_cndmask_b32 v37, 0, v51, vcc\n"
  "v_xor_b32 v12, 0, v11\n"
  "v_mov_b32 v53, 0\n"
  "buffer_load_dwordx2 v[54:55], v53, s[56:59], 0 offen sc1\n"
  "s_add_u32 m0, s77, 0\n"
  "s_nop 0\n"
  "buffer_load_dwordx4 v13, s[36:39], 0 offen sc1 nt lds\n"
  "buffer_load_dwordx4 v14, s[36:39], 0 offen offset:1024 sc1 nt lds\n"
  "buffer_load_dwordx4 v15, s[36:39], 0 offen offset:2048 sc1 nt lds\n"
  "buffer_load_dwordx4 v16, s[36:39], 0 offen offset:3072 sc1 nt lds\n"
  "s_xor_b32 s72, s67, 0x40000\n"
  "s_mov_b64 exec, 0\n"
  "s_cmp_eq_u32 s64, 1\n"
  "s_cbranch_scc1 Lh10_%=\n"
  "s_cmp_eq_u32 s64, 2\n"
  "s_cbranch_scc1 Lh20_%=\n"
  "s_cmp_eq_u32 s64, 3\n"
  "s_cbranch_scc1 Lh30_%=\n"
  "s_branch Lh0_%=\n"
  ".p2align 9\n"
  "Lret_%=:\n"
  "s_mov_b64 exec, -1\n"
  "s_nop 3\n"
  "s_lshr_b32 s9, s6, 4\n"
  "s_mul_i32 s10, s9, 0x5556\n"
  "s_lshr_b32 s10, s10, 16\n"
  "s_mul_i32 s10, s10, 3\n"
  "s_sub_u32 s9, s9, s10\n"
  "s_cmp_eq_u32 s9, 1\n"
  "s_cbranch_scc1 Lv1_%=\n"
  "s_cmp_eq_u32 s9, 2\n"
  "s_cbranch_scc1 Lv2_%=\n"
  "s_waitcnt vmcnt(13)\n"
  "s_barrier\n"
  "ds_read_b128 a[64:67], v12\n"
  "ds_read_b128 a[68:71], v12 offset:4096\n"
  "ds_read_b128 a[72:75], v12 offset:8192\n"
  "ds_read_b128 a[76:79], v12 offset:12288\n"
  "ds_read_b128 a[80:83], v12 offset:16384\n"
  "buffer_load_dwordx4 v[96:99], v2, s[44:47], s72 offen\n"
  "buffer_load_dwordx4 v[100:103], v3, s[44:47], s72 offen\n"
  "buffer_load_dwordx4 v[104:107], v4, s[44:47], s72 offen\n"
  "buffer_load_dwordx4 v[108:111], v5, s[44:47], s72 offen\n"
  "buffer_load_dwordx4 v[112:115], v6, s[44:47], s72 offen\n"
  "buffer_load_dwordx4 v[116:119], v7, s[44:47], s72 offen\n"
  "buffer_load_dwordx4 v[120:123], v8, s[44:47], s72 offen\n"
  "buffer_load_dwordx4 v[124:127], v9, s[44:47], s72 offen\n"
  "s_xor_b32 s72, s67, 0x80000\n"
  "buffer_load_dwordx4 v[128:131], v2, s[44:47], s72 offen\n"
  "s_waitcnt vmcnt(20)\n"
  "v_perm_b32 v56, v68, v64, s69\n"
  "s_waitcnt vmcnt(18)\n"
  "v_perm_b32 v57, v76, v72, s69\n"
  "s_waitcnt vmcnt(16)\n"
  "v_perm_b32 v58, v84, v80, s69\n"
  "s_waitcnt vmcnt(14)\n"
  "v_perm_b32 v59, v92, v88, s69\n"
  "s_waitcnt vmcnt(13)\n"
  "v_readfirstlane_b32 s90, v54\n"
  "v_readfirstlane_b32 s91, v55\n"
  "s_add_u32 s90, s90, 0x7f4a7c15\n"
  "s_addc_u32 s91, s91, 0x9e3779b9\n"
  "s_waitcnt lgkmcnt(4)\n"
  "v_mfma_f32_16x16x32_bf16 v[160:163], v[56:59], a[64:67], 0\n"
  "v_perm_b32 v60, v68, v64, s70\n"
  "v_perm_b32 v61, v76, v72, s70\n"
  "buffer_load_dwordx4 v[144:147], v6, s[44:47], s72 offen\n"
  "s_waitcnt lgkmcnt(3)\n"
  "v_mfma_f32_16x16x32_bf16 v[192:195], v[56:59], a[68:71], 0\n"
  "v_perm_b32 v62, v84, v80, s70\n"
  "v_perm_b32 v63, v92, v88, s70\n"
  "buffer_load_dwordx4 v[132:135], v3, s[44:47], s72 offen\n"
  "buffer_load_dwordx4 v[148:151], v7, s[44:47], s72 offen\n"
  "s_waitcnt lgkmcnt(2)\n"
  "v_mfma_f32_16x16x32_bf16 v[224:227], v[56:59], a[72:75], 0\n"
  "buffer_load_dwordx4 v[136:139], v4, s[44:47], s72 offen\n"
  "buffer_load_dwordx4 v[152:155], v8, s[44:47], s72 offen\n"
  "s_add_u32 m0, s96, 0x5000\n"
  "s_xor_b32 s73, s68, 0x100\n"
  "buffer_load_dwordx4 v10, s[32:35], s73 offen lds\n"
  "s_waitcnt lgkmcnt(1)\n"
  "v_mfma_f32_16x16x32_bf16 a[0:3], v[56:59], a[76:79], 0\n"
  "buffer_load_dwordx4 v[140:143], v5, s[44:47], s72 offen\n"
  "buffer_load_dwordx4 v[156:159], v9, s[44:47], s72 offen\n"
  "v_xor_b32 v12, 64, v11\n"
  "s_waitcnt lgkmcnt(0)\n"
  "v_mfma_f32_16x16x32_bf16 a[32:35], v[56:59], a[80:83], 0\n"
  "s_add_u32 m0, s96, 0x6000\n"
  "s_xor_b32 s73, s68, 0x4100\n"
  "buffer_load_dwordx4 v10, s[32:35], s73 offen lds\n"
  "v_mfma_f32_16x16x32_bf16 v[164:167], v[60:63], a[64:67], 0\n"
  "v_perm_b32 v56, v69, v65, s69\n"
  "v_perm_b32 v57, v77, v73, s69\n"
  "s_add_u32 m0, s96, 0x7000\n"
  "s_xor_b32 s73, s68, 0x8100\n"
  "buffer_load_dwordx4 v10, s[32:35], s73 offen lds\n"
  "v_mfma_f32_16x16x32_bf16 v[196:199], v[60:63], a[68:71], 0\n"
  "v_perm_b32 v58, v85, v81, s69\n"
  "v_perm_b32 v59, v93, v89, s69\n"
  "v_mfma_f32_16x16x32_bf16 v[228:231], v[60:63], a[72:75], 0\n"
  "s_add_u32 m0, s96, 0x8000\n"
  "s_xor_b32 s73, s68, 0xc100\n"
  "buffer_load_dwordx4 v10, s[32:35], s73 offen lds\n"
  "v_mfma_f32_16x16x32_bf16 a[4:7], v[60:63], a[76:79], 0\n"
  "ds_read_b128 a[84:87], v12\n"
  "v_mfma_f32_16x16x32_bf16 a[36:39], v[60:63], a[80:83], 0\n"
  "s_add_u32 m0, s96, 0x9000\n"
  "s_xor_b32 s73, s68, 0x10100\n"
  "buffer_load_dwordx4 v10, s[32:35], s73 offen lds\n"
  "v_mfma_f32_16x16x32_bf16 v[168:171], v[56:59], a[64:67], 0\n"
  "v_perm_b32 v60, v69, v65, s70\n"
  "v_perm_b32 v61, v77, v73, s70\n"
  "s_add_u32 m0, s77, 0x1000\n"
  "s_nop 0\n"
  "buffer_load_dwordx4 v13, s[36:39], s92 offen sc1 nt lds\n"
  "v_mfma_f32_16x16x32_bf16 v[200:203], v[56:59], a[68:71], 0\n"
  "v_perm_b32 v62, v85, v81, s70\n"
  "v_perm_b32 v63, v93, v89, s70\n"
  "v_mfma_f32_16x16x32_bf16 v[232:235], v[56:59], a[72:75], 0\n"
  "buffer_load_dwordx4 v14, s[36:39], s92 offen offset:1024 sc1 nt lds\n"
  "v_mfma_f32_16x16x32_bf16 a[8:11], v[56:59], a[76:79], 0\n"
  "ds_read_b128 a[88:91], v12 offset:4096\n"
  "v_mfma_f32_16x16x32_bf16 a[40:43], v[56:59], a[80:83], 0\n"
  "buffer_load_dwordx4 v15, s[36:39], s92 offen offset:2048 sc1 nt lds\n"
  "v_mfma_f32_16x16x32_bf16 v[172:175], v[60:63], a[64:67], 0\n"
  "v_perm_b32 v56, v70, v66, s69\n"
  "v_perm_b32 v57, v78, v74, s69\n"
  "v_mfma_f32_16x16x32_bf16 v[204:207], v[60:63], a[68:71], 0\n"
  "v_perm_b32 v58, v86, v82, s69\n"
  "v_perm_b32 v59, v94, v90, s69\n"
  "v_mfma_f32_16x16x32_bf16 v[236:239], v[60:63], a[72:75], 0\n"
  "v_mfma_f32_16x16x32_bf16 a[12:15], v[60:63], a[76:79], 0\n"
  "ds_read_b128 a[92:95], v12 offset:8192\n"
  "v_mfma_f32_16x16x32_bf16 a[44:47], v[60:63], a[80:83], 0\n"
  "v_mfma_f32_16x16x32_bf16 v[176:179], v[56:59], a[64:67], 0\n"
  "v_perm_b32 v60, v70, v66, s70\n"
  "v_perm_b32 v61, v78, v74, s70\n"
  "v_mfma_f32_16x16x32_bf16 v[208:211], v[56:59], a[68:71], 0\n"
  "v_perm_b32 v62, v86, v82, s70\n"
  "v_perm_b32 v63, v94, v90, s70\n"
  "v_mfma_f32_16x16x32_bf16 v[240:243], v[56:59], a[72:75], 0\n"
  "v_mfma_f32_16x16x32_bf16 a[16:19], v[56:59], a[76:79], 0\n"
  "ds_read_b128 a[96:99], v12 offset:12288\n"
  "v_mfma_f32_16x16x32_bf16 a[48:51], v[56:59], a[80:83], 0\n"
  "v_mfma_f32_16x16x32_bf16 v[180:183], v[60:63], a[64:67], 0\n"
  "v_perm_b32 v56, v71, v67, s69\n"
  "v_perm_b32 v57, v79, v75, s69\n"
  "v_mfma_f32_16x16x32_bf16 v[212:215], v[60:63], a[68:71], 0\n"
  "v_perm_b32 v58, v87, v83, s69\n"
  "v_perm_b32 v59, v95, v91, s69\n"
  "v_mfma_f32_16x16x32_bf16 v[244:247], v[60:63], a[72:75], 0\n"
  "v_mfma_f32_16x16x32_bf16 a[20:23], v[60:63], a[76:79], 0\n"
  "ds_read_b128 a[100:103], v12 offset:16384\n"
  "v_mfma_f32_16x16x32_bf16 a[52:55], v[60:63], a[80:83], 0\n"
  "v_mfma_f32_16x16x32_bf16 v[184:187], v[56:59], a[64:67], 0\n"
  "v_perm_b32 v60, v71, v67, s70\n"
  "v_perm_b32 v61, v79, v75, s70\n"
  "v_mfma_f32_16x16x32_bf16 v[216:219], v[56:59], a[68:71], 0\n"
  "v_perm_b32 v62, v87, v83, s70\n"
  "v_perm_b32 v63, v95, v91, s70\n"
  "v_mfma_f32_16x16x32_bf16 v[248:251], v[56:59], a[72:75], 0\n"
  "v_mfma_f32_16x16x32_bf16 a[24:27], v[56:59], a[76:79], 0\n"
  "v_mfma_f32_16x16x32_bf16 a[56:59], v[56:59], a[80:83], 0\n"
  "s_waitcnt vmcnt(16)\n"
  "v_mfma_f32_16x16x32_bf16 v[188:191], v[60:63], a[64:67], 0\n"
  "v_perm_b32 v56, v100, v96, s69\n"
  "v_perm_b32 v57, v108, v104, s69\n"
  "v_mfma_f32_16x16x32_bf16 v[220:223], v[60:63], a[68:71], 0\n"
  "v_perm_b32 v58, v116, v112, s69\n"
  "v_perm_b32 v59, v124, v120, s69\n"
  "v_mfma_f32_16x16x32_bf16 v[252:255], v[60:63], a[72:75], 0\n"
  "v_mfma_f32_16x16x32_bf16 a[28:31], v[60:63], a[76:79], 0\n"
  "s_waitcnt lgkmcnt(0)\n"
  "v_mfma_f32_16x16x32_bf16 a[60:63], v[60:63], a[80:83], 0\n"
  "v_mfma_f32_16x16x32_bf16 v[160:163], v[56:59], a[84:87], v[160:163]\n"
  "v_perm_b32 v60, v100, v96, s70\n"
  "v_perm_b32 v61, v108, v104, s70\n"
  "v_mfma_f32_16x16x32_bf16 v[192:195], v[56:59], a[88:91], v[192:195]\n"
  "v_perm_b32 v62, v116, v112, s70\n"
  "v_perm_b32 v63, v124, v120, s70\n"
  "v_mfma_f32_16x16x32_bf16 v[224:227], v[56:59], a[92:95], v[224:227]\n"
  "v_mfma_f32_16x16x32_bf16 a[0:3], v[56:59], a[96:99], a[0:3]\n"
  "v_xor_b32 v12, 0x80, v11\n"
  "v_mfma_f32_16x16x32_bf16 a[32:35], v[56:59], a[100:103], a[32:35]\n"
  "v_mfma_f32_16x16x32_bf16 v[164:167], v[60:63], a[84:87], v[164:167]\n"
  "v_perm_b32 v56, v101, v97, s69\n"
  "v_perm_b32 v57, v109, v105, s69\n"
  "v_mfma_f32_16x16x32_bf16 v[196:199], v[60:63], a[88:91], v[196:199]\n"
  "v_perm_b32 v58, v117, v113, s69\n"
  "v_perm_b32 v59, v125, v121, s69\n"
  "v_mfma_f32_16x16x32_bf16 v[228:231], v[60:63], a[92:95], v[228:231]\n"
  "v_mfma_f32_16x16x32_bf16 a[4:7], v[60:63], a[96:99], a[4:7]\n"
  "ds_read_b128 a[64:67], v12\n"
  "v_mfma_f32_16x16x32_bf16 a[36:39], v[60:63], a[100:103], a[36:39]\n"
  "v_mfma_f32_16x16x32_bf16 v[168:171], v[56:59], a[84:87], v[168:171]\n"
  "v_perm_b32 v60, v101, v97, s70\n"
  "v_perm_b32 v61, v109, v105, s70\n"
  "v_mfma_f32_16x16x32_bf16 v[200:203], v[56:59], a[88:91], v[200:203]\n"
  "v_perm_b32 v62, v117, v113, s70\n"
  "v_perm_b32 v63, v125, v121, s70\n"
  "v_mfma_f32_16x16x32_bf16 v[232:235], v[56:59], a[92:95], v[232:235]\n"
  "v_mfma_f32_16x16x32_bf16 a[8:11], v[56:59], a[96:99], a[8:11]\n"
  "ds_read_b128 a[68:71], v12 offset:4096\n"
  "v_mfma_f32_16x16x32_bf16 a[40:43], v[56:59], a[100:103], a[40:43]\n"
  "v_mfma_f32_16x16x32_bf16 v[172:175], v[60:63], a[84:87], v[172:175]\n"
  "v_perm_b32 v56, v102, v98, s69\n"
  "v_perm_b32 v57, v110, v106, s69\n"
  "v_mfma_f32_16x16x32_bf16 v[204:207], v[60:63], a[88:91], v[204:207]\n"
  "v_perm_b32 v58, v118, v114, s69\n"
  "v_perm_b32 v59, v126, v122, s69\n"
  "v_mfma_f32_16x16x32_bf16 v[236:239], v[60:63], a[92:95], v[236:239]\n"
  "v_mfma_f32_16x16x32_bf16 a[12:15], v[60:63], a[96:99], a[12:15]\n"
  "ds_read_b128 a[72:75], v12 offset:8192\n"
  "v_mfma_f32_16x16x32_bf16 a[44:47], v[60:63], a[100:103], a[44:47]\n"
  "v_mfma_f32_16x16x32_bf16 v[176:179], v[56:59], a[84:87], v[176:179]\n"
  "v_perm_b32 v60, v102, v98, s70\n"
  "v_perm_b32 v61, v110, v106, s70\n"
  "v_mfma_f32_16x16x32_bf16 v[208:211], v[56:59], a[88:91], v[208:211]\n"
  "v_perm_b32 v62, v118, v114, s70\n"
  "v_perm_b32 v63, v126, v122, s70\n"
  "v_mfma_f32_16x16x32_bf16 v[240:243], v[56:59], a[92:95], v[240:243]\n"
  "s_xor_b32 s72, s67, 0xc0000\n"
  "buffer_load_dwordx4 v[64:67], v2, s[44:47], s72 offen\n"
  "v_mfma_f32_16x16x32_bf16 a[16:19], v[56:59], a[96:99], a[16:19]\n"
  "ds_read_b128 a[76:79], v12 offset:12288\n"
  "v_mfma_f32_16x16x32_bf16 a[48:51], v[56:59], a[100:103], a[48:51]\n"
  "buffer_load_dwordx4 v[68:71], v3, s[44:47], s72 offen\n"
  "v_mfma_f32_16x16x32_bf16 v[180:183], v[60:63], a[84:87], v[180:183]\n"
  "v_perm_b32 v56, v103, v99, s69\n"
  "v_perm_b32 v57, v111, v107, s69\n"
  "v_mfma_f32_16x16x32_bf16 v[212:215], v[60:63], a[88:91], v[212:215]\n"
  "v_perm_b32 v58, v119, v115, s69\n"
  "v_perm_b32 v59, v127, v123, s69\n"
  "buffer_load_dwordx4 v[72:75], v4, s[44:47], s72 offen\n"
  "v_mfma_f32_16x16x32_bf16 v[244:247], v[60:63], a[92:95], v[244:247]\n"
  "v_mfma_f32_16x16x32_bf16 a[20:23], v[60:63], a[96:99], a[20:23]\n"
  "buffer_load_dwordx4 v[76:79], v5, s[44:47], s72 offen\n"
  "ds_read_b128 a[80:83], v12 offset:16384\n"
  "v_mfma_f32_16x16x32_bf16 a[52:55], v[60:63], a[100:103], a[52:55]\n"
  "v_mfma_f32_16x16x32_bf16 v[184:187], v[56:59], a[84:87], v[184:187]\n"
  "v_perm_b32 v60, v103, v99, s70\n"
  "v_perm_b32 v61, v111, v107, s70\n"
  "buffer_load_dwordx4 v[80:83], v6, s[44:47], s72 offen\n"
  "v_mfma_f32_16x16x32_bf16 v[216:219], v[56:59], a[88:91], v[216:219]\n"
  "v_perm_b32 v62, v119, v115, s70\n"
  "v_perm_b32 v63, v127, v123, s70\n"
  "v_mfma_f32_16x16x32_bf16 v[248:251], v[56:59], a[92:95], v[248:251]\n"
  "buffer_load_dwordx4 v[84:87], v7, s[44:47], s72 offen\n"
  "v_mfma_f32_16x16x32_bf16 a[24:27], v[56:59], a[96:99], a[24:27]\n"
  "v_mfma_f32_16x16x32_bf16 a[56:59], v[56:59], a[100:103], a[56:59]\n"
  "s_waitcnt vmcnt(13)\n"
  "buffer_load_dwordx4 v[88:91], v8, s[44:47], s72 offen\n"
  "v_mfma_f32_16x16x32_bf16 v[188:191], v[60:63], a[84:87], v[188:191]\n"
  "v_perm_b32 v56, v132, v128, s69\n"
  "v_perm_b32 v57, v140, v136, s69\n"
  "v_mfma_f32_16x16x32_bf16 v[220:223], v[60:63], a[88:91], v[220:223]\n"
  "v_perm_b32 v58, v148, v144, s69\n"
  "v_perm_b32 v59, v156, v152, s69\n"
  "buffer_load_dwordx4 v[92:95], v9, s[44:47], s72 offen\n"
  "v_mfma_f32_16x16x32_bf16 v[252:255], v[60:63], a[92:95], v[252:255]\n"
  "v_mfma_f32_16x16x32_bf16 a[28:31], v[60:63], a[96:99], a[28:31]\n"
  "s_waitcnt lgkmcnt(0)\n"
  "v_mfma_f32_16x16x32_bf16 a[60:63], v[60:63], a[100:103], a[60:63]\n"
  "v_mfma_f32_16x16x32_bf16 v[160:163], v[56:59], a[64:67], v[160:163]\n"
  "v_perm_b32 v60, v132, v128, s70\n"
  "v_perm_b32 v61, v140, v136, s70\n"
  "s_xor_b32 s72, s67, 0x100000\n"
  "buffer_load_dwordx4 v[96:99], v2, s[44:47], s72 offen\n"
  "v_mfma_f32_16x16x32_bf16 v[192:195], v[56:59], a[68:71], v[192:195]\n"
  "v_perm_b32 v62, v148, v144, s70\n"
  "v_perm_b32 v63, v156, v152, s70\n"
  "v_mfma_f32_16x16x32_bf16 v[224:227], v[56:59], a[72:75], v[224:227]\n"
  "buffer_load_dwordx4 v[100:103], v3, s[44:47], s72 offen\n"
  "v_mfma_f32_16x16x32_bf16 a[0:3], v[56:59], a[76:79], a[0:3]\n"
  "v_xor_b32 v12, 0xc0, v11\n"
  "v_mfma_f32_16x16x32_bf16 a[32:35], v[56:59], a[80:83], a[32:35]\n"
  "buffer_load_dwordx4 v[104:107], v4, s[44:47], s72 offen\n"
  "v_mfma_f32_16x16x32_bf16 v[164:167], v[60:63], a[64:67], v[164:167]\n"
  "v_perm_b32 v56, v133, v129, s69\n"
  "v_perm_b32 v57, v141, v137, s69\n"
  "v_mfma_f32_16x16x32_bf16 v[196:199], v[60:63], a[68:71], v[196:199]\n"
  "v_perm_b32 v58, v149, v145, s69\n"
  "v_perm_b32 v59, v157, v153, s69\n"
  "buffer_load_dwordx4 v[108:111], v5, s[44:47], s72 offen\n"
  "v_mfma_f32_16x16x32_bf16 v[228:231], v[60:63], a[72:75], v[228:231]\n"
  "v_mfma_f32_16x16x32_bf16 a[4:7], v[60:63], a[76:79], a[4:7]\n"
  "buffer_load_dwordx4 v[112:115], v6, s[44:47], s72 offen\n"
  "ds_read_b128 a[84:87], v12\n"
  "v_mfma_f32_16x16x32_bf16 a[36:39], v[60:63], a[80:83], a[36:39]\n"
  "v_mfma_f32_16x16x32_bf16 v[168:171], v[56:59], a[64:67], v[168:171]\n"
  "v_perm_b32 v60, v133, v129, s70\n"
  "v_perm_b32 v61, v141, v137, s70\n"
  "buffer_load_dwordx4 v[116:119], v7, s[44:47], s72 offen\n"
  "v_mfma_f32_16x16x32_bf16 v[200:203], v[56:59], a[68:71], v[200:203]\n"
  "v_perm_b32 v62, v149, v145, s70\n"
  "v_perm_b32 v63, v157, v153, s70\n"
  "v_mfma_f32_16x16x32_bf16 v[232:235], v[56:59], a[72:75], v[232:235]\n"
  "buffer_load_dwordx4 v[120:123], v8, s[44:47], s72 offen\n"
  "v_mfma_f32_16x16x32_bf16 a[8:11], v[56:59], a[76:79], a[8:11]\n"
  "ds_read_b128 a[88:91], v12 offset:4096\n"
  "v_mfma_f32_16x16x32_bf16 a[40:43], v[56:59], a[80:83], a[40:43]\n"
  "buffer_load_dwordx4 v[124:127], v9, s[44:47], s72 offen\n"
  "v_mfma_f32_16x16x32_bf16 v[172:175], v[60:63], a[64:67], v[172:175]\n"
  "v_perm_b32 v56, v134, v130, s69\n"
  "v_perm_b32 v57, v142, v138, s69\n"
  "v_mfma_f32_16x16x32_bf16 v[204:207], v[60:63], a[68:71], v[204:207]\n"
  "v_perm_b32 v58, v150, v146, s69\n"
  "v_perm_b32 v59, v158, v154, s69\n"
  "v_mfma_f32_16x16x32_bf16 v[236:239], v[60:63], a[72:75], v[236:239]\n"
  "v_mfma_f32_16x16x32_bf16 a[12:15], v[60:63], a[76:79], a[12:15]\n"
  "ds_read_b128 a[92:95], v12 offset:8192\n"
  "v_mfma_f32_16x16x32_bf16 a[44:47], v[60:63], a[80:83], a[44:47]\n"
  "v_mfma_f32_16x16x32_bf16 v[176:179], v[56:59], a[64:67], v[176:179]\n"
  "v_perm_b32 v60, v134, v130, s70\n"
  "v_perm_b32 v61, v142, v138, s70\n"
  "v_mfma_f32_16x16x32_bf16 v[208:211], v[56:59], a[68:71], v[208:211]\n"
  "v_perm_b32 v62, v150, v146, s70\n"
  "v_perm_b32 v63, v158, v154, s70\n"
  "v_mfma_f32_16x16x32_bf16 v[240:243], v[56:59], a[72:75], v[240:243]\n"
  "v_mfma_f32_16x16x32_bf16 a[16:19], v[56:59], a[76:79], a[16:19]\n"
  "ds_read_b128 a[96:99], v12 offset:12288\n"
  "v_mfma_f32_16x16x32_bf16 a[48:51], v[56:59], a[80:83], a[48:51]\n"
  "v_mfma_f32_16x16x32_bf16 v[180:183], v[60:63], a[64:67], v[180:183]\n"
  "v_perm_b32 v56, v135, v131, s69\n"
  "v_perm_b32 v57, v143, v139, s69\n"
  "v_mfma_f32_16x16x32_bf16 v[212:215], v[60:63], a[68:71], v[212:215]\n"
  "v_perm_b32 v58, v151, v147, s69\n"
  "v_perm_b32 v59, v159, v155, s69\n"
  "v_mfma_f32_16x16x32_bf16 v[244:247], v[60:63], a[72:75], v[244:247]\n"
  "v_mfma_f32_16x16x32_bf16 a[20:23], v[60:63], a[76:79], a[20:23]\n"
  "ds_read_b128 a[100:103], v12 offset:16384\n"
  "v_mfma_f32_16x16x32_bf16 a[52:55], v[60:63], a[80:83], a[52:55]\n"
  "v_mfma_f32_16x16x32_bf16 v[184:187], v[56:59], a[64:67], v[184:187]\n"
  "v_perm_b32 v60, v135, v131, s70\n"
  "v_perm_b32 v61, v143, v139, s70\n"
  "v_mfma_f32_16x16x32_bf16 v[216:219], v[56:59], a[68:71], v[216:219]\n"
  "v_perm_b32 v62, v151, v147, s70\n"
  "v_perm_b32 v63, v159, v155, s70\n"
  "v_mfma_f32_16x16x32_bf16 v[248:251], v[56:59], a[72:75], v[248:251]\n"
  "s_xor_b32 s72, s67, 0x140000\n"
  "buffer_load_dwordx4 v[128:131], v2, s[44:47], s72 offen\n"
  "v_mfma_f32_16x16x32_bf16 a[24:27], v[56:59], a[76:79], a[24:27]\n"
  "buffer_load_dwordx4 v[132:135], v3, s[44:47], s72 offen\n"
  "v_mfma_f32_16x16x32_bf16 a[56:59], v[56:59], a[80:83], a[56:59]\n"
  "s_waitcnt vmcnt(10)\n"
  "buffer_load_dwordx4 v[136:139], v4, s[44:47], s72 offen\n"
  "v_mfma_f32_16x16x32_bf16 v[188:191], v[60:63], a[64:67], v[188:191]\n"
  "v_perm_b32 v56, v68, v64, s69\n"
  "v_perm_b32 v57, v76, v72, s69\n"
  "buffer_load_dwordx4 v[140:143], v5, s[44:47], s72 offen\n"
  "v_mfma_f32_16x16x32_bf16 v[220:223], v[60:63], a[68:71], v[220:223]\n"
  "v_perm_b32 v58, v84, v80, s69\n"
  "v_perm_b32 v59, v92, v88, s69\n"
  "buffer_load_dwordx4 v[144:147], v6, s[44:47], s72 offen\n"
  "v_mfma_f32_16x16x32_bf16 v[252:255], v[60:63], a[72:75], v[252:255]\n"
  "buffer_load_dwordx4 v[148:151], v7, s[44:47], s72 offen\n"
  "v_mfma_f32_16x16x32_bf16 a[28:31], v[60:63], a[76:79], a[28:31]\n"
  "s_waitcnt lgkmcnt(0)\n"
  "buffer_load_dwordx4 v[152:155], v8, s[44:47], s72 offen\n"
  "v_mfma_f32_16x16x32_bf16 a[60:63], v[60:63], a[80:83], a[60:63]\n"
  "buffer_load_dwordx4 v[156:159], v9, s[44:47], s72 offen\n"
  "v_mfma_f32_16x16x32_bf16 v[160:163], v[56:59], a[84:87], v[160:163]\n"
  "v_perm_b32 v60, v68, v64, s70\n"
  "v_perm_b32 v61, v76, v72, s70\n"
  "v_mfma_f32_16x16x32_bf16 v[192:195], v[56:59], a[88:91], v[192:195]\n"
  "v_perm_b32 v62, v84, v80, s70\n"
  "v_perm_b32 v63, v92, v88, s70\n"
  "v_mfma_f32_16x16x32_bf16 v[224:227], v[56:59], a[92:95], v[224:227]\n"
  "s_add_u32 m0, s96, 0xa000\n"
  "s_xor_b32 s73, s68, 0x200\n"
  "buffer_load_dwordx4 v10, s[32:35], s73 offen lds\n"
  "v_mfma_f32_16x16x32_bf16 a[0:3], v[56:59], a[96:99], a[0:3]\n"
  "v_xor_b32 v12, 0x5000, v11\n"
  "v_mfma_f32_16x16x32_bf16 a[32:35], v[56:59], a[100:103], a[32:35]\n"
  "s_barrier\n"
  "s_add_u32 m0, s96, 0xb000\n"
  "s_xor_b32 s73, s68, 0x4200\n"
  "buffer_load_dwordx4 v10, s[32:35], s73 offen lds\n"
  "v_mfma_f32_16x16x32_bf16 v[164:167], v[60:63], a[84:87], v[164:167]\n"
  "v_perm_b32 v56, v69, v65, s69\n"
  "v_perm_b32 v57, v77, v73, s69\n"
  "s_add_u32 m0, s96, 0xc000\n"
  "s_xor_b32 s73, s68, 0x8200\n"
  "buffer_load_dwordx4 v10, s[32:35], s73 offen lds\n"
  "v_mfma_f32_16x16x32_bf16 v[196:199], v[60:63], a[88:91], v[196:199]\n"
  "v_perm_b32 v58, v85, v81, s69\n"
  "v_perm_b32 v59, v93, v89, s69\n"
  "v_mfma_f32_16x16x32_bf16 v[228:231], v[60:63], a[92:95], v[228:231]\n"
  "s_add_u32 m0, s96, 0xd000\n"
  "s_xor_b32 s73, s68, 0xc200\n"
  "buffer_load_dwordx4 v10, s[32:35], s73 offen lds\n"
  "v_mfma_f32_16x16x32_bf16 a[4:7], v[60:63], a[96:99], a[4:7]\n"
  "ds_read_b128 a[64:67], v12\n"
  "v_mfma_f32_16x16x32_bf16 a[36:39], v[60:63], a[100:103], a[36:39]\n"
  "s_add_u32 m0, s96, 0xe000\n"
  "s_xor_b32 s73, s68, 0x10200\n"
  "buffer_load_dwordx4 v10, s[32:35], s73 offen lds\n"
  "v_mfma_f32_16x16x32_bf16 v[168:171], v[56:59], a[84:87], v[168:171]\n"
  "v_perm_b32 v60, v69, v65, s70\n"
  "v_perm_b32 v61, v77, v73, s70\n"
  "s_add_u32 m0, s77, 0x1000\n"
  "s_nop 0\n"
  "buffer_load_dwordx4 v16, s[36:39], s92 offen offset:3072 sc1 nt lds\n"
  "v_mfma_f32_16x16x32_bf16 v[200:203], v[56:59], a[88:91], v[200:203]\n"
  "v_perm_b32 v62, v85, v81, s70\n"
  "v_perm_b32 v63, v93, v89, s70\n"
  "v_mfma_f32_16x16x32_bf16 v[232:235], v[56:59], a[92:95], v[232:235]\n"
  "s_add_u32 m0, s77, 0x2000\n"
  "s_nop 0\n"
  "buffer_load_dwordx4 v13, s[36:39], s93 offen sc1 nt lds\n"
  "v_mfma_f32_16x16x32_bf16 a[8:11], v[56:59], a[96:99], a[8:11]\n"
  "ds_read_b128 a[68:71], v12 offset:4096\n"
  "v_mfma_f32_16x16x32_bf16 a[40:43], v[56:59], a[100:103], a[40:43]\n"
  "buffer_load_dwordx4 v14, s[36:39], s93 offen offset:1024 sc1 nt lds\n"
  "v_mfma_f32_16x16x32_bf16 v[172:175], v[60:63], a[84:87], v[172:175]\n"
  "v_perm_b32 v56, v70, v66, s69\n"
  "v_perm_b32 v57, v78, v74, s69\n"
  "v_mfma_f32_16x16x32_bf16 v[204:207], v[60:63], a[88:91], v[204:207]\n"
  "v_perm_b32 v58, v86, v82, s69\n"
  "v_perm_b32 v59, v94, v90, s69\n"
  "v_mfma_f32_16x16x32_bf16 v[236:239], v[60:63], a[92:95], v[236:239]\n"
  "v_mfma_f32_16x16x32_bf16 a[12:15], v[60:63], a[96:99], a[12:15]\n"
  "ds_read_b128 a[72:75], v12 offset:8192\n"
  "v_mfma_f32_16x16x32_bf16 a[44:47], v[60:63], a[100:103], a[44:47]\n"
  "v_mfma_f32_16x16x32_bf16 v[176:179], v[56:59], a[84:87], v[176:179]\n"
  "v_perm_b32 v60, v70, v66, s70\n"
  "v_perm_b32 v61, v78, v74, s70\n"
  "v_mfma_f32_16x16x32_bf16 v[208:211], v[56:59], a[88:91], v[208:211]\n"
  "v_perm_b32 v62, v86, v82, s70\n"
  "v_perm_b32 v63, v94, v90, s70\n"
  "v_mfma_f32_16x16x32_bf16 v[240:243], v[56:59], a[92:95], v[240:243]\n"
  "v_mfma_f32_16x16x32_bf16 a[16:19], v[56:59], a[96:99], a[16:19]\n"
  "ds_read_b128 a[76:79], v12 offset:12288\n"
  "v_mfma_f32_16x16x32_bf16 a[48:51], v[56:59], a[100:103], a[48:51]\n"
  "v_mfma_f32_16x16x32_bf16 v[180:183], v[60:63], a[84:87], v[180:183]\n"
  "v_perm_b32 v56, v71, v67, s69\n"
  "v_perm_b32 v57, v79, v75, s69\n"
  "v_mfma_f32_16x16x32_bf16 v[212:215], v[60:63], a[88:91], v[212:215]\n"
  "v_perm_b32 v58, v87, v83, s69\n"
  "v_perm_b32 v59, v95, v91, s69\n"
  "v_mfma_f32_16x16x32_bf16 v[244:247], v[60:63], a[92:95], v[244:247]\n"
  "v_mfma_f32_16x16x32_bf16 a[20:23], v[60:63], a[96:99], a[20:23]\n"
  "ds_read_b128 a[80:83], v12 offset:16384\n"
  "v_mfma_f32_16x16x32_bf16 a[52:55], v[60:63], a[100:103], a[52:55]\n"
  "v_mfma_f32_16x16x32_bf16 v[184:187], v[56:59], a[84:87], v[184:187]\n"
  "v_perm_b32 v60, v71, v67, s70\n"
  "v_perm_b32 v61, v79, v75, s70\n"
  "v_mfma_f32_16x16x32_bf16 v[216:219], v[56:59], a[88:91], v[216:219]\n"
  "v_perm_b32 v62, v87, v83, s70\n"
  "v_perm_b32 v63, v95, v91, s70\n"
  "v_mfma_f32_16x16x32_bf16 v[248:251], v[56:59], a[92:95], v[248:251]\n"
  "v_mfma_f32_16x16x32_bf16 a[24:27], v[56:59], a[96:99], a[24:27]\n"
  "v_mfma_f32_16x16x32_bf16 a[56:59], v[56:59], a[100:103], a[56:59]\n"
  "s_waitcnt vmcnt(16)\n"
  "v_mfma_f32_16x16x32_bf16 v[188:191], v[60:63], a[84:87], v[188:191]\n"
  "v_perm_b32 v56, v100, v96, s69\n"
  "v_perm_b32 v57, v108, v104, s69\n"
  "v_mfma_f32_16x16x32_bf16 v[220:223], v[60:63], a[88:91], v[220:223]\n"
  "v_perm_b32 v58, v116, v112, s69\n"
  "v_perm_b32 v59, v124, v120, s69\n"
  "v_mfma_f32_16x16x32_bf16 v[252:255], v[60:63], a[92:95], v[252:255]\n"
  "v_mfma_f32_16x16x32_bf16 a[28:31], v[60:63], a[96:99], a[28:31]\n"
  "s_waitcnt lgkmcnt(0)\n"
  "v_mfma_f32_16x16x32_bf16 a[60:63], v[60:63], a[100:103], a[60:63]\n"
  "v_mfma_f32_16x16x32_bf16 v[160:163], v[56:59], a[64:67], v[160:163]\n"
  "v_perm_b32 v60, v100, v96, s70\n"
  "v_perm_b32 v61, v108, v104, s70\n"
  "v_mfma_f32_16x16x32_bf16 v[192:195], v[56:59], a[68:71], v[192:195]\n"
  "v_perm_b32 v62, v116, v112, s70\n"
  "v_perm_b32 v63, v124, v120, s70\n"
  "v_mfma_f32_16x16x32_bf16 v[224:227], v[56:59], a[72:75], v[224:227]\n"
  "v_mfma_f32_16x16x32_bf16 a[0:3], v[56:59], a[76:79], a[0:3]\n"
  "v_xor_b32 v12, 0x5040, v11\n"
  "v_mfma_f32_16x16x32_bf16 a[32:35], v[56:59], a[80:83], a[32:35]\n"
  "v_mfma_f32_16x16x32_bf16 v[164:167], v[60:63], a[64:67], v[164:167]\n"
  "v_perm_b32 v56, v101, v97, s69\n"
  "v_perm_b32 v57, v109, v105, s69\n"
  "v_mfma_f32_16x16x32_bf16 v[196:199], v[60:63], a[68:71], v[196:199]\n"
  "v_perm_b32 v58, v117, v113, s69\n"
  "v_perm_b32 v59, v125, v121, s69\n"
  "v_mfma_f32_16x16x32_bf16 v[228:231], v[60:63], a[72:75], v[228:231]\n"
  "v_mfma_f32_16x16x32_bf16 a[4:7], v[60:63], a[76:79], a[4:7]\n"
  "ds_read_b128 a[84:87], v12\n"
  "v_mfma_f32_16x16x32_bf16 a[36:39], v[60:63], a[80:83], a[36:39]\n"
  "v_mfma_f32_16x16x32_bf16 v[168:171], v[56:59], a[64:67], v[168:171]\n"
  "v_perm_b32 v60, v101, v97, s70\n"
  "v_perm_b32 v61, v109, v105, s70\n"
  "v_mfma_f32_16x16x32_bf16 v[200:203], v[56:59], a[68:71], v[200:203]\n"
  "v_perm_b32 v62, v117, v113, s70\n"
  "v_perm_b32 v63, v125, v121, s70\n"
  "v_mfma_f32_16x16x32_bf16 v[232:235], v[56:59], a[72:75], v[232:235]\n"
  "v_mfma_f32_16x16x32_bf16 a[8:11], v[56:59], a[76:79], a[8:11]\n"
  "ds_read_b128 a[88:91], v12 offset:4096\n"
  "v_mfma_f32_16x16x32_bf16 a[40:43], v[56:59], a[80:83], a[40:43]\n"
  "v_mfma_f32_16x16x32_bf16 v[172:175], v[60:63], a[64:67], v[172:175]\n"
  "v_perm_b32 v56, v102, v98, s69\n"
  "v_perm_b32 v57, v110, v106, s69\n"
  "v_mfma_f32_16x16x32_bf16 v[204:207], v[60:63], a[68:71], v[204:207]\n"
  "v_perm_b32 v58, v118, v114, s69\n"
  "v_perm_b32 v59, v126, v122, s69\n"
  "v_mfma_f32_16x16x32_bf16 v[236:239], v[60:63], a[72:75], v[236:239]\n"
  "v_mfma_f32_16x16x32_bf16 a[12:15], v[60:63], a[76:79], a[12:15]\n"
  "ds_read_b128 a[92:95], v12 offset:8192\n"
  "v_mfma_f32_16x16x32_bf16 a[44:47], v[60:63], a[80:83], a[44:47]\n"
  "v_mfma_f32_16x16x32_bf16 v[176:179], v[56:59], a[64:67], v[176:179]\n"
  "v_perm_b32 v60, v102, v98, s70\n"
  "v_perm_b32 v61, v110, v106, s70\n"
  "v_mfma_f32_16x16x32_bf16 v[208:211], v[56:59], a[68:71], v[208:211]\n"
  "v_perm_b32 v62, v118, v114, s70\n"
  "v_perm_b32 v63, v126, v122, s70\n"
  "v_mfma_f32_16x16x32_bf16 v[240:243], v[56:59], a[72:75], v[240:243]\n"
  "s_xor_b32 s72, s67, 0x180000\n"
  "buffer_load_dwordx4 v[64:67], v2, s[44:47], s72 offen\n"
  "v_mfma_f32_16x16x32_bf16 a[16:19], v[56:59], a[76:79], a[16:19]\n"
  "ds_read_b128 a[96:99], v12 offset:12288\n"
  "v_mfma_f32_16x16x32_bf16 a[48:51], v[56:59], a[80:83], a[48:51]\n"
  "buffer_load_dwordx4 v[68:71], v3, s[44:47], s72 offen\n"
  "v_mfma_f32_16x16x32_bf16 v[180:183], v[60:63], a[64:67], v[180:183]\n"
  "v_perm_b32 v56, v103, v99, s69\n"
  "v_perm_b32 v57, v111, v107, s69\n"
  "v_mfma_f32_16x16x32_bf16 v[212:215], v[60:63], a[68:71], v[212:215]\n"
  "v_perm_b32 v58, v119, v115, s69\n"
  "v_perm_b32 v59, v127, v123, s69\n"
  "buffer_load_dwordx4 v[72:75], v4, s[44:47], s72 offen\n"
  "v_mfma_f32_16x16x32_bf16 v[244:247], v[60:63], a[72:75], v[244:247]\n"
  "v_mfma_f32_16x16x32_bf16 a[20:23], v[60:63], a[76:79], a[20:23]\n"
  "buffer_load_dwordx4 v[76:79], v5, s[44:47], s72 offen\n"
  "ds_read_b128 a[100:103], v12 offset:16384\n"
  "v_mfma_f32_16x16x32_bf16 a[52:55], v[60:63], a[80:83], a[52:55]\n"
  "v_mfma_f32_16x16x32_bf16 v[184:187], v[56:59], a[64:67], v[184:187]\n"
  "v_perm_b32 v60, v103, v99, s70\n"
  "v_perm_b32 v61, v111, v107, s70\n"
  "buffer_load_dwordx4 v[80:83], v6, s[44:47], s72 offen\n"
  "v_mfma_f32_16x16x32_bf16 v[216:219], v[56:59], a[68:71], v[216:219]\n"
  "v_perm_b32 v62, v119, v115, s70\n"
  "v_perm_b32 v63, v127, v123, s70\n"
  "v_mfma_f32_16x16x32_bf16 v[248:251], v[56:59], a[72:75], v[248:251]\n"
  "buffer_load_dwordx4 v[84:87], v7, s[44:47], s72 offen\n"
  "v_mfma_f32_16x16x32_bf16 a[24:27], v[56:59], a[76:79], a[24:27]\n"
  "v_mfma_f32_16x16x32_bf16 a[56:59], v[56:59], a[80:83], a[56:59]\n"
  "s_waitcnt vmcnt(14)\n"
  "buffer_load_dwordx4 v[88:91], v8, s[44:47], s72 offen\n"
  "v_mfma_f32_16x16x32_bf16 v[188:191], v[60:63], a[64:67], v[188:191]\n"
  "v_perm_b32 v56, v132, v128, s69\n"
  "v_perm_b32 v57, v140, v136, s69\n"
  "v_mfma_f32_16x16x32_bf16 v[220:223], v[60:63], a[68:71], v[220:223]\n"
  "v_perm_b32 v58, v148, v144, s69\n"
  "v_perm_b32 v59, v156, v152, s69\n"
  "buffer_load_dwordx4 v[92:95], v9, s[44:47], s72 offen\n"
  "v_mfma_f32_16x16x32_bf16 v[252:255], v[60:63], a[72:75], v[252:255]\n"
  "v_mfma_f32_16x16x32_bf16 a[28:31], v[60:63], a[76:79], a[28:31]\n"
  "s_waitcnt lgkmcnt(0)\n"
  "v_mfma_f32_16x16x32_bf16 a[60:63], v[60:63], a[80:83], a[60:63]\n"
  "v_mfma_f32_16x16x32_bf16 v[160:163], v[56:59], a[84:87], v[160:163]\n"
  "v_perm_b32 v60, v132, v128, s70\n"
  "v_perm_b32 v61, v140, v136, s70\n"
  "s_xor_b32 s72, s67, 0x1c0000\n"
  "buffer_load_dwordx4 v[96:99], v2, s[44:47], s72 offen\n"
  "v_mfma_f32_16x16x32_bf16 v[192:195], v[56:59], a[88:91], v[192:195]\n"
  "v_perm_b32 v62, v148, v144, s70\n"
  "v_perm_b32 v63, v156, v152, s70\n"
  "v_mfma_f32_16x16x32_bf16 v[224:227], v[56:59], a[92:95], v[224:227]\n"
  "buffer_load_dwordx4 v[100:103], v3, s[44:47], s72 offen\n"
  "v_mfma_f32_16x16x32_bf16 a[0:3], v[56:59], a[96:99], a[0:3]\n"
  "v_xor_b32 v12, 0x5080, v11\n"
  "v_mfma_f32_16x16x32_bf16 a[32:35], v[56:59], a[100:103], a[32:35]\n"
  "buffer_load_dwordx4 v[104:107], v4, s[44:47], s72 offen\n"
  "v_mfma_f32_16x16x32_bf16 v[164:167], v[60:63], a[84:87], v[164:167]\n"
  "v_perm_b32 v56, v133, v129, s69\n"
  "v_perm_b32 v57, v141, v137, s69\n"
  "v_mfma_f32_16x16x32_bf16 v[196:199], v[60:63], a[88:91], v[196:199]\n"
  "v_perm_b32 v58, v149, v145, s69\n"
  "v_perm_b32 v59, v157, v153, s69\n"
  "buffer_load_dwordx4 v[108:111], v5, s[44:47], s72 offen\n"
  "v_mfma_f32_16x16x32_bf16 v[228:231], v[60:63], a[92:95], v[228:231]\n"
  "v_mfma_f32_16x16x32_bf16 a[4:7], v[60:63], a[96:99], a[4:7]\n"
  "buffer_load_dwordx4 v[112:115], v6, s[44:47], s72 offen\n"
  "ds_read_b128 a[64:67], v12\n"
  "v_mfma_f32_16x16x32_bf16 a[36:39], v[60:63], a[100:103], a[36:39]\n"
  "v_mfma_f32_16x16x32_bf16 v[168:171], v[56:59], a[84:87], v[168:171]\n"
  "v_perm_b32 v60, v133, v129, s70\n"
  "v_perm_b32 v61, v141, v137, s70\n"
  "buffer_load_dwordx4 v[116:119], v7, s[44:47], s72 offen\n"
  "v_mfma_f32_16x16x32_bf16 v[200:203], v[56:59], a[88:91], v[200:203]\n"
  "v_perm_b32 v62, v149, v145, s70\n"
  "v_perm_b32 v63, v157, v153, s70\n"
  "v_mfma_f32_16x16x32_bf16 v[232:235], v[56:59], a[92:95], v[232:235]\n"
  "buffer_load_dwordx4 v[120:123], v8, s[44:47], s72 offen\n"
  "v_mfma_f32_16x16x32_bf16 a[8:11], v[56:59], a[96:99], a[8:11]\n"
  "ds_read_b128 a[68:71], v12 offset:4096\n"
  "v_mfma_f32_16x16x32_bf16 a[40:43], v[56:59], a[100:103], a[40:43]\n"
  "buffer_load_dwordx4 v[124:127], v9, s[44:47], s72 offen\n"
  "v_mfma_f32_16x16x32_bf16 v[172:175], v[60:63], a[84:87], v[172:175]\n"
  "v_perm_b32 v56, v134, v130, s69\n"
  "v_perm_b32 v57, v142, v138, s69\n"
  "v_mfma_f32_16x16x32_bf16 v[204:207], v[60:63], a[88:91], v[204:207]\n"
  "v_perm_b32 v58, v150, v146, s69\n"
  "v_perm_b32 v59, v158, v154, s69\n"
  "v_mfma_f32_16x16x32_bf16 v[236:239], v[60:63], a[92:95], v[236:239]\n"
  "v_mfma_f32_16x16x32_bf16 a[12:15], v[60:63], a[96:99], a[12:15]\n"
  "ds_read_b128 a[72:75], v12 offset:8192\n"
  "v_mfma_f32_16x16x32_bf16 a[44:47], v[60:63], a[100:103], a[44:47]\n"
  "v_mfma_f32_16x16x32_bf16 v[176:179], v[56:59], a[84:87], v[176:179]\n"
  "v_perm_b32 v60, v134, v130, s70\n"
  "v_perm_b32 v61, v142, v138, s70\n"
  "v_mfma_f32_16x16x32_bf16 v[208:211], v[56:59], a[88:91], v[208:211]\n"
  "v_perm_b32 v62, v150, v146, s70\n"
  "v_perm_b32 v63, v158, v154, s70\n"
  "v_mfma_f32_16x16x32_bf16 v[240:243], v[56:59], a[92:95], v[240:243]\n"
  "v_mfma_f32_16x16x32_bf16 a[16:19], v[56:59], a[96:99], a[16:19]\n"
  "ds_read_b128 a[76:79], v12 offset:12288\n"
  "v_mfma_f32_16x16x32_bf16 a[48:51], v[56:59], a[100:103], a[48:51]\n"
  "v_mfma_f32_16x16x32_bf16 v[180:183], v[60:63], a[84:87], v[180:183]\n"
  "v_perm_b32 v56, v135, v131, s69\n"
  "v_perm_b32 v57, v143, v139, s69\n"
  "v_mfma_f32_16x16x32_bf16 v[212:215], v[60:63], a[88:91], v[212:215]\n"
  "v_perm_b32 v58, v151, v147, s69\n"
  "v_perm_b32 v59, v159, v155, s69\n"
  "v_mfma_f32_16x16x32_bf16 v[244:247], v[60:63], a[92:95], v[244:247]\n"
  "v_mfma_f32_16x16x32_bf16 a[20:23], v[60:63], a[96:99], a[20:23]\n"
  "ds_read_b128 a[80:83], v12 offset:16384\n"
  "v_mfma_f32_16x16x32_bf16 a[52:55], v[60:63], a[100:103], a[52:55]\n"
  "v_mfma_f32_16x16x32_bf16 v[184:187], v[56:59], a[84:87], v[184:187]\n"
  "v_perm_b32 v60, v135, v131, s70\n"
  "v_perm_b32 v61, v143, v139, s70\n"
  "v_mfma_f32_16x16x32_bf16 v[216:219], v[56:59], a[88:91], v[216:219]\n"
  "v_perm_b32 v62, v151, v147, s70\n"
  "v_perm_b32 v63, v159, v155, s70\n"
  "v_mfma_f32_16x16x32_bf16 v[248:251], v[56:59], a[92:95], v[248:251]\n"
  "s_xor_b32 s72, s67, 0x200000\n"
  "buffer_load_dwordx4 v[128:131], v2, s[44:47], s72 offen\n"
  "v_mfma_f32_16x16x32_bf16 a[24:27], v[56:59], a[96:99], a[24:27]\n"
  "buffer_load_dwordx4 v[132:135], v3, s[44:47], s72 offen\n"
  "v_mfma_f32_16x16x32_bf16 a[56:59], v[56:59], a[100:103], a[56:59]\n"
  "s_waitcnt vmcnt(10)\n"
  "buffer_load_dwordx4 v[136:139], v4, s[44:47], s72 offen\n"
  "v_mfma_f32_16x16x32_bf16 v[188:191], v[60:63], a[84:87], v[188:191]\n"
  "v_perm_b32 v56, v68, v64, s69\n"
  "v_perm_b32 v57, v76, v72, s69\n"
  "buffer_load_dwordx4 v[140:143], v5, s[44:47], s72 offen\n"
  "v_mfma_f32_16x16x32_bf16 v[220:223], v[60:63], a[88:91], v[220:223]\n"
  "v_perm_b32 v58, v84, v80, s69\n"
  "v_perm_b32 v59, v92, v88, s69\n"
  "buffer_load_dwordx4 v[144:147], v6, s[44:47], s72 offen\n"
  "v_mfma_f32_16x16x32_bf16 v[252:255], v[60:63], a[92:95], v[252:255]\n"
  "buffer_load_dwordx4 v[148:151], v7, s[44:47], s72 offen\n"
  "v_mfma_f32_16x16x32_bf16 a[28:31], v[60:63], a[96:99], a[28:31]\n"
  "s_waitcnt lgkmcnt(0)\n"
  "buffer_load_dwordx4 v[152:155], v8, s[44:47], s72 offen\n"
  "v_mfma_f32_16x16x32_bf16 a[60:63], v[60:63], a[100:103], a[60:63]\n"
  "buffer_load_dwordx4 v[156:159], v9, s[44:47], s72 offen\n"
  "v_mfma_f32_16x16x32_bf16 v[160:163], v[56:59], a[64:67], v[160:163]\n"
  "v_perm_b32 v60, v68, v64, s70\n"
  "v_perm_b32 v61, v76, v72, s70\n"
  "v_mfma_f32_16x16x32_bf16 v[192:195], v[56:59], a[68:71], v[192:195]\n"
  "v_perm_b32 v62, v84, v80, s70\n"
  "v_perm_b32 v63, v92, v88, s70\n"
  "v_mfma_f32_16x16x32_bf16 v[224:227], v[56:59], a[72:75], v[224:227]\n"
  "s_add_u32 m0, s96, 0xf000\n"
  "s_xor_b32 s73, s68, 0x300\n"
  "buffer_load_dwordx4 v10, s[32:35], s73 offen lds\n"
  "v_mfma_f32_16x16x32_bf16 a[0:3], v[56:59], a[76:79], a[0:3]\n"
  "v_xor_b32 v12, 0x50c0, v11\n"
  "v_mfma_f32_16x16x32_bf16 a[32:35], v[56:59], a[80:83], a[32:35]\n"
  "s_add_u32 m0, s96, 0x10000\n"
  "s_xor_b32 s73, s68, 0x4300\n"
  "buffer_load_dwordx4 v10, s[32:35], s73 offen lds\n"
  "v_mfma_f32_16x16x32_bf16 v[164:167], v[60:63], a[64:67], v[164:167]\n"
  "v_perm_b32 v56, v69, v65, s69\n"
  "v_perm_b32 v57, v77, v73, s69\n"
  "s_add_u32 m0, s96, 0x11000\n"
  "s_xor_b32 s73, s68, 0x8300\n"
  "buffer_load_dwordx4 v10, s[32:35], s73 offen lds\n"
  "v_mfma_f32_16x16x32_bf16 v[196:199], v[60:63], a[68:71], v[196:199]\n"
  "v_perm_b32 v58, v85, v81, s69\n"
  "v_perm_b32 v59, v93, v89, s69\n"
  "v_mfma_f32_16x16x32_bf16 v[228:231], v[60:63], a[72:75], v[228:231]\n"
  "s_add_u32 m0, s96, 0x12000\n"
  "s_xor_b32 s73, s68, 0xc300\n"
  "buffer_load_dwordx4 v10, s[32:35], s73 offen lds\n"
  "v_mfma_f32_16x16x32_bf16 a[4:7], v[60:63], a[76:79], a[4:7]\n"
  "ds_read_b128 a[84:87], v12\n"
  "v_mfma_f32_16x16x32_bf16 a[36:39], v[60:63], a[80:83], a[36:39]\n"
  "s_add_u32 m0, s96, 0x13000\n"
  "s_xor_b32 s73, s68, 0x10300\n"
  "buffer_load_dwordx4 v10, s[32:35], s73 offen lds\n"
  "v_mfma_f32_16x16x32_bf16 v[168:171], v[56:59], a[64:67], v[168:171]\n"
  "v_perm_b32 v60, v69, v65, s70\n"
  "v_perm_b32 v61, v77, v73, s70\n"
  "s_add_u32 m0, s77, 0x2000\n"
  "s_nop 0\n"
  "buffer_load_dwordx4 v15, s[36:39], s93 offen offset:2048 sc1 nt lds\n"
  "v_mfma_f32_16x16x32_bf16 v[200:203], v[56:59], a[68:71], v[200:203]\n"
  "v_perm_b32 v62, v85, v81, s70\n"
  "v_perm_b32 v63, v93, v89, s70\n"
  "v_mfma_f32_16x16x32_bf16 v[232:235], v[56:59], a[72:75], v[232:235]\n"
  "buffer_load_dwordx4 v16, s[36:39], s93 offen offset:3072 sc1 nt lds\n"
  "v_mfma_f32_16x16x32_bf16 a[8:11], v[56:59], a[76:79], a[8:11]\n"
  "ds_read_b128 a[88:91], v12 offset:4096\n"
  "v_mfma_f32_16x16x32_bf16 a[40:43], v[56:59], a[80:83], a[40:43]\n"
  "s_add_u32 m0, s77, 0x3000\n"
  "s_nop 0\n"
  "buffer_load_dwordx4 v13, s[36:39], s94 offen sc1 nt lds\n"
  "v_mfma_f32_16x16x32_bf16 v[172:175], v[60:63], a[64:67], v[172:175]\n"
  "v_perm_b32 v56, v70, v66, s69\n"
  "v_perm_b32 v57, v78, v74, s69\n"
  "v_mfma_f32_16x16x32_bf16 v[204:207], v[60:63], a[68:71], v[204:207]\n"
  "v_perm_b32 v58, v86, v82, s69\n"
  "v_perm_b32 v59, v94, v90, s69\n"
  "v_mfma_f32_16x16x32_bf16 v[236:239], v[60:63], a[72:75], v[236:239]\n"
  "v_mfma_f32_16x16x32_bf16 a[12:15], v[60:63], a[76:79], a[12:15]\n"
  "ds_read_b128 a[92:95], v12 offset:8192\n"
  "v_mfma_f32_16x16x32_bf16 a[44:47], v[60:63], a[80:83], a[44:47]\n"
  "v_mfma_f32_16x16x32_bf16 v[176:179], v[56:59], a[64:67], v[176:179]\n"
  "v_perm_b32 v60, v70, v66, s70\n"
  "v_perm_b32 v61, v78, v74, s70\n"
  "v_mfma_f32_16x16x32_bf16 v[208:211], v[56:59], a[68:71], v[208:211]\n"
  "v_perm_b32 v62, v86, v82, s70\n"
  "v_perm_b32 v63, v94, v90, s70\n"
  "v_mfma_f32_16x16x32_bf16 v[240:243], v[56:59], a[72:75], v[240:243]\n"
  "v_mfma_f32_16x16x32_bf16 a[16:19], v[56:59], a[76:79], a[16:19]\n"
  "ds_read_b128 a[96:99], v12 offset:12288\n"
  "v_mfma_f32_16x16x32_bf16 a[48:51], v[56:59], a[80:83], a[48:51]\n"
  "v_mfma_f32_16x16x32_bf16 v[180:183], v[60:63], a[64:67], v[180:183]\n"
  "v_perm_b32 v56, v71, v67, s69\n"
  "v_perm_b32 v57, v79, v75, s69\n"
  "v_mfma_f32_16x16x32_bf16 v[212:215], v[60:63], a[68:71], v[212:215]\n"
  "v_perm_b32 v58, v87, v83, s69\n"
  "v_perm_b32 v59, v95, v91, s69\n"
  "v_mfma_f32_16x16x32_bf16 v[244:247], v[60:63], a[72:75], v[244:247]\n"
  "v_mfma_f32_16x16x32_bf16 a[20:23], v[60:63], a[76:79], a[20:23]\n"
  "ds_read_b128 a[100:103], v12 offset:16384\n"
  "v_mfma_f32_16x16x32_bf16 a[52:55], v[60:63], a[80:83], a[52:55]\n"
  "v_mfma_f32_16x16x32_bf16 v[184:187], v[56:59], a[64:67], v[184:187]\n"
  "v_perm_b32 v60, v71, v67, s70\n"
  "v_perm_b32 v61, v79, v75, s70\n"
  "v_mfma_f32_16x16x32_bf16 v[216:219], v[56:59], a[68:71], v[216:219]\n"
  "v_perm_b32 v62, v87, v83, s70\n"
  "v_perm_b32 v63, v95, v91, s70\n"
  "v_mfma_f32_16x16x32_bf16 v[248:251], v[56:59], a[72:75], v[248:251]\n"
  "v_mfma_f32_16x16x32_bf16 a[24:27], v[56:59], a[76:79], a[24:27]\n"
  "v_mfma_f32_16x16x32_bf16 a[56:59], v[56:59], a[80:83], a[56:59]\n"
  "s_waitcnt vmcnt(16)\n"
  "v_mfma_f32_16x16x32_bf16 v[188:191], v[60:63], a[64:67], v[188:191]\n"
  "v_perm_b32 v56, v100, v96, s69\n"
  "v_perm_b32 v57, v108, v104, s69\n"
  "v_mfma_f32_16x16x32_bf16 v[220:223], v[60:63], a[68:71], v[220:223]\n"
  "v_perm_b32 v58, v116, v112, s69\n"
  "v_perm_b32 v59, v124, v120, s69\n"
  "v_mfma_f32_16x16x32_bf16 v[252:255], v[60:63], a[72:75], v[252:255]\n"
  "v_mfma_f32_16x16x32_bf16 a[28:31], v[60:63], a[76:79], a[28:31]\n"
  "s_waitcnt lgkmcnt(0)\n"
  "v_mfma_f32_16x16x32_bf16 a[60:63], v[60:63], a[80:83], a[60:63]\n"
  "v_mfma_f32_16x16x32_bf16 v[160:163], v[56:59], a[84:87], v[160:163]\n"
  "v_perm_b32 v60, v100, v96, s70\n"
  "v_perm_b32 v61, v108, v104, s70\n"
  "v_mfma_f32_16x16x32_bf16 v[192:195], v[56:59], a[88:91], v[192:195]\n"
  "v_perm_b32 v62, v116, v112, s70\n"
  "v_perm_b32 v63, v124, v120, s70\n"
  "v_mfma_f32_16x16x32_bf16 v[224:227], v[56:59], a[92:95], v[224:227]\n"
  "v_mfma_f32_16x16x32_bf16 a[0:3], v[56:59], a[96:99], a[0:3]\n"
  "v_xor_b32 v12, 0xa000, v11\n"
  "v_mfma_f32_16x16x32_bf16 a[32:35], v[56:59], a[100:103], a[32:35]\n"
  "s_barrier\n"
  "v_mfma_f32_16x16x32_bf16 v[164:167], v[60:63], a[84:87], v[164:167]\n"
  "v_perm_b32 v56, v101, v97, s69\n"
  "v_perm_b32 v57, v109, v105, s69\n"
  "v_mfma_f32_16x16x32_bf16 v[196:199], v[60:63], a[88:91], v[196:199]\n"
  "v_perm_b32 v58, v117, v113, s69\n"
  "v_perm_b32 v59, v125, v121, s69\n"
  "v_mfma_f32_16x16x32_bf16 v[228:231], v[60:63], a[92:95], v[228:231]\n"
  "v_mfma_f32_16x16x32_bf16 a[4:7], v[60:63], a[96:99], a[4:7]\n"
  "ds_read_b128 a[64:67], v12\n"
  "v_mfma_f32_16x16x32_bf16 a[36:39], v[60:63], a[100:103], a[36:39]\n"
  "v_mfma_f32_16x16x32_bf16 v[168:171], v[56:59], a[84:87], v[168:171]\n"
  "v_perm_b32 v60, v101, v97, s70\n"
  "v_perm_b32 v61, v109, v105, s70\n"
  "v_mfma_f32_16x16x32_bf16 v[200:203], v[56:59], a[88:91], v[200:203]\n"
  "v_perm_b32 v62, v117, v113, s70\n"
  "v_perm_b32 v63, v125, v121, s70\n"
  "v_mfma_f32_16x16x32_bf16 v[232:235], v[56:59], a[92:95], v[232:235]\n"
  "v_mfma_f32_16x16x32_bf16 a[8:11], v[56:59], a[96:99], a[8:11]\n"
  "ds_read_b128 a[68:71], v12 offset:4096\n"
  "v_mfma_f32_16x16x32_bf16 a[40:43], v[56:59], a[100:103], a[40:43]\n"
  "v_mfma_f32_16x16x32_bf16 v[172:175], v[60:63], a[84:87], v[172:175]\n"
  "v_perm_b32 v56, v102, v98, s69\n"
  "v_perm_b32 v57, v110, v106, s69\n"
  "v_mfma_f32_16x16x32_bf16 v[204:207], v[60:63], a[88:91], v[204:207]\n"
  "v_perm_b32 v58, v118, v114, s69\n"
  "v_perm_b32 v59, v126, v122, s69\n"
  "v_mfma_f32_16x16x32_bf16 v[236:239], v[60:63], a[92:95], v[236:239]\n"
  "v_mfma_f32_16x16x32_bf16 a[12:15], v[60:63], a[96:99], a[12:15]\n"
  "ds_read_b128 a[72:75], v12 offset:8192\n"
  "v_mfma_f32_16x16x32_bf16 a[44:47], v[60:63], a[100:103], a[44:47]\n"
  "v_mfma_f32_16x16x32_bf16 v[176:179], v[56:59], a[84:87], v[176:179]\n"
  "v_perm_b32 v60, v102, v98, s70\n"
  "v_perm_b32 v61, v110, v106, s70\n"
  "v_mfma_f32_16x16x32_bf16 v[208:211], v[56:59], a[88:91], v[208:211]\n"
  "v_perm_b32 v62, v118, v114, s70\n"
  "v_perm_b32 v63, v126, v122, s70\n"
  "v_mfma_f32_16x16x32_bf16 v[240:243], v[56:59], a[92:95], v[240:243]\n"
  "s_xor_b32 s72, s67, 0x240000\n"
  "buffer_load_dwordx4 v[64:67], v2, s[44:47], s72 offen\n"
  "v_mfma_f32_16x16x32_bf16 a[16:19], v[56:59], a[96:99], a[16:19]\n"
  "ds_read_b128 a[76:79], v12 offset:12288\n"
  "v_mfma_f32_16x16x32_bf16 a[48:51], v[56:59], a[100:103], a[48:51]\n"
  "buffer_load_dwordx4 v[68:71], v3, s[44:47], s72 offen\n"
  "v_mfma_f32_16x16x32_bf16 v[180:183], v[60:63], a[84:87], v[180:183]\n"
  "v_perm_b32 v56, v103, v99, s69\n"
  "v_perm_b32 v57, v111, v107, s69\n"
  "v_mfma_f32_16x16x32_bf16 v[212:215], v[60:63], a[88:91], v[212:215]\n"
  "v_perm_b32 v58, v119, v115, s69\n"
  "v_perm_b32 v59, v127, v123, s69\n"
  "buffer_load_dwordx4 v[72:75], v4, s[44:47], s72 offen\n"
  "v_mfma_f32_16x16x32_bf16 v[244:247], v[60:63], a[92:95], v[244:247]\n"
  "v_mfma_f32_16x16x32_bf16 a[20:23], v[60:63], a[96:99], a[20:23]\n"
  "buffer_load_dwordx4 v[76:79], v5, s[44:47], s72 offen\n"
  "ds_read_b128 a[80:83], v12 offset:16384\n"
  "v_mfma_f32_16x16x32_bf16 a[52:55], v[60:63], a[100:103], a[52:55]\n"
  "v_mfma_f32_16x16x32_bf16 v[184:187], v[56:59], a[84:87], v[184:187]\n"
  "v_perm_b32 v60, v103, v99, s70\n"
  "v_perm_b32 v61, v111, v107, s70\n"
  "buffer_load_dwordx4 v[80:83], v6, s[44:47], s72 offen\n"
  "v_mfma_f32_16x16x32_bf16 v[216:219], v[56:59], a[88:91], v[216:219]\n"
  "v_perm_b32 v62, v119, v115, s70\n"
  "v_perm_b32 v63, v127, v123, s70\n"
  "v_mfma_f32_16x16x32_bf16 v[248:251], v[56:59], a[92:95], v[248:251]\n"
  "buffer_load_dwordx4 v[84:87], v7, s[44:47], s72 offen\n"
  "v_mfma_f32_16x16x32_bf16 a[24:27], v[56:59], a[96:99], a[24:27]\n"
  "v_mfma_f32_16x16x32_bf16 a[56:59], v[56:59], a[100:103], a[56:59]\n"
  "s_waitcnt vmcnt(14)\n"
  "buffer_load_dwordx4 v[88:91], v8, s[44:47], s72 offen\n"
  "v_mfma_f32_16x16x32_bf16 v[188:191], v[60:63], a[84:87], v[188:191]\n"
  "v_perm_b32 v56, v132, v128, s69\n"
  "v_perm_b32 v57, v140, v136, s69\n"
  "v_mfma_f32_16x16x32_bf16 v[220:223], v[60:63], a[88:91], v[220:223]\n"
  "v_perm_b32 v58, v148, v144, s69\n"
  "v_perm_b32 v59, v156, v152, s69\n"
  "buffer_load_dwordx4 v[92:95], v9, s[44:47], s72 offen\n"
  "v_mfma_f32_16x16x32_bf16 v[252:255], v[60:63], a[92:95], v[252:255]\n"
  "v_mfma_f32_16x16x32_bf16 a[28:31], v[60:63], a[96:99], a[28:31]\n"
  "s_waitcnt lgkmcnt(0)\n"
  "v_mfma_f32_16x16x32_bf16 a[60:63], v[60:63], a[100:103], a[60:63]\n"
  "v_mfma_f32_16x16x32_bf16 v[160:163], v[56:59], a[64:67], v[160:163]\n"
  "v_perm_b32 v60, v132, v128, s70\n"
  "v_perm_b32 v61, v140, v136, s70\n"
  "s_xor_b32 s72, s67, 0x280000\n"
  "buffer_load_dwordx4 v[96:99], v2, s[44:47], s72 offen\n"
  "v_mfma_f32_16x16x32_bf16 v[192:195], v[56:59], a[68:71], v[192:195]\n"
  "v_perm_b32 v62, v148, v144, s70\n"
  "v_perm_b32 v63, v156, v152, s70\n"
  "v_mfma_f32_16x16x32_bf16 v[224:227], v[56:59], a[72:75], v[224:227]\n"
  "buffer_load_dwordx4 v[100:103], v3, s[44:47], s72 offen\n"
  "v_mfma_f32_16x16x32_bf16 a[0:3], v[56:59], a[76:79], a[0:3]\n"
  "v_xor_b32 v12, 0xa040, v11\n"
  "v_mfma_f32_16x16x32_bf16 a[32:35], v[56:59], a[80:83], a[32:35]\n"
  "buffer_load_dwordx4 v[104:107], v4, s[44:47], s72 offen\n"
  "v_mfma_f32_16x16x32_bf16 v[164:167], v[60:63], a[64:67], v[164:167]\n"
  "v_perm_b32 v56, v133, v129, s69\n"
  "v_perm_b32 v57, v141, v137, s69\n"
  "v_mfma_f32_16x16x32_bf16 v[196:199], v[60:63], a[68:71], v[196:199]\n"
  "v_perm_b32 v58, v149, v145, s69\n"
  "v_perm_b32 v59, v157, v153, s69\n"
  "buffer_load_dwordx4 v[108:111], v5, s[44:47], s72 offen\n"
  "v_mfma_f32_16x16x32_bf16 v[228:231], v[60:63], a[72:75], v[228:231]\n"
  "v_mfma_f32_16x16x32_bf16 a[4:7], v[60:63], a[76:79], a[4:7]\n"
  "buffer_load_dwordx4 v[112:115], v6, s[44:47], s72 offen\n"
  "ds_read_b128 a[84:87], v12\n"
  "v_mfma_f32_16x16x32_bf16 a[36:39], v[60:63], a[80:83], a[36:39]\n"
  "v_mfma_f32_16x16x32_bf16 v[168:171], v[56:59], a[64:67], v[168:171]\n"
  "v_perm_b32 v60, v133, v129, s70\n"
  "v_perm_b32 v61, v141, v137, s70\n"
  "buffer_load_dwordx4 v[116:119], v7, s[44:47], s72 offen\n"
  "v_mfma_f32_16x16x32_bf16 v[200:203], v[56:59], a[68:71], v[200:203]\n"
  "v_perm_b32 v62, v149, v145, s70\n"
  "v_perm_b32 v63, v157, v153, s70\n"
  "v_mfma_f32_16x16x32_bf16 v[232:235], v[56:59], a[72:75], v[232:235]\n"
  "buffer_load_dwordx4 v[120:123], v8, s[44:47], s72 offen\n"
  "v_mfma_f32_16x16x32_bf16 a[8:11], v[56:59], a[76:79], a[8:11]\n"
  "ds_read_b128 a[88:91], v12 offset:4096\n"
  "v_mfma_f32_16x16x32_bf16 a[40:43], v[56:59], a[80:83], a[40:43]\n"
  "buffer_load_dwordx4 v[124:127], v9, s[44:47], s72 offen\n"
  "v_mfma_f32_16x16x32_bf16 v[172:175], v[60:63], a[64:67], v[172:175]\n"
  "v_perm_b32 v56, v134, v130, s69\n"
  "v_perm_b32 v57, v142, v138, s69\n"
  "v_mfma_f32_16x16x32_bf16 v[204:207], v[60:63], a[68:71], v[204:207]\n"
  "v_perm_b32 v58, v150, v146, s69\n"
  "v_perm_b32 v59, v158, v154, s69\n"
  "v_mfma_f32_16x16x32_bf16 v[236:239], v[60:63], a[72:75], v[236:239]\n"
  "v_mfma_f32_16x16x32_bf16 a[12:15], v[60:63], a[76:79], a[12:15]\n"
  "ds_read_b128 a[92:95], v12 offset:8192\n"
  "v_mfma_f32_16x16x32_bf16 a[44:47], v[60:63], a[80:83], a[44:47]\n"
  "v_mfma_f32_16x16x32_bf16 v[176:179], v[56:59], a[64:67], v[176:179]\n"
  "v_perm_b32 v60, v134, v130, s70\n"
  "v_perm_b32 v61, v142, v138, s70\n"
  "v_mfma_f32_16x16x32_bf16 v[208:211], v[56:59], a[68:71], v[208:211]\n"
  "v_perm_b32 v62, v150, v146, s70\n"
  "v_perm_b32 v63, v158, v154, s70\n"
  "v_mfma_f32_16x16x32_bf16 v[240:243], v[56:59], a[72:75], v[240:243]\n"
  "v_mfma_f32_16x16x32_bf16 a[16:19], v[56:59], a[76:79], a[16:19]\n"
  "ds_read_b128 a[96:99], v12 offset:12288\n"
  "v_mfma_f32_16x16x32_bf16 a[48:51], v[56:59], a[80:83], a[48:51]\n"
  "v_mfma_f32_16x16x32_bf16 v[180:183], v[60:63], a[64:67], v[180:183]\n"
  "v_perm_b32 v56, v135, v131, s69\n"
  "v_perm_b32 v57, v143, v139, s69\n"
  "v_mfma_f32_16x16x32_bf16 v[212:215], v[60:63], a[68:71], v[212:215]\n"
  "v_perm_b32 v58, v151, v147, s69\n"
  "v_perm_b32 v59, v159, v155, s69\n"
  "v_mfma_f32_16x16x32_bf16 v[244:247], v[60:63], a[72:75], v[244:247]\n"
  "v_mfma_f32_16x16x32_bf16 a[20:23], v[60:63], a[76:79], a[20:23]\n"
  "ds_read_b128 a[100:103], v12 offset:16384\n"
  "v_mfma_f32_16x16x32_bf16 a[52:55], v[60:63], a[80:83], a[52:55]\n"
  "v_mfma_f32_16x16x32_bf16 v[184:187], v[56:59], a[64:67], v[184:187]\n"
  "v_perm_b32 v60, v135, v131, s70\n"
  "v_perm_b32 v61, v143, v139, s70\n"
  "v_mfma_f32_16x16x32_bf16 v[216:219], v[56:59], a[68:71], v[216:219]\n"
  "v_perm_b32 v62, v151, v147, s70\n"
  "v_perm_b32 v63, v159, v155, s70\n"
  "v_mfma_f32_16x16x32_bf16 v[248:251], v[56:59], a[72:75], v[248:251]\n"
  "s_xor_b32 s72, s67, 0x2c0000\n"
  "buffer_load_dwordx4 v[128:131], v2, s[44:47], s72 offen\n"
  "v_mfma_f32_16x16x32_bf16 a[24:27], v[56:59], a[76:79], a[24:27]\n"
  "buffer_load_dwordx4 v[132:135], v3, s[44:47], s72 offen\n"
  "v_mfma_f32_16x16x32_bf16 a[56:59], v[56:59], a[80:83], a[56:59]\n"
  "s_waitcnt vmcnt(10)\n"
  "buffer_load_dwordx4 v[136:139], v4, s[44:47], s72 offen\n"
  "v_mfma_f32_16x16x32_bf16 v[188:191], v[60:63], a[64:67], v[188:191]\n"
  "v_perm_b32 v56, v68, v64, s69\n"
  "v_perm_b32 v57, v76, v72, s69\n"
  "buffer_load_dwordx4 v[140:143], v5, s[44:47], s72 offen\n"
  "v_mfma_f32_16x16x32_bf16 v[220:223], v[60:63], a[68:71], v[220:223]\n"
  "v_perm_b32 v58, v84, v80, s69\n"
  "v_perm_b32 v59, v92, v88, s69\n"
  "buffer_load_dwordx4 v[144:147], v6, s[44:47], s72 offen\n"
  "v_mfma_f32_16x16x32_bf16 v[252:255], v[60:63], a[72:75], v[252:255]\n"
  "buffer_load_dwordx4 v[148:151], v7, s[44:47], s72 offen\n"
  "v_mfma_f32_16x16x32_bf16 a[28:31], v[60:63], a[76:79], a[28:31]\n"
  "s_waitcnt lgkmcnt(0)\n"
  "buffer_load_dwordx4 v[152:155], v8, s[44:47], s72 offen\n"
  "v_mfma_f32_16x16x32_bf16 a[60:63], v[60:63], a[80:83], a[60:63]\n"
  "buffer_load_dwordx4 v[156:159], v9, s[44:47], s72 offen\n"
  "v_mfma_f32_16x16x32_bf16 v[160:163], v[56:59], a[84:87], v[160:163]\n"
  "v_perm_b32 v60, v68, v64, s70\n"
  "v_perm_b32 v61, v76, v72, s70\n"
  "v_mfma_f32_16x16x32_bf16 v[192:195], v[56:59], a[88:91], v[192:195]\n"
  "v_perm_b32 v62, v84, v80, s70\n"
  "v_perm_b32 v63, v92, v88, s70\n"
  "v_mfma_f32_16x16x32_bf16 v[224:227], v[56:59], a[92:95], v[224:227]\n"
  "buffer_load_dwordx4 v14, s[36:39], s94 offen offset:1024 sc1 nt lds\n"
  "v_mfma_f32_16x16x32_bf16 a[0:3], v[56:59], a[96:99], a[0:3]\n"
  "v_xor_b32 v12, 0xa080, v11\n"
  "v_mfma_f32_16x16x32_bf16 a[32:35], v[56:59], a[100:103], a[32:35]\n"
  "buffer_load_dwordx4 v15, s[36:39], s94 offen offset:2048 sc1 nt lds\n"
  "v_mfma_f32_16x16x32_bf16 v[164:167], v[60:63], a[84:87], v[164:167]\n"
  "v_perm_b32 v56, v69, v65, s69\n"
  "v_perm_b32 v57, v77, v73, s69\n"
  "buffer_load_dwordx4 v16, s[36:39], s94 offen offset:3072 sc1 nt lds\n"
  "v_mfma_f32_16x16x32_bf16 v[196:199], v[60:63], a[88:91], v[196:199]\n"
  "v_perm_b32 v58, v85, v81, s69\n"
  "v_perm_b32 v59, v93, v89, s69\n"
  "v_mfma_f32_16x16x32_bf16 v[228:231], v[60:63], a[92:95], v[228:231]\n"
  "s_add_u32 m0, s77, 0x4000\n"
  "s_nop 0\n"
  "buffer_load_dwordx4 v13, s[36:39], s95 offen sc1 nt lds\n"
  "v_mfma_f32_16x16x32_bf16 a[4:7], v[60:63], a[96:99], a[4:7]\n"
  "ds_read_b128 a[64:67], v12\n"
  "v_mfma_f32_16x16x32_bf16 a[36:39], v[60:63], a[100:103], a[36:39]\n"
  "buffer_load_dwordx4 v14, s[36:39], s95 offen offset:1024 sc1 nt lds\n"
  "v_mfma_f32_16x16x32_bf16 v[168:171], v[56:59], a[84:87], v[168:171]\n"
  "v_perm_b32 v60, v69, v65, s70\n"
  "v_perm_b32 v61, v77, v73, s70\n"
  "buffer_load_dwordx4 v15, s[36:39], s95 offen offset:2048 sc1 nt lds\n"
  "v_mfma_f32_16x16x32_bf16 v[200:203], v[56:59], a[88:91], v[200:203]\n"
  "v_perm_b32 v62, v85, v81, s70\n"
  "v_perm_b32 v63, v93, v89, s70\n"
  "v_mfma_f32_16x16x32_bf16 v[232:235], v[56:59], a[92:95], v[232:235]\n"
  "buffer_load_dwordx4 v16, s[36:39], s95 offen offset:3072 sc1 nt lds\n"
  "v_mfma_f32_16x16x32_bf16 a[8:11], v[56:59], a[96:99], a[8:11]\n"
  "ds_read_b128 a[68:71], v12 offset:4096\n"
  "v_mfma_f32_16x16x32_bf16 a[40:43], v[56:59], a[100:103], a[40:43]\n"
  "s_add_u32 m0, s76, 0\n"
  "s_nop 0\n"
  "buffer_load_dwordx4 v17, s[40:43], 0 offen sc1 nt lds\n"
  "v_mfma_f32_16x16x32_bf16 v[172:175], v[60:63], a[84:87], v[172:175]\n"
  "v_perm_b32 v56, v70, v66, s69\n"
  "v_perm_b32 v57, v78, v74, s69\n"
  "v_mfma_f32_16x16x32_bf16 v[204:207], v[60:63], a[88:91], v[204:207]\n"
  "v_perm_b32 v58, v86, v82, s69\n"
  "v_perm_b32 v59, v94, v90, s69\n"
  "v_mfma_f32_16x16x32_bf16 v[236:239], v[60:63], a[92:95], v[236:239]\n"
  "v_mfma_f32_16x16x32_bf16 a[12:15], v[60:63], a[96:99], a[12:15]\n"
  "ds_read_b128 a[72:75], v12 offset:8192\n"
  "v_mfma_f32_16x16x32_bf16 a[44:47], v[60:63], a[100:103], a[44:47]\n"
  "v_mfma_f32_16x16x32_bf16 v[176:179], v[56:59], a[84:87], v[176:179]\n"
  "v_perm_b32 v60, v70, v66, s70\n"
  "v_perm_b32 v61, v78, v74, s70\n"
  "v_mfma_f32_16x16x32_bf16 v[208:211], v[56:59], a[88:91], v[208:211]\n"
  "v_perm_b32 v62, v86, v82, s70\n"
  "v_perm_b32 v63, v94, v90, s70\n"
  "v_mfma_f32_16x16x32_bf16 v[240:243], v[56:59], a[92:95], v[240:243]\n"
  "v_mfma_f32_16x16x32_bf16 a[16:19], v[56:59], a[96:99], a[16:19]\n"
  "ds_read_b128 a[76:79], v12 offset:12288\n"
  "v_mfma_f32_16x16x32_bf16 a[48:51], v[56:59], a[100:103], a[48:51]\n"
  "v_mfma_f32_16x16x32_bf16 v[180:183], v[60:63], a[84:87], v[180:183]\n"
  "v_perm_b32 v56, v71, v67, s69\n"
  "v_perm_b32 v57, v79, v75, s69\n"
  "v_mfma_f32_16x16x32_bf16 v[212:215], v[60:63], a[88:91], v[212:215]\n"
  "v_perm_b32 v58, v87, v83, s69\n"
  "v_perm_b32 v59, v95, v91, s69\n"
  "v_mfma_f32_16x16x32_bf16 v[244:247], v[60:63], a[92:95], v[244:247]\n"
  "v_mfma_f32_16x16x32_bf16 a[20:23], v[60:63], a[96:99], a[20:23]\n"
  "ds_read_b128 a[80:83], v12 offset:16384\n"
  "v_mfma_f32_16x16x32_bf16 a[52:55], v[60:63], a[100:103], a[52:55]\n"
  "v_mfma_f32_16x16x32_bf16 v[184:187], v[56:59], a[84:87], v[184:187]\n"
  "v_perm_b32 v60, v71, v67, s70\n"
  "v_perm_b32 v61, v79, v75, s70\n"
  "v_mfma_f32_16x16x32_bf16 v[216:219], v[56:59], a[88:91], v[216:219]\n"
  "v_perm_b32 v62, v87, v83, s70\n"
  "v_perm_b32 v63, v95, v91, s70\n"
  "v_mfma_f32_16x16x32_bf16 v[248:251], v[56:59], a[92:95], v[248:251]\n"
  "v_mfma_f32_16x16x32_bf16 a[24:27], v[56:59], a[96:99], a[24:27]\n"
  "v_mfma_f32_16x16x32_bf16 a[56:59], v[56:59], a[100:103], a[56:59]\n"
  "s_waitcnt vmcnt(16)\n"
  "v_mfma_f32_16x16x32_bf16 v[188:191], v[60:63], a[84:87], v[188:191]\n"
  "v_perm_b32 v56, v100, v96, s69\n"
  "v_perm_b32 v57, v108, v104, s69\n"
  "v_mfma_f32_16x16x32_bf16 v[220:223], v[60:63], a[88:91], v[220:223]\n"
  "v_perm_b32 v58, v116, v112, s69\n"
  "v_perm_b32 v59, v124, v120, s69\n"
  "v_mfma_f32_16x16x32_bf16 v[252:255], v[60:63], a[92:95], v[252:255]\n"
  "v_mfma_f32_16x16x32_bf16 a[28:31], v[60:63], a[96:99], a[28:31]\n"
  "s_waitcnt lgkmcnt(0)\n"
  "v_mfma_f32_16x16x32_bf16 a[60:63], v[60:63], a[100:103], a[60:63]\n"
  "v_mfma_f32_16x16x32_bf16 v[160:163], v[56:59], a[64:67], v[160:163]\n"
  "v_perm_b32 v60, v100, v96, s70\n"
  "v_perm_b32 v61, v108, v104, s70\n"
  "v_mfma_f32_16x16x32_bf16 v[192:195], v[56:59], a[68:71], v[192:195]\n"
  "v_perm_b32 v62, v116, v112, s70\n"
  "v_perm_b32 v63, v124, v120, s70\n"
  "v_mfma_f32_16x16x32_bf16 v[224:227], v[56:59], a[72:75], v[224:227]\n"
  "v_mfma_f32_16x16x32_bf16 a[0:3], v[56:59], a[76:79], a[0:3]\n"
  "v_xor_b32 v12, 0xa0c0, v11\n"
  "v_mfma_f32_16x16x32_bf16 a[32:35], v[56:59], a[80:83], a[32:35]\n"
  "v_mfma_f32_16x16x32_bf16 v[164:167], v[60:63], a[64:67], v[164:167]\n"
  "v_perm_b32 v56, v101, v97, s69\n"
  "v_perm_b32 v57, v109, v105, s69\n"
  "v_mfma_f32_16x16x32_bf16 v[196:199], v[60:63], a[68:71], v[196:199]\n"
  "v_perm_b32 v58, v117, v113, s69\n"
  "v_perm_b32 v59, v125, v121, s69\n"
  "v_mfma_f32_16x16x32_bf16 v[228:231], v[60:63], a[72:75], v[228:231]\n"
  "v_mfma_f32_16x16x32_bf16 a[4:7], v[60:63], a[76:79], a[4:7]\n"
  "ds_read_b128 a[84:87], v12\n"
  "v_mfma_f32_16x16x32_bf16 a[36:39], v[60:63], a[80:83], a[36:39]\n"
  "v_mfma_f32_16x16x32_bf16 v[168:171], v[56:59], a[64:67], v[168:171]\n"
  "v_perm_b32 v60, v101, v97, s70\n"
  "v_perm_b32 v61, v109, v105, s70\n"
  "v_mfma_f32_16x16x32_bf16 v[200:203], v[56:59], a[68:71], v[200:203]\n"
  "v_perm_b32 v62, v117, v113, s70\n"
  "v_perm_b32 v63, v125, v121, s70\n"
  "v_mfma_f32_16x16x32_bf16 v[232:235], v[56:59], a[72:75], v[232:235]\n"
  "v_mfma_f32_16x16x32_bf16 a[8:11], v[56:59], a[76:79], a[8:11]\n"
  "ds_read_b128 a[88:91], v12 offset:4096\n"
  "v_mfma_f32_16x16x32_bf16 a[40:43], v[56:59], a[80:83], a[40:43]\n"
  "v_mfma_f32_16x16x32_bf16 v[172:175], v[60:63], a[64:67], v[172:175]\n"
  "v_perm_b32 v56, v102, v98, s69\n"
  "v_perm_b32 v57, v110, v106, s69\n"
  "v_mfma_f32_16x16x32_bf16 v[204:207], v[60:63], a[68:71], v[204:207]\n"
  "v_perm_b32 v58, v118, v114, s69\n"
  "v_perm_b32 v59, v126, v122, s69\n"
  "v_mfma_f32_16x16x32_bf16 v[236:239], v[60:63], a[72:75], v[236:239]\n"
  "v_mfma_f32_16x16x32_bf16 a[12:15], v[60:63], a[76:79], a[12:15]\n"
  "ds_read_b128 a[92:95], v12 offset:8192\n"
  "v_mfma_f32_16x16x32_bf16 a[44:47], v[60:63], a[80:83], a[44:47]\n"
  "v_mfma_f32_16x16x32_bf16 v[176:179], v[56:59], a[64:67], v[176:179]\n"
  "v_perm_b32 v60, v102, v98, s70\n"
  "v_perm_b32 v61, v110, v106, s70\n"
  "v_mfma_f32_16x16x32_bf16 v[208:211], v[56:59], a[68:71], v[208:211]\n"
  "v_perm_b32 v62, v118, v114, s70\n"
  "v_perm_b32 v63, v126, v122, s70\n"
  "v_mfma_f32_16x16x32_bf16 v[240:243], v[56:59], a[72:75], v[240:243]\n"
  "s_xor_b32 s72, s67, 0x300000\n"
  "buffer_load_dwordx4 v[64:67], v2, s[44:47], s72 offen\n"
  "v_mfma_f32_16x16x32_bf16 a[16:19], v[56:59], a[76:79], a[16:19]\n"
  "ds_read_b128 a[96:99], v12 offset:12288\n"
  "v_mfma_f32_16x16x32_bf16 a[48:51], v[56:59], a[80:83], a[48:51]\n"
  "buffer_load_dwordx4 v[68:71], v3, s[44:47], s72 offen\n"
  "v_mfma_f32_16x16x32_bf16 v[180:183], v[60:63], a[64:67], v[180:183]\n"
  "v_perm_b32 v56, v103, v99, s69\n"
  "v_perm_b32 v57, v111, v107, s69\n"
  "v_mfma_f32_16x16x32_bf16 v[212:215], v[60:63], a[68:71], v[212:215]\n"
  "v_perm_b32 v58, v119, v115, s69\n"
  "v_perm_b32 v59, v127, v123, s69\n"
  "buffer_load_dwordx4 v[72:75], v4, s[44:47], s72 offen\n"
  "v_mfma_f32_16x16x32_bf16 v[244:247], v[60:63], a[72:75], v[244:247]\n"
  "v_mfma_f32_16x16x32_bf16 a[20:23], v[60:63], a[76:79], a[20:23]\n"
  "buffer_load_dwordx4 v[76:79], v5, s[44:47], s72 offen\n"
  "ds_read_b128 a[100:103], v12 offset:16384\n"
  "v_mfma_f32_16x16x32_bf16 a[52:55], v[60:63], a[80:83], a[52:55]\n"
  "v_mfma_f32_16x16x32_bf16 v[184:187], v[56:59], a[64:67], v[184:187]\n"
  "v_perm_b32 v60, v103, v99, s70\n"
  "v_perm_b32 v61, v111, v107, s70\n"
  "buffer_load_dwordx4 v[80:83], v6, s[44:47], s72 offen\n"
  "v_mfma_f32_16x16x32_bf16 v[216:219], v[56:59], a[68:71], v[216:219]\n"
  "v_perm_b32 v62, v119, v115, s70\n"
  "v_perm_b32 v63, v127, v123, s70\n"
  "v_mfma_f32_16x16x32_bf16 v[248:251], v[56:59], a[72:75], v[248:251]\n"
  "buffer_load_dwordx4 v[84:87], v7, s[44:47], s72 offen\n"
  "v_mfma_f32_16x16x32_bf16 a[24:27], v[56:59], a[76:79], a[24:27]\n"
  "v_mfma_f32_16x16x32_bf16 a[56:59], v[56:59], a[80:83], a[56:59]\n"
  "s_waitcnt vmcnt(14)\n"
  "buffer_load_dwordx4 v[88:91], v8, s[44:47], s72 offen\n"
  "v_mfma_f32_16x16x32_bf16 v[188:191], v[60:63], a[64:67], v[188:191]\n"
  "v_perm_b32 v56, v132, v128, s69\n"
  "v_perm_b32 v57, v140, v136, s69\n"
  "v_mfma_f32_16x16x32_bf16 v[220:223], v[60:63], a[68:71], v[220:223]\n"
  "v_perm_b32 v58, v148, v144, s69\n"
  "v_perm_b32 v59, v156, v152, s69\n"
  "buffer_load_dwordx4 v[92:95], v9, s[44:47], s72 offen\n"
  "v_mfma_f32_16x16x32_bf16 v[252:255], v[60:63], a[72:75], v[252:255]\n"
  "v_mfma_f32_16x16x32_bf16 a[28:31], v[60:63], a[76:79], a[28:31]\n"
  "s_waitcnt lgkmcnt(0)\n"
  "v_mfma_f32_16x16x32_bf16 a[60:63], v[60:63], a[80:83], a[60:63]\n"
  "v_mfma_f32_16x16x32_bf16 v[160:163], v[56:59], a[84:87], v[160:163]\n"
  "v_perm_b32 v60, v132, v128, s70\n"
  "v_perm_b32 v61, v140, v136, s70\n"
  "s_xor_b32 s72, s67, 0x340000\n"
  "buffer_load_dwordx4 v[96:99], v2, s[44:47], s72 offen\n"
  "v_mfma_f32_16x16x32_bf16 v[192:195], v[56:59], a[88:91], v[192:195]\n"
  "v_perm_b32 v62, v148, v144, s70\n"
  "v_perm_b32 v63, v156, v152, s70\n"
  "v_mfma_f32_16x16x32_bf16 v[224:227], v[56:59], a[92:95], v[224:227]\n"
  "buffer_load_dwordx4 v[100:103], v3, s[44:47], s72 offen\n"
  "v_mfma_f32_16x16x32_bf16 a[0:3], v[56:59], a[96:99], a[0:3]\n"
  "v_xor_b32 v12, 0xf000, v11\n"
  "v_mfma_f32_16x16x32_bf16 a[32:35], v[56:59], a[100:103], a[32:35]\n"
  "buffer_load_dwordx4 v[104:107], v4, s[44:47], s72 offen\n"
  "s_barrier\n"
  "v_mfma_f32_16x16x32_bf16 v[164:167], v[60:63], a[84:87], v[164:167]\n"
  "v_perm_b32 v56, v133, v129, s69\n"
  "v_perm_b32 v57, v141, v137, s69\n"
  "v_mfma_f32_16x16x32_bf16 v[196:199], v[60:63], a[88:91], v[196:199]\n"
  "v_perm_b32 v58, v149, v145, s69\n"
  "v_perm_b32 v59, v157, v153, s69\n"
  "buffer_load_dwordx4 v[108:111], v5, s[44:47], s72 offen\n"
  "v_mfma_f32_16x16x32_bf16 v[228:231], v[60:63], a[92:95], v[228:231]\n"
  "v_mfma_f32_16x16x32_bf16 a[4:7], v[60:63], a[96:99], a[4:7]\n"
  "buffer_load_dwordx4 v[112:115], v6, s[44:47], s72 offen\n"
  "ds_read_b128 a[64:67], v12\n"
  "v_mfma_f32_16x16x32_bf16 a[36:39], v[60:63], a[100:103], a[36:39]\n"
  "v_mfma_f32_16x16x32_bf16 v[168:171], v[56:59], a[84:87], v[168:171]\n"
  "v_perm_b32 v60, v133, v129, s70\n"
  "v_perm_b32 v61, v141, v137, s70\n"
  "buffer_load_dwordx4 v[116:119], v7, s[44:47], s72 offen\n"
  "v_mfma_f32_16x16x32_bf16 v[200:203], v[56:59], a[88:91], v[200:203]\n"
  "v_perm_b32 v62, v149, v145, s70\n"
  "v_perm_b32 v63, v157, v153, s70\n"
  "v_mfma_f32_16x16x32_bf16 v[232:235], v[56:59], a[92:95], v[232:235]\n"
  "buffer_load_dwordx4 v[120:123], v8, s[44:47], s72 offen\n"
  "v_mfma_f32_16x16x32_bf16 a[8:11], v[56:59], a[96:99], a[8:11]\n"
  "ds_read_b128 a[68:71], v12 offset:4096\n"
  "v_mfma_f32_16x16x32_bf16 a[40:43], v[56:59], a[100:103], a[40:43]\n"
  "buffer_load_dwordx4 v[124:127], v9, s[44:47], s72 offen\n"
  "v_mfma_f32_16x16x32_bf16 v[172:175], v[60:63], a[84:87], v[172:175]\n"
  "v_perm_b32 v56, v134, v130, s69\n"
  "v_perm_b32 v57, v142, v138, s69\n"
  "v_mfma_f32_16x16x32_bf16 v[204:207], v[60:63], a[88:91], v[204:207]\n"
  "v_perm_b32 v58, v150, v146, s69\n"
  "v_perm_b32 v59, v158, v154, s69\n"
  "v_mfma_f32_16x16x32_bf16 v[236:239], v[60:63], a[92:95], v[236:239]\n"
  "v_mfma_f32_16x16x32_bf16 a[12:15], v[60:63], a[96:99], a[12:15]\n"
  "ds_read_b128 a[72:75], v12 offset:8192\n"
  "v_mfma_f32_16x16x32_bf16 a[44:47], v[60:63], a[100:103], a[44:47]\n"
  "v_mfma_f32_16x16x32_bf16 v[176:179], v[56:59], a[84:87], v[176:179]\n"
  "v_perm_b32 v60, v134, v130, s70\n"
  "v_perm_b32 v61, v142, v138, s70\n"
  "v_mfma_f32_16x16x32_bf16 v[208:211], v[56:59], a[88:91], v[208:211]\n"
  "v_perm_b32 v62, v150, v146, s70\n"
  "v_perm_b32 v63, v158, v154, s70\n"
  "v_mfma_f32_16x16x32_bf16 v[240:243], v[56:59], a[92:95], v[240:243]\n"
  "v_mfma_f32_16x16x32_bf16 a[16:19], v[56:59], a[96:99], a[16:19]\n"
  "ds_read_b128 a[76:79], v12 offset:12288\n"
  "v_mfma_f32_16x16x32_bf16 a[48:51], v[56:59], a[100:103], a[48:51]\n"
  "v_mfma_f32_16x16x32_bf16 v[180:183], v[60:63], a[84:87], v[180:183]\n"
  "v_perm_b32 v56, v135, v131, s69\n"
  "v_perm_b32 v57, v143, v139, s69\n"
  "v_mfma_f32_16x16x32_bf16 v[212:215], v[60:63], a[88:91], v[212:215]\n"
  "v_perm_b32 v58, v151, v147, s69\n"
  "v_perm_b32 v59, v159, v155, s69\n"
  "v_mfma_f32_16x16x32_bf16 v[244:247], v[60:63], a[92:95], v[244:247]\n"
  "v_mfma_f32_16x16x32_bf16 a[20:23], v[60:63], a[96:99], a[20:23]\n"
  "ds_read_b128 a[80:83], v12 offset:16384\n"
  "v_mfma_f32_16x16x32_bf16 a[52:55], v[60:63], a[100:103], a[52:55]\n"
  "v_mfma_f32_16x16x32_bf16 v[184:187], v[56:59], a[84:87], v[184:187]\n"
  "v_perm_b32 v60, v135, v131, s70\n"
  "v_perm_b32 v61, v143, v139, s70\n"
  "v_mfma_f32_16x16x32_bf16 v[216:219], v[56:59], a[88:91], v[216:219]\n"
  "v_perm_b32 v62, v151, v147, s70\n"
  "v_perm_b32 v63, v159, v155, s70\n"
  "v_mfma_f32_16x16x32_bf16 v[248:251], v[56:59], a[92:95], v[248:251]\n"
  "s_xor_b32 s72, s67, 0x380000\n"
  "buffer_load_dwordx4 v[128:131], v2, s[44:47], s72 offen\n"
  "v_mfma_f32_16x16x32_bf16 a[24:27], v[56:59], a[96:99], a[24:27]\n"
  "buffer_load_dwordx4 v[132:135], v3, s[44:47], s72 offen\n"
  "v_mfma_f32_16x16x32_bf16 a[56:59], v[56:59], a[100:103], a[56:59]\n"
  "s_waitcnt vmcnt(10)\n"
  "buffer_load_dwordx4 v[136:139], v4, s[44:47], s72 offen\n"
  "v_mfma_f32_16x16x32_bf16 v[188:191], v[60:63], a[84:87], v[188:191]\n"
  "v_perm_b32 v56, v68, v64, s69\n"
  "v_perm_b32 v57, v76, v72, s69\n"
  "buffer_load_dwordx4 v[140:143], v5, s[44:47], s72 offen\n"
  "v_mfma_f32_16x16x32_bf16 v[220:223], v[60:63], a[88:91], v[220:223]\n"
  "v_perm_b32 v58, v84, v80, s69\n"
  "v_perm_b32 v59, v92, v88, s69\n"
  "buffer_load_dwordx4 v[144:147], v6, s[44:47], s72 offen\n"
  "v_mfma_f32_16x16x32_bf16 v[252:255], v[60:63], a[92:95], v[252:255]\n"
  "buffer_load_dwordx4 v[148:151], v7, s[44:47], s72 offen\n"
  "v_mfma_f32_16x16x32_bf16 a[28:31], v[60:63], a[96:99], a[28:31]\n"
  "s_waitcnt lgkmcnt(0)\n"
  "buffer_load_dwordx4 v[152:155], v8, s[44:47], s72 offen\n"
  "v_mfma_f32_16x16x32_bf16 a[60:63], v[60:63], a[100:103], a[60:63]\n"
  "buffer_load_dwordx4 v[156:159], v9, s[44:47], s72 offen\n"
  "v_mfma_f32_16x16x32_bf16 v[160:163], v[56:59], a[64:67], v[160:163]\n"
  "v_perm_b32 v60, v68, v64, s70\n"
  "v_perm_b32 v61, v76, v72, s70\n"
  "v_mfma_f32_16x16x32_bf16 v[192:195], v[56:59], a[68:71], v[192:195]\n"
  "v_perm_b32 v62, v84, v80, s70\n"
  "v_perm_b32 v63, v92, v88, s70\n"
  "v_mfma_f32_16x16x32_bf16 v[224:227], v[56:59], a[72:75], v[224:227]\n"
  "buffer_load_dwordx4 v18, s[40:43], 0 offen offset:1024 sc1 nt lds\n"
  "v_mfma_f32_16x16x32_bf16 a[0:3], v[56:59], a[76:79], a[0:3]\n"
  "v_xor_b32 v12, 0xf040, v11\n"
  "v_mfma_f32_16x16x32_bf16 a[32:35], v[56:59], a[80:83], a[32:35]\n"
  "buffer_load_dwordx4 v17, s[40:43], 0 offen offset:2048 sc1 nt lds\n"
  "v_mfma_f32_16x16x32_bf16 v[164:167], v[60:63], a[64:67], v[164:167]\n"
  "v_perm_b32 v56, v69, v65, s69\n"
  "v_perm_b32 v57, v77, v73, s69\n"
  "buffer_load_dwordx4 v18, s[40:43], 0 offen offset:3072 sc1 nt lds\n"
  "v_mfma_f32_16x16x32_bf16 v[196:199], v[60:63], a[68:71], v[196:199]\n"
  "v_perm_b32 v58, v85, v81, s69\n"
  "v_perm_b32 v59, v93, v89, s69\n"
  "v_mfma_f32_16x16x32_bf16 v[228:231], v[60:63], a[72:75], v[228:231]\n"
  "s_add_u32 m0, s76, 0x4000\n"
  "s_nop 0\n"
  "buffer_load_dwordx4 v17, s[40:43], s92 offen sc1 nt lds\n"
  "v_mfma_f32_16x16x32_bf16 a[4:7], v[60:63], a[76:79], a[4:7]\n"
  "ds_read_b128 a[84:87], v12\n"
  "v_mfma_f32_16x16x32_bf16 a[36:39], v[60:63], a[80:83], a[36:39]\n"
  "buffer_load_dwordx4 v18, s[40:43], s92 offen offset:1024 sc1 nt lds\n"
  "v_mfma_f32_16x16x32_bf16 v[168:171], v[56:59], a[64:67], v[168:171]\n"
  "v_perm_b32 v60, v69, v65, s70\n"
  "v_perm_b32 v61, v77, v73, s70\n"
  "buffer_load_dwordx4 v17, s[40:43], s92 offen offset:2048 sc1 nt lds\n"
  "v_mfma_f32_16x16x32_bf16 v[200:203], v[56:59], a[68:71], v[200:203]\n"
  "v_perm_b32 v62, v85, v81, s70\n"
  "v_perm_b32 v63, v93, v89, s70\n"
  "v_mfma_f32_16x16x32_bf16 v[232:235], v[56:59], a[72:75], v[232:235]\n"
  "buffer_load_dwordx4 v18, s[40:43], s92 offen offset:3072 sc1 nt lds\n"
  "v_mfma_f32_16x16x32_bf16 a[8:11], v[56:59], a[76:79], a[8:11]\n"
  "ds_read_b128 a[88:91], v12 offset:4096\n"
  "v_mfma_f32_16x16x32_bf16 a[40:43], v[56:59], a[80:83], a[40:43]\n"
  "v_mfma_f32_16x16x32_bf16 v[172:175], v[60:63], a[64:67], v[172:175]\n"
  "v_perm_b32 v56, v70, v66, s69\n"
  "v_perm_b32 v57, v78, v74, s69\n"
  "v_mfma_f32_16x16x32_bf16 v[204:207], v[60:63], a[68:71], v[204:207]\n"
  "v_perm_b32 v58, v86, v82, s69\n"
  "v_perm_b32 v59, v94, v90, s69\n"
  "v_mfma_f32_16x16x32_bf16 v[236:239], v[60:63], a[72:75], v[236:239]\n"
  "v_mfma_f32_16x16x32_bf16 a[12:15], v[60:63], a[76:79], a[12:15]\n"
  "ds_read_b128 a[92:95], v12 offset:8192\n"
  "v_mfma_f32_16x16x32_bf16 a[44:47], v[60:63], a[80:83], a[44:47]\n"
  "v_mfma_f32_16x16x32_bf16 v[176:179], v[56:59], a[64:67], v[176:179]\n"
  "v_perm_b32 v60, v70, v66, s70\n"
  "v_perm_b32 v61, v78, v74, s70\n"
  "v_mfma_f32_16x16x32_bf16 v[208:211], v[56:59], a[68:71], v[208:211]\n"
  "v_perm_b32 v62, v86, v82, s70\n"
  "v_perm_b32 v63, v94, v90, s70\n"
  "v_mfma_f32_16x16x32_bf16 v[240:243], v[56:59], a[72:75], v[240:243]\n"
  "v_mfma_f32_16x16x32_bf16 a[16:19], v[56:59], a[76:79], a[16:19]\n"
  "ds_read_b128 a[96:99], v12 offset:12288\n"
  "v_mfma_f32_16x16x32_bf16 a[48:51], v[56:59], a[80:83], a[48:51]\n"
  "v_mfma_f32_16x16x32_bf16 v[180:183], v[60:63], a[64:67], v[180:183]\n"
  "v_perm_b32 v56, v71, v67, s69\n"
  "v_perm_b32 v57, v79, v75, s69\n"
  "v_mfma_f32_16x16x32_bf16 v[212:215], v[60:63], a[68:71], v[212:215]\n"
  "v_perm_b32 v58, v87, v83, s69\n"
  "v_perm_b32 v59, v95, v91, s69\n"
  "v_mfma_f32_16x16x32_bf16 v[244:247], v[60:63], a[72:75], v[244:247]\n"
  "v_mfma_f32_16x16x32_bf16 a[20:23], v[60:63], a[76:79], a[20:23]\n"
  "ds_read_b128 a[100:103], v12 offset:16384\n"
  "v_mfma_f32_16x16x32_bf16 a[52:55], v[60:63], a[80:83], a[52:55]\n"
  "v_mfma_f32_16x16x32_bf16 v[184:187], v[56:59], a[64:67], v[184:187]\n"
  "v_perm_b32 v60, v71, v67, s70\n"
  "v_perm_b32 v61, v79, v75, s70\n"
  "v_mfma_f32_16x16x32_bf16 v[216:219], v[56:59], a[68:71], v[216:219]\n"
  "v_perm_b32 v62, v87, v83, s70\n"
  "v_perm_b32 v63, v95, v91, s70\n"
  "v_mfma_f32_16x16x32_bf16 v[248:251], v[56:59], a[72:75], v[248:251]\n"
  "v_mfma_f32_16x16x32_bf16 a[24:27], v[56:59], a[76:79], a[24:27]\n"
  "v_mfma_f32_16x16x32_bf16 a[56:59], v[56:59], a[80:83], a[56:59]\n"
  "s_waitcnt vmcnt(15)\n"
  "v_mfma_f32_16x16x32_bf16 v[188:191], v[60:63], a[64:67], v[188:191]\n"
  "v_perm_b32 v56, v100, v96, s69\n"
  "v_perm_b32 v57, v108, v104, s69\n"
  "v_mfma_f32_16x16x32_bf16 v[220:223], v[60:63], a[68:71], v[220:223]\n"
  "v_perm_b32 v58, v116, v112, s69\n"
  "v_perm_b32 v59, v124, v120, s69\n"
  "v_mfma_f32_16x16x32_bf16 v[252:255], v[60:63], a[72:75], v[252:255]\n"
  "v_mfma_f32_16x16x32_bf16 a[28:31], v[60:63], a[76:79], a[28:31]\n"
  "s_waitcnt lgkmcnt(0)\n"
  "v_mfma_f32_16x16x32_bf16 a[60:63], v[60:63], a[80:83], a[60:63]\n"
  "v_mfma_f32_16x16x32_bf16 v[160:163], v[56:59], a[84:87], v[160:163]\n"
  "v_perm_b32 v60, v100, v96, s70\n"
  "v_perm_b32 v61, v108, v104, s70\n"
  "v_mfma_f32_16x16x32_bf16 v[192:195], v[56:59], a[88:91], v[192:195]\n"
  "v_perm_b32 v62, v116, v112, s70\n"
  "v_perm_b32 v63, v124, v120, s70\n"
  "v_mfma_f32_16x16x32_bf16 v[224:227], v[56:59], a[92:95], v[224:227]\n"
  "v_mfma_f32_16x16x32_bf16 a[0:3], v[56:59], a[96:99], a[0:3]\n"
  "v_xor_b32 v12, 0xf080, v11\n"
  "v_mfma_f32_16x16x32_bf16 a[32:35], v[56:59], a[100:103], a[32:35]\n"
  "v_mfma_f32_16x16x32_bf16 v[164:167], v[60:63], a[84:87], v[164:167]\n"
  "v_perm_b32 v56, v101, v97, s69\n"
  "v_perm_b32 v57, v109, v105, s69\n"
  "v_mfma_f32_16x16x32_bf16 v[196:199], v[60:63], a[88:91], v[196:199]\n"
  "v_perm_b32 v58, v117, v113, s69\n"
  "v_perm_b32 v59, v125, v121, s69\n"
  "v_mfma_f32_16x16x32_bf16 v[228:231], v[60:63], a[92:95], v[228:231]\n"
  "v_mfma_f32_16x16x32_bf16 a[4:7], v[60:63], a[96:99], a[4:7]\n"
  "ds_read_b128 a[64:67], v12\n"
  "v_mfma_f32_16x16x32_bf16 a[36:39], v[60:63], a[100:103], a[36:39]\n"
  "v_mfma_f32_16x16x32_bf16 v[168:171], v[56:59], a[84:87], v[168:171]\n"
  "v_perm_b32 v60, v101, v97, s70\n"
  "v_perm_b32 v61, v109, v105, s70\n"
  "v_mfma_f32_16x16x32_bf16 v[200:203], v[56:59], a[88:91], v[200:203]\n"
  "v_perm_b32 v62, v117, v113, s70\n"
  "v_perm_b32 v63, v125, v121, s70\n"
  "v_mfma_f32_16x16x32_bf16 v[232:235], v[56:59], a[92:95], v[232:235]\n"
  "v_mfma_f32_16x16x32_bf16 a[8:11], v[56:59], a[96:99], a[8:11]\n"
  "ds_read_b128 a[68:71], v12 offset:4096\n"
  "v_mfma_f32_16x16x32_bf16 a[40:43], v[56:59], a[100:103], a[40:43]\n"
  "v_mfma_f32_16x16x32_bf16 v[172:175], v[60:63], a[84:87], v[172:175]\n"
  "v_perm_b32 v56, v102, v98, s69\n"
  "v_perm_b32 v57, v110, v106, s69\n"
  "v_mfma_f32_16x16x32_bf16 v[204:207], v[60:63], a[88:91], v[204:207]\n"
  "v_perm_b32 v58, v118, v114, s69\n"
  "v_perm_b32 v59, v126, v122, s69\n"
  "v_mfma_f32_16x16x32_bf16 v[236:239], v[60:63], a[92:95], v[236:239]\n"
  "v_mfma_f32_16x16x32_bf16 a[12:15], v[60:63], a[96:99], a[12:15]\n"
  "ds_read_b128 a[72:75], v12 offset:8192\n"
  "v_mfma_f32_16x16x32_bf16 a[44:47], v[60:63], a[100:103], a[44:47]\n"
  "v_mfma_f32_16x16x32_bf16 v[176:179], v[56:59], a[84:87], v[176:179]\n"
  "v_perm_b32 v60, v102, v98, s70\n"
  "v_perm_b32 v61, v110, v106, s70\n"
  "v_mfma_f32_16x16x32_bf16 v[208:211], v[56:59], a[88:91], v[208:211]\n"
  "v_perm_b32 v62, v118, v114, s70\n"
  "v_perm_b32 v63, v126, v122, s70\n"
  "v_mfma_f32_16x16x32_bf16 v[240:243], v[56:59], a[92:95], v[240:243]\n"
  "s_xor_b32 s72, s67, 0x3c0000\n"
  "buffer_load_dwordx4 v[64:67], v2, s[44:47], s72 offen\n"
  "v_mfma_f32_16x16x32_bf16 a[16:19], v[56:59], a[96:99], a[16:19]\n"
  "ds_read_b128 a[76:79], v12 offset:12288\n"
  "v_mfma_f32_16x16x32_bf16 a[48:51], v[56:59], a[100:103], a[48:51]\n"
  "buffer_load_dwordx4 v[68:71], v3, s[44:47], s72 offen\n"
  "v_mfma_f32_16x16x32_bf16 v[180:183], v[60:63], a[84:87], v[180:183]\n"
  "v_perm_b32 v56, v103, v99, s69\n"
  "v_perm_b32 v57, v111, v107, s69\n"
  "v_mfma_f32_16x16x32_bf16 v[212:215], v[60:63], a[88:91], v[212:215]\n"
  "v_perm_b32 v58, v119, v115, s69\n"
  "v_perm_b32 v59, v127, v123, s69\n"
  "buffer_load_dwordx4 v[72:75], v4, s[44:47], s72 offen\n"
  "v_mfma_f32_16x16x32_bf16 v[244:247], v[60:63], a[92:95], v[244:247]\n"
  "v_mfma_f32_16x16x32_bf16 a[20:23], v[60:63], a[96:99], a[20:23]\n"
  "buffer_load_dwordx4 v[76:79], v5, s[44:47], s72 offen\n"
  "ds_read_b128 a[80:83], v12 offset:16384\n"
  "v_mfma_f32_16x16x32_bf16 a[52:55], v[60:63], a[100:103], a[52:55]\n"
  "v_mfma_f32_16x16x32_bf16 v[184:187], v[56:59], a[84:87], v[184:187]\n"
  "v_perm_b32 v60, v103, v99, s70\n"
  "v_perm_b32 v61, v111, v107, s70\n"
  "buffer_load_dwordx4 v[80:83], v6, s[44:47], s72 offen\n"
  "v_mfma_f32_16x16x32_bf16 v[216:219], v[56:59], a[88:91], v[216:219]\n"
  "v_perm_b32 v62, v119, v115, s70\n"
  "v_perm_b32 v63, v127, v123, s70\n"
  "v_mfma_f32_16x16x32_bf16 v[248:251], v[56:59], a[92:95], v[248:251]\n"
  "buffer_load_dwordx4 v[84:87], v7, s[44:47], s72 offen\n"
  "v_mfma_f32_16x16x32_bf16 a[24:27], v[56:59], a[96:99], a[24:27]\n"
  "v_xor_b32 v49, 16, v19\n"
  "v_xor_b32 v50, 32, v19\n"
  "v_mfma_f32_16x16x32_bf16 a[56:59], v[56:59], a[100:103], a[56:59]\n"
  "s_waitcnt vmcnt(13)\n"
  "buffer_load_dwordx4 v[88:91], v8, s[44:47], s72 offen\n"
  "v_mfma_f32_16x16x32_bf16 v[188:191], v[60:63], a[84:87], v[188:191]\n"
  "v_perm_b32 v56, v132, v128, s69\n"
  "v_perm_b32 v57, v140, v136, s69\n"
  "v_mfma_f32_16x16x32_bf16 v[220:223], v[60:63], a[88:91], v[220:223]\n"
  "v_perm_b32 v58, v148, v144, s69\n"
  "v_perm_b32 v59, v156, v152, s69\n"
  "buffer_load_dwordx4 v[92:95], v9, s[44:47], s72 offen\n"
  "v_mfma_f32_16x16x32_bf16 v[252:255], v[60:63], a[92:95], v[252:255]\n"
  "v_mfma_f32_16x16x32_bf16 a[28:31], v[60:63], a[96:99], a[28:31]\n"
  "s_waitcnt lgkmcnt(0)\n"
  "v_mfma_f32_16x16x32_bf16 a[60:63], v[60:63], a[100:103], a[60:63]\n"
  "v_xor_b32 v51, 48, v19\n"
  "v_mfma_f32_16x16x32_bf16 v[160:163], v[56:59], a[64:67], v[160:163]\n"
  "v_perm_b32 v60, v132, v128, s70\n"
  "v_perm_b32 v61, v140, v136, s70\n"
  "v_mfma_f32_16x16x32_bf16 v[192:195], v[56:59], a[68:71], v[192:195]\n"
  "v_perm_b32 v62, v148, v144, s70\n"
  "v_perm_b32 v63, v156, v152, s70\n"
  "v_mfma_f32_16x16x32_bf16 v[224:227], v[56:59], a[72:75], v[224:227]\n"
  "ds_read_b128 a[104:107], v19\n"
  "ds_read_b128 a[108:111], v49\n"
  "v_mfma_f32_16x16x32_bf16 a[0:3], v[56:59], a[76:79], a[0:3]\n"
  "v_xor_b32 v12, 0xf0c0, v11\n"
  "v_mfma_f32_16x16x32_bf16 a[32:35], v[56:59], a[80:83], a[32:35]\n"
  "v_mfma_f32_16x16x32_bf16 v[164:167], v[60:63], a[64:67], v[164:167]\n"
  "v_perm_b32 v56, v133, v129, s69\n"
  "v_perm_b32 v57, v141, v137, s69\n"
  "v_mfma_f32_16x16x32_bf16 v[196:199], v[60:63], a[68:71], v[196:199]\n"
  "v_perm_b32 v58, v149, v145, s69\n"
  "v_perm_b32 v59, v157, v153, s69\n"
  "v_mfma_f32_16x16x32_bf16 v[228:231], v[60:63], a[72:75], v[228:231]\n"
  "ds_read_b128 a[112:115], v50\n"
  "ds_read_b128 a[116:119], v51\n"
  "v_mfma_f32_16x16x32_bf16 a[4:7], v[60:63], a[76:79], a[4:7]\n"
  "ds_read_b128 a[84:87], v12\n"
  "v_mfma_f32_16x16x32_bf16 a[36:39], v[60:63], a[80:83], a[36:39]\n"
  "v_mfma_f32_16x16x32_bf16 v[168:171], v[56:59], a[64:67], v[168:171]\n"
  "v_perm_b32 v60, v133, v129, s70\n"
  "v_perm_b32 v61, v141, v137, s70\n"
  "v_mfma_f32_16x16x32_bf16 v[200:203], v[56:59], a[68:71], v[200:203]\n"
  "v_perm_b32 v62, v149, v145, s70\n"
  "v_perm_b32 v63, v157, v153, s70\n"
  "v_mfma_f32_16x16x32_bf16 v[232:235], v[56:59], a[72:75], v[232:235]\n"
  "ds_read_b128 a[120:123], v19 offset:4096\n"
  "ds_read_b128 a[124:127], v49 offset:4096\n"
  "v_mfma_f32_16x16x32_bf16 a[8:11], v[56:59], a[76:79], a[8:11]\n"
  "ds_read_b128 a[88:91], v12 offset:4096\n"
  "v_mfma_f32_16x16x32_bf16 a[40:43], v[56:59], a[80:83], a[40:43]\n"
  "v_mfma_f32_16x16x32_bf16 v[172:175], v[60:63], a[64:67], v[172:175]\n"
  "v_perm_b32 v56, v134, v130, s69\n"
  "v_perm_b32 v57, v142, v138, s69\n"
  "v_mfma_f32_16x16x32_bf16 v[204:207], v[60:63], a[68:71], v[204:207]\n"
  "v_perm_b32 v58, v150, v146, s69\n"
  "v_perm_b32 v59, v158, v154, s69\n"
  "v_mfma_f32_16x16x32_bf16 v[236:239], v[60:63], a[72:75], v[236:239]\n"
  "ds_read_b128 a[128:131], v50 offset:4096\n"
  "ds_read_b128 a[132:135], v51 offset:4096\n"
  "v_mfma_f32_16x16x32_bf16 a[12:15], v[60:63], a[76:79], a[12:15]\n"
  "ds_read_b128 a[92:95], v12 offset:8192\n"
  "v_mfma_f32_16x16x32_bf16 a[44:47], v[60:63], a[80:83], a[44:47]\n"
  "v_mfma_f32_16x16x32_bf16 v[176:179], v[56:59], a[64:67], v[176:179]\n"
  "v_perm_b32 v60, v134, v130, s70\n"
  "v_perm_b32 v61, v142, v138, s70\n"
  "v_mfma_f32_16x16x32_bf16 v[208:211], v[56:59], a[68:71], v[208:211]\n"
  "v_perm_b32 v62, v150, v146, s70\n"
  "v_perm_b32 v63, v158, v154, s70\n"
  "v_mfma_f32_16x16x32_bf16 v[240:243], v[56:59], a[72:75], v[240:243]\n"
  "ds_read_b128 a[136:139], v19 offset:8192\n"
  "ds_read_b128 a[140:143], v49 offset:8192\n"
  "v_mfma_f32_16x16x32_bf16 a[16:19], v[56:59], a[76:79], a[16:19]\n"
  "ds_read_b128 a[96:99], v12 offset:12288\n"
  "v_mfma_f32_16x16x32_bf16 a[48:51], v[56:59], a[80:83], a[48:51]\n"
  "v_mfma_f32_16x16x32_bf16 v[180:183], v[60:63], a[64:67], v[180:183]\n"
  "v_perm_b32 v56, v135, v131, s69\n"
  "v_perm_b32 v57, v143, v139, s69\n"
  "v_mfma_f32_16x16x32_bf16 v[212:215], v[60:63], a[68:71], v[212:215]\n"
  "v_perm_b32 v58, v151, v147, s69\n"
  "v_perm_b32 v59, v159, v155, s69\n"
  "v_mfma_f32_16x16x32_bf16 v[244:247], v[60:63], a[72:75], v[244:247]\n"
  "ds_read_b128 a[144:147], v50 offset:8192\n"
  "ds_read_b128 a[148:151], v51 offset:8192\n"
  "v_mfma_f32_16x16x32_bf16 a[20:23], v[60:63], a[76:79], a[20:23]\n"
  "ds_read_b128 a[100:103], v12 offset:16384\n"
  "v_mfma_f32_16x16x32_bf16 a[52:55], v[60:63], a[80:83], a[52:55]\n"
  "v_mfma_f32_16x16x32_bf16 v[184:187], v[56:59], a[64:67], v[184:187]\n"
  "v_perm_b32 v60, v135, v131, s70\n"
  "v_perm_b32 v61, v143, v139, s70\n"
  "v_mfma_f32_16x16x32_bf16 v[216:219], v[56:59], a[68:71], v[216:219]\n"
  "v_perm_b32 v62, v151, v147, s70\n"
  "v_perm_b32 v63, v159, v155, s70\n"
  "v_mfma_f32_16x16x32_bf16 v[248:251], v[56:59], a[72:75], v[248:251]\n"
  "ds_read_b128 a[152:155], v19 offset:12288\n"
  "ds_read_b128 a[156:159], v49 offset:12288\n"
  "v_mfma_f32_16x16x32_bf16 a[24:27], v[56:59], a[76:79], a[24:27]\n"
  "ds_read_b128 a[160:163], v50 offset:12288\n"
  "ds_read_b128 a[164:167], v51 offset:12288\n"
  "v_mfma_f32_16x16x32_bf16 a[56:59], v[56:59], a[80:83], a[56:59]\n"
  "s_waitcnt vmcnt(0)\n"
  "v_mfma_f32_16x16x32_bf16 v[188:191], v[60:63], a[64:67], v[188:191]\n"
  "v_perm_b32 v96, v68, v64, s69\n"
  "v_perm_b32 v97, v76, v72, s69\n"
  "v_mfma_f32_16x16x32_bf16 v[220:223], v[60:63], a[68:71], v[220:223]\n"
  "v_perm_b32 v98, v84, v80, s69\n"
  "v_perm_b32 v99, v92, v88, s69\n"
  "v_mfma_f32_16x16x32_bf16 v[252:255], v[60:63], a[72:75], v[252:255]\n"
  "ds_read_b128 a[168:171], v19 offset:16384\n"
  "ds_read_b128 a[172:175], v49 offset:16384\n"
  "v_mfma_f32_16x16x32_bf16 a[28:31], v[60:63], a[76:79], a[28:31]\n"
  "ds_read_b128 a[176:179], v50 offset:16384\n"
  "ds_read_b128 a[180:183], v51 offset:16384\n"
  "v_mfma_f32_16x16x32_bf16 a[60:63], v[60:63], a[80:83], a[60:63]\n"
  "s_branch Lj_%=\n"
  "Lv1_%=:\n"
  "s_waitcnt vmcnt(13)\n"
  "s_barrier\n"
  "ds_read_b128 a[64:67], v12\n"
  "ds_read_b128 a[68:71], v12 offset:4096\n"
  "ds_read_b128 a[72:75], v12 offset:8192\n"
  "ds_read_b128 a[76:79], v12 offset:12288\n"
  "ds_read_b128 a[80:83], v12 offset:16384\n"
  "buffer_load_dwordx4 v[96:99], v2, s[44:47], s72 offen\n"
  "buffer_load_dwordx4 v[100:103], v3, s[44:47], s72 offen\n"
  "buffer_load_dwordx4 v[104:107], v4, s[44:47], s72 offen\n"
  "buffer_load_dwordx4 v[108:111], v5, s[44:47], s72 offen\n"
  "buffer_load_dwordx4 v[112:115], v6, s[44:47], s72 offen\n"
  "buffer_load_dwordx4 v[116:119], v7, s[44:47], s72 offen\n"
  "buffer_load_dwordx4 v[120:123], v8, s[44:47], s72 offen\n"
  "buffer_load_dwordx4 v[124:127], v9, s[44:47], s72 offen\n"
  "s_xor_b32 s72, s67, 0x80000\n"
  "buffer_load_dwordx4 v[128:131], v2, s[44:47], s72 offen\n"
  "s_waitcnt vmcnt(20)\n"
  "v_perm_b32 v56, v68, v64, s69\n"
  "s_waitcnt vmcnt(18)\n"
  "v_perm_b32 v57, v76, v72, s69\n"
  "s_waitcnt vmcnt(16)\n"
  "v_perm_b32 v58, v84, v80, s69\n"
  "s_waitcnt vmcnt(14)\n"
  "v_perm_b32 v59, v92, v88, s69\n"
  "s_waitcnt vmcnt(13)\n"
  "v_readfirstlane_b32 s90, v54\n"
  "v_readfirstlane_b32 s91, v55\n"
  "s_add_u32 s90, s90, 0x7f4a7c15\n"
  "s_addc_u32 s91, s91, 0x9e3779b9\n"
  "s_waitcnt lgkmcnt(4)\n"
  "v_mfma_f32_16x16x32_bf16 v[160:163], v[56:59], a[64:67], 0\n"
  "v_perm_b32 v60, v68, v64, s70\n"
  "v_perm_b32 v61, v76, v72, s70\n"
  "s_waitcnt lgkmcnt(3)\n"
  "v_mfma_f32_16x16x32_bf16 v[192:195], v[56:59], a[68:71], 0\n"
  "v_perm_b32 v62, v84, v80, s70\n"
  "v_perm_b32 v63, v92, v88, s70\n"
  "s_waitcnt lgkmcnt(2)\n"
  "v_mfma_f32_16x16x32_bf16 v[224:227], v[56:59], a[72:75], 0\n"
  "buffer_load_dwordx4 v[132:135], v3, s[44:47], s72 offen\n"
  "s_waitcnt lgkmcnt(1)\n"
  "v_mfma_f32_16x16x32_bf16 a[0:3], v[56:59], a[76:79], 0\n"
  "v_xor_b32 v12, 64, v11\n"
  "s_waitcnt lgkmcnt(0)\n"
  "v_mfma_f32_16x16x32_bf16 a[32:35], v[56:59], a[80:83], 0\n"
  "buffer_load_dwordx4 v[136:139], v4, s[44:47], s72 offen\n"
  "v_mfma_f32_16x16x32_bf16 v[164:167], v[60:63], a[64:67], 0\n"
  "v_perm_b32 v56, v69, v65, s69\n"
  "v_perm_b32 v57, v77, v73, s69\n"
  "v_mfma_f32_16x16x32_bf16 v[196:199], v[60:63], a[68:71], 0\n"
  "v_perm_b32 v58, v85, v81, s69\n"
  "v_perm_b32 v59, v93, v89, s69\n"
  "buffer_load_dwordx4 v[140:143], v5, s[44:47], s72 offen\n"
  "v_mfma_f32_16x16x32_bf16 v[228:231], v[60:63], a[72:75], 0\n"
  "v_mfma_f32_16x16x32_bf16 a[4:7], v[60:63], a[76:79], 0\n"
  "buffer_load_dwordx4 v[144:147], v6, s[44:47], s72 offen\n"
  "ds_read_b128 a[84:87], v12\n"
  "v_mfma_f32_16x16x32_bf16 a[36:39], v[60:63], a[80:83], 0\n"
  "v_mfma_f32_16x16x32_bf16 v[168:171], v[56:59], a[64:67], 0\n"
  "v_perm_b32 v60, v69, v65, s70\n"
  "v_perm_b32 v61, v77, v73, s70\n"
  "buffer_load_dwordx4 v[148:151], v7, s[44:47], s72 offen\n"
  "v_mfma_f32_16x16x32_bf16 v[200:203], v[56:59], a[68:71], 0\n"
  "v_perm_b32 v62, v85, v81, s70\n"
  "v_perm_b32 v63, v93, v89, s70\n"
  "v_mfma_f32_16x16x32_bf16 v[232:235], v[56:59], a[72:75], 0\n"
  "buffer_load_dwordx4 v[152:155], v8, s[44:47], s72 offen\n"
  "v_mfma_f32_16x16x32_bf16 a[8:11], v[56:59], a[76:79], 0\n"
  "ds_read_b128 a[88:91], v12 offset:4096\n"
  "v_mfma_f32_16x16x32_bf16 a[40:43], v[56:59], a[80:83], 0\n"
  "buffer_load_dwordx4 v[156:159], v9, s[44:47], s72 offen\n"
  "v_mfma_f32_16x16x32_bf16 v[172:175], v[60:63], a[64:67], 0\n"
  "v_perm_b32 v56, v70, v66, s69\n"
  "v_perm_b32 v57, v78, v74, s69\n"
  "v_mfma_f32_16x16x32_bf16 v[204:207], v[60:63], a[68:71], 0\n"
  "v_perm_b32 v58, v86, v82, s69\n"
  "v_perm_b32 v59, v94, v90, s69\n"
  "v_mfma_f32_16x16x32_bf16 v[236:239], v[60:63], a[72:75], 0\n"
  "v_mfma_f32_16x16x32_bf16 a[12:15], v[60:63], a[76:79], 0\n"
  "ds_read_b128 a[92:95], v12 offset:8192\n"
  "v_mfma_f32_16x16x32_bf16 a[44:47], v[60:63], a[80:83], 0\n"
  "v_mfma_f32_16x16x32_bf16 v[176:179], v[56:59], a[64:67], 0\n"
  "v_perm_b32 v60, v70, v66, s70\n"
  "v_perm_b32 v61, v78, v74, s70\n"
  "v_mfma_f32_16x16x32_bf16 v[208:211], v[56:59], a[68:71], 0\n"
  "v_perm_b32 v62, v86, v82, s70\n"
  "v_perm_b32 v63, v94, v90, s70\n"
  "v_mfma_f32_16x16x32_bf16 v[240:243], v[56:59], a[72:75], 0\n"
  "v_mfma_f32_16x16x32_bf16 a[16:19], v[56:59], a[76:79], 0\n"
  "ds_read_b128 a[96:99], v12 offset:12288\n"
  "v_mfma_f32_16x16x32_bf16 a[48:51], v[56:59], a[80:83], 0\n"
  "v_mfma_f32_16x16x32_bf16 v[180:183], v[60:63], a[64:67], 0\n"
  "v_perm_b32 v56, v71, v67, s69\n"
  "v_perm_b32 v57, v79, v75, s69\n"
  "v_mfma_f32_16x16x32_bf16 v[212:215], v[60:63], a[68:71], 0\n"
  "v_perm_b32 v58, v87, v83, s69\n"
  "v_perm_b32 v59, v95, v91, s69\n"
  "v_mfma_f32_16x16x32_bf16 v[244:247], v[60:63], a[72:75], 0\n"
  "v_mfma_f32_16x16x32_bf16 a[20:23], v[60:63], a[76:79], 0\n"
  "ds_read_b128 a[100:103], v12 offset:16384\n"
  "v_mfma_f32_16x16x32_bf16 a[52:55], v[60:63], a[80:83], 0\n"
  "v_mfma_f32_16x16x32_bf16 v[184:187], v[56:59], a[64:67], 0\n"
  "v_perm_b32 v60, v71, v67, s70\n"
  "v_perm_b32 v61, v79, v75, s70\n"
  "v_mfma_f32_16x16x32_bf16 v[216:219], v[56:59], a[68:71], 0\n"
  "v_perm_b32 v62, v87, v83, s70\n"
  "v_perm_b32 v63, v95, v91, s70\n"
  "v_mfma_f32_16x16x32_bf16 v[248:251], v[56:59], a[72:75], 0\n"
  "s_xor_b32 s72, s67, 0xc0000\n"
  "buffer_load_dwordx4 v[64:67], v2, s[44:47], s72 offen\n"
  "v_mfma_f32_16x16x32_bf16 a[24:27], v[56:59], a[76:79], 0\n"
  "buffer_load_dwordx4 v[68:71], v3, s[44:47], s72 offen\n"
  "v_mfma_f32_16x16x32_bf16 a[56:59], v[56:59], a[80:83], 0\n"
  "s_waitcnt vmcnt(10)\n"
  "buffer_load_dwordx4 v[72:75], v4, s[44:47], s72 offen\n"
  "v_mfma_f32_16x16x32_bf16 v[188:191], v[60:63], a[64:67], 0\n"
  "v_perm_b32 v56, v100, v96, s69\n"
  "v_perm_b32 v57, v108, v104, s69\n"
  "buffer_load_dwordx4 v[76:79], v5, s[44:47], s72 offen\n"
  "v_mfma_f32_16x16x32_bf16 v[220:223], v[60:63], a[68:71], 0\n"
  "v_perm_b32 v58, v116, v112, s69\n"
  "v_perm_b32 v59, v124, v120, s69\n"
  "buffer_load_dwordx4 v[80:83], v6, s[44:47], s72 offen\n"
  "v_mfma_f32_16x16x32_bf16 v[252:255], v[60:63], a[72:75], 0\n"
  "buffer_load_dwordx4 v[84:87], v7, s[44:47], s72 offen\n"
  "v_mfma_f32_16x16x32_bf16 a[28:31], v[60:63], a[76:79], 0\n"
  "s_waitcnt lgkmcnt(0)\n"
  "buffer_load_dwordx4 v[88:91], v8, s[44:47], s72 offen\n"
  "v_mfma_f32_16x16x32_bf16 a[60:63], v[60:63], a[80:83], 0\n"
  "buffer_load_dwordx4 v[92:95], v9, s[44:47], s72 offen\n"
  "v_mfma_f32_16x16x32_bf16 v[160:163], v[56:59], a[84:87], v[160:163]\n"
  "v_perm_b32 v60, v100, v96, s70\n"
  "v_perm_b32 v61, v108, v104, s70\n"
  "v_mfma_f32_16x16x32_bf16 v[192:195], v[56:59], a[88:91], v[192:195]\n"
  "v_perm_b32 v62, v116, v112, s70\n"
  "v_perm_b32 v63, v124, v120, s70\n"
  "v_mfma_f32_16x16x32_bf16 v[224:227], v[56:59], a[92:95], v[224:227]\n"
  "s_add_u32 m0, s96, 0x5000\n"
  "s_xor_b32 s73, s68, 0x100\n"
  "buffer_load_dwordx4 v10, s[32:35], s73 offen lds\n"
  "v_mfma_f32_16x16x32_bf16 a[0:3], v[56:59], a[96:99], a[0:3]\n"
  "v_xor_b32 v12, 0x80, v11\n"
  "v_mfma_f32_16x16x32_bf16 a[32:35], v[56:59], a[100:103], a[32:35]\n"
  "s_add_u32 m0, s96, 0x6000\n"
  "s_xor_b32 s73, s68, 0x4100\n"
  "buffer_load_dwordx4 v10, s[32:35], s73 offen lds\n"
  "v_mfma_f32_16x16x32_bf16 v[164:167], v[60:63], a[84:87], v[164:167]\n"
  "v_perm_b32 v56, v101, v97, s69\n"
  "v_perm_b32 v57, v109, v105, s69\n"
  "s_add_u32 m0, s96, 0x7000\n"
  "s_xor_b32 s73, s68, 0x8100\n"
  "buffer_load_dwordx4 v10, s[32:35], s73 offen lds\n"
  "v_mfma_f32_16x16x32_bf16 v[196:199], v[60:63], a[88:91], v[196:199]\n"
  "v_perm_b32 v58, v117, v113, s69\n"
  "v_perm_b32 v59, v125, v121, s69\n"
  "v_mfma_f32_16x16x32_bf16 v[228:231], v[60:63], a[92:95], v[228:231]\n"
  "s_add_u32 m0, s96, 0x8000\n"
  "s_xor_b32 s73, s68, 0xc100\n"
  "buffer_load_dwordx4 v10, s[32:35], s73 offen lds\n"
  "v_mfma_f32_16x16x32_bf16 a[4:7], v[60:63], a[96:99], a[4:7]\n"
  "ds_read_b128 a[64:67], v12\n"
  "v_mfma_f32_16x16x32_bf16 a[36:39], v[60:63], a[100:103], a[36:39]\n"
  "s_add_u32 m0, s96, 0x9000\n"
  "s_xor_b32 s73, s68, 0x10100\n"
  "buffer_load_dwordx4 v10, s[32:35], s73 offen lds\n"
  "v_mfma_f32_16x16x32_bf16 v[168:171], v[56:59], a[84:87], v[168:171]\n"
  "v_perm_b32 v60, v101, v97, s70\n"
  "v_perm_b32 v61, v109, v105, s70\n"
  "s_add_u32 m0, s77, 0x1000\n"
  "s_nop 0\n"
  "buffer_load_dwordx4 v13, s[36:39], s92 offen sc1 nt lds\n"
  "v_mfma_f32_16x16x32_bf16 v[200:203], v[56:59], a[88:91], v[200:203]\n"
  "v_perm_b32 v62, v117, v113, s70\n"
  "v_perm_b32 v63, v125, v121, s70\n"
  "v_mfma_f32_16x16x32_bf16 v[232:235], v[56:59], a[92:95], v[232:235]\n"
  "buffer_load_dwordx4 v14, s[36:39], s92 offen offset:1024 sc1 nt lds\n"
  "v_mfma_f32_16x16x32_bf16 a[8:11], v[56:59], a[96:99], a[8:11]\n"
  "ds_read_b128 a[68:71], v12 offset:4096\n"
  "v_mfma_f32_16x16x32_bf16 a[40:43], v[56:59], a[100:103], a[40:43]\n"
  "buffer_load_dwordx4 v15, s[36:39], s92 offen offset:2048 sc1 nt lds\n"
  "v_mfma_f32_16x16x32_bf16 v[172:175], v[60:63], a[84:87], v[172:175]\n"
  "v_perm_b32 v56, v102, v98, s69\n"
  "v_perm_b32 v57, v110, v106, s69\n"
  "v_mfma_f32_16x16x32_bf16 v[204:207], v[60:63], a[88:91], v[204:207]\n"
  "v_perm_b32 v58, v118, v114, s69\n"
  "v_perm_b32 v59, v126, v122, s69\n"
  "v_mfma_f32_16x16x32_bf16 v[236:239], v[60:63], a[92:95], v[236:239]\n"
  "v_mfma_f32_16x16x32_bf16 a[12:15], v[60:63], a[96:99], a[12:15]\n"
  "ds_read_b128 a[72:75], v12 offset:8192\n"
  "v_mfma_f32_16x16x32_bf16 a[44:47], v[60:63], a[100:103], a[44:47]\n"
  "v_mfma_f32_16x16x32_bf16 v[176:179], v[56:59], a[84:87], v[176:179]\n"
  "v_perm_b32 v60, v102, v98, s70\n"
  "v_perm_b32 v61, v110, v106, s70\n"
  "v_mfma_f32_16x16x32_bf16 v[208:211], v[56:59], a[88:91], v[208:211]\n"
  "v_perm_b32 v62, v118, v114, s70\n"
  "v_perm_b32 v63, v126, v122, s70\n"
  "v_mfma_f32_16x16x32_bf16 v[240:243], v[56:59], a[92:95], v[240:243]\n"
  "v_mfma_f32_16x16x32_bf16 a[16:19], v[56:59], a[96:99], a[16:19]\n"
  "ds_read_b128 a[76:79], v12 offset:12288\n"
  "v_mfma_f32_16x16x32_bf16 a[48:51], v[56:59], a[100:103], a[48:51]\n"
  "v_mfma_f32_16x16x32_bf16 v[180:183], v[60:63], a[84:87], v[180:183]\n"
  "v_perm_b32 v56, v103, v99, s69\n"
  "v_perm_b32 v57, v111, v107, s69\n"
  "v_mfma_f32_16x16x32_bf16 v[212:215], v[60:63], a[88:91], v[212:215]\n"
  "v_perm_b32 v58, v119, v115, s69\n"
  "v_perm_b32 v59, v127, v123, s69\n"
  "v_mfma_f32_16x16x32_bf16 v[244:247], v[60:63], a[92:95], v[244:247]\n"
  "v_mfma_f32_16x16x32_bf16 a[20:23], v[60:63], a[96:99], a[20:23]\n"
  "ds_read_b128 a[80:83], v12 offset:16384\n"
  "v_mfma_f32_16x16x32_bf16 a[52:55], v[60:63], a[100:103], a[52:55]\n"
  "v_mfma_f32_16x16x32_bf16 v[184:187], v[56:59], a[84:87], v[184:187]\n"
  "v_perm_b32 v60, v103, v99, s70\n"
  "v_perm_b32 v61, v111, v107, s70\n"
  "v_mfma_f32_16x16x32_bf16 v[216:219], v[56:59], a[88:91], v[216:219]\n"
  "v_perm_b32 v62, v119, v115, s70\n"
  "v_perm_b32 v63, v127, v123, s70\n"
  "v_mfma_f32_16x16x32_bf16 v[248:251], v[56:59], a[92:95], v[248:251]\n"
  "v_mfma_f32_16x16x32_bf16 a[24:27], v[56:59], a[96:99], a[24:27]\n"
  "v_mfma_f32_16x16x32_bf16 a[56:59], v[56:59], a[100:103], a[56:59]\n"
  "s_waitcnt vmcnt(16)\n"
  "v_mfma_f32_16x16x32_bf16 v[188:191], v[60:63], a[84:87], v[188:191]\n"
  "v_perm_b32 v56, v132, v128, s69\n"
  "v_perm_b32 v57, v140, v136, s69\n"
  "v_mfma_f32_16x16x32_bf16 v[220:223], v[60:63], a[88:91], v[220:223]\n"
  "v_perm_b32 v58, v148, v144, s69\n"
  "v_perm_b32 v59, v156, v152, s69\n"
  "v_mfma_f32_16x16x32_bf16 v[252:255], v[60:63], a[92:95], v[252:255]\n"
  "v_mfma_f32_16x16x32_bf16 a[28:31], v[60:63], a[96:99], a[28:31]\n"
  "s_waitcnt lgkmcnt(0)\n"
  "v_mfma_f32_16x16x32_bf16 a[60:63], v[60:63], a[100:103], a[60:63]\n"
  "v_mfma_f32_16x16x32_bf16 v[160:163], v[56:59], a[64:67], v[160:163]\n"
  "v_perm_b32 v60, v132, v128, s70\n"
  "v_perm_b32 v61, v140, v136, s70\n"
  "v_mfma_f32_16x16x32_bf16 v[192:195], v[56:59], a[68:71], v[192:195]\n"
  "v_perm_b32 v62, v148, v144, s70\n"
  "v_perm_b32 v63, v156, v152, s70\n"
  "v_mfma_f32_16x16x32_bf16 v[224:227], v[56:59], a[72:75], v[224:227]\n"
  "v_mfma_f32_16x16x32_bf16 a[0:3], v[56:59], a[76:79], a[0:3]\n"
  "v_xor_b32 v12, 0xc0, v11\n"
  "v_mfma_f32_16x16x32_bf16 a[32:35], v[56:59], a[80:83], a[32:35]\n"
  "v_mfma_f32_16x16x32_bf16 v[164:167], v[60:63], a[64:67], v[164:167]\n"
  "v_perm_b32 v56, v133, v129, s69\n"
  "v_perm_b32 v57, v141, v137, s69\n"
  "v_mfma_f32_16x16x32_bf16 v[196:199], v[60:63], a[68:71], v[196:199]\n"
  "v_perm_b32 v58, v149, v145, s69\n"
  "v_perm_b32 v59, v157, v153, s69\n"
  "v_mfma_f32_16x16x32_bf16 v[228:231], v[60:63], a[72:75], v[228:231]\n"
  "v_mfma_f32_16x16x32_bf16 a[4:7], v[60:63], a[76:79], a[4:7]\n"
  "ds_read_b128 a[84:87], v12\n"
  "v_mfma_f32_16x16x32_bf16 a[36:39], v[60:63], a[80:83], a[36:39]\n"
  "v_mfma_f32_16x16x32_bf16 v[168:171], v[56:59], a[64:67], v[168:171]\n"
  "v_perm_b32 v60, v133, v129, s70\n"
  "v_perm_b32 v61, v141, v137, s70\n"
  "v_mfma_f32_16x16x32_bf16 v[200:203], v[56:59], a[68:71], v[200:203]\n"
  "v_perm_b32 v62, v149, v145, s70\n"
  "v_perm_b32 v63, v157, v153, s70\n"
  "v_mfma_f32_16x16x32_bf16 v[232:235], v[56:59], a[72:75], v[232:235]\n"
  "v_mfma_f32_16x16x32_bf16 a[8:11], v[56:59], a[76:79], a[8:11]\n"
  "ds_read_b128 a[88:91], v12 offset:4096\n"
  "v_mfma_f32_16x16x32_bf16 a[40:43], v[56:59], a[80:83], a[40:43]\n"
  "v_mfma_f32_16x16x32_bf16 v[172:175], v[60:63], a[64:67], v[172:175]\n"
  "v_perm_b32 v56, v134, v130, s69\n"
  "v_perm_b32 v57, v142, v138, s69\n"
  "v_mfma_f32_16x16x32_bf16 v[204:207], v[60:63], a[68:71], v[204:207]\n"
  "v_perm_b32 v58, v150, v146, s69\n"
  "v_perm_b32 v59, v158, v154, s69\n"
  "v_mfma_f32_16x16x32_bf16 v[236:239], v[60:63], a[72:75], v[236:239]\n"
  "v_mfma_f32_16x16x32_bf16 a[12:15], v[60:63], a[76:79], a[12:15]\n"
  "ds_read_b128 a[92:95], v12 offset:8192\n"
  "v_mfma_f32_16x16x32_bf16 a[44:47], v[60:63], a[80:83], a[44:47]\n"
  "v_mfma_f32_16x16x32_bf16 v[176:179], v[56:59], a[64:67], v[176:179]\n"
  "v_perm_b32 v60, v134, v130, s70\n"
  "v_perm_b32 v61, v142, v138, s70\n"
  "v_mfma_f32_16x16x32_bf16 v[208:211], v[56:59], a[68:71], v[208:211]\n"
  "v_perm_b32 v62, v150, v146, s70\n"
  "v_perm_b32 v63, v158, v154, s70\n"
  "v_mfma_f32_16x16x32_bf16 v[240:243], v[56:59], a[72:75], v[240:243]\n"
  "s_xor_b32 s72, s67, 0x100000\n"
  "buffer_load_dwordx4 v[96:99], v2, s[44:47], s72 offen\n"
  "v_mfma_f32_16x16x32_bf16 a[16:19], v[56:59], a[76:79], a[16:19]\n"
  "ds_read_b128 a[96:99], v12 offset:12288\n"
  "v_mfma_f32_16x16x32_bf16 a[48:51], v[56:59], a[80:83], a[48:51]\n"
  "buffer_load_dwordx4 v[100:103], v3, s[44:47], s72 offen\n"
  "v_mfma_f32_16x16x32_bf16 v[180:183], v[60:63], a[64:67], v[180:183]\n"
  "v_perm_b32 v56, v135, v131, s69\n"
  "v_perm_b32 v57, v143, v139, s69\n"
  "v_mfma_f32_16x16x32_bf16 v[212:215], v[60:63], a[68:71], v[212:215]\n"
  "v_perm_b32 v58, v151, v147, s69\n"
  "v_perm_b32 v59, v159, v155, s69\n"
  "buffer_load_dwordx4 v[104:107], v4, s[44:47], s72 offen\n"
  "v_mfma_f32_16x16x32_bf16 v[244:247], v[60:63], a[72:75], v[244:247]\n"
  "v_mfma_f32_16x16x32_bf16 a[20:23], v[60:63], a[76:79], a[20:23]\n"
  "buffer_load_dwordx4 v[108:111], v5, s[44:47], s72 offen\n"
  "ds_read_b128 a[100:103], v12 offset:16384\n"
  "v_mfma_f32_16x16x32_bf16 a[52:55], v[60:63], a[80:83], a[52:55]\n"
  "v_mfma_f32_16x16x32_bf16 v[184:187], v[56:59], a[64:67], v[184:187]\n"
  "v_perm_b32 v60, v135, v131, s70\n"
  "v_perm_b32 v61, v143, v139, s70\n"
  "buffer_load_dwordx4 v[112:115], v6, s[44:47], s72 offen\n"
  "v_mfma_f32_16x16x32_bf16 v[216:219], v[56:59], a[68:71], v[216:219]\n"
  "v_perm_b32 v62, v151, v147, s70\n"
  "v_perm_b32 v63, v159, v155, s70\n"
  "v_mfma_f32_16x16x32_bf16 v[248:251], v[56:59], a[72:75], v[248:251]\n"
  "buffer_load_dwordx4 v[116:119], v7, s[44:47], s72 offen\n"
  "v_mfma_f32_16x16x32_bf16 a[24:27], v[56:59], a[76:79], a[24:27]\n"
  "v_mfma_f32_16x16x32_bf16 a[56:59], v[56:59], a[80:83], a[56:59]\n"
  "s_waitcnt vmcnt(14)\n"
  "buffer_load_dwordx4 v[120:123], v8, s[44:47], s72 offen\n"
  "v_mfma_f32_16x16x32_bf16 v[188:191], v[60:63], a[64:67], v[188:191]\n"
  "v_perm_b32 v56, v68, v64, s69\n"
  "v_perm_b32 v57, v76, v72, s69\n"
  "v_mfma_f32_16x16x32_bf16 v[220:223], v[60:63], a[68:71], v[220:223]\n"
  "v_perm_b32 v58, v84, v80, s69\n"
  "v_perm_b32 v59, v92, v88, s69\n"
  "buffer_load_dwordx4 v[124:127], v9, s[44:47], s72 offen\n"
  "v_mfma_f32_16x16x32_bf16 v[252:255], v[60:63], a[72:75], v[252:255]\n"
  "v_mfma_f32_16x16x32_bf16 a[28:31], v[60:63], a[76:79], a[28:31]\n"
  "s_waitcnt lgkmcnt(0)\n"
  "v_mfma_f32_16x16x32_bf16 a[60:63], v[60:63], a[80:83], a[60:63]\n"
  "v_mfma_f32_16x16x32_bf16 v[160:163], v[56:59], a[84:87], v[160:163]\n"
  "v_perm_b32 v60, v68, v64, s70\n"
  "v_perm_b32 v61, v76, v72, s70\n"
  "s_xor_b32 s72, s67, 0x140000\n"
  "buffer_load_dwordx4 v[128:131], v2, s[44:47], s72 offen\n"
  "v_mfma_f32_16x16x32_bf16 v[192:195], v[56:59], a[88:91], v[192:195]\n"
  "v_perm_b32 v62, v84, v80, s70\n"
  "v_perm_b32 v63, v92, v88, s70\n"
  "v_mfma_f32_16x16x32_bf16 v[224:227], v[56:59], a[92:95], v[224:227]\n"
  "buffer_load_dwordx4 v[132:135], v3, s[44:47], s72 offen\n"
  "v_mfma_f32_16x16x32_bf16 a[0:3], v[56:59], a[96:99], a[0:3]\n"
  "v_xor_b32 v12, 0x5000, v11\n"
  "v_mfma_f32_16x16x32_bf16 a[32:35], v[56:59], a[100:103], a[32:35]\n"
  "buffer_load_dwordx4 v[136:139], v4, s[44:47], s72 offen\n"
  "s_waitcnt vmcnt(14)\n"
  "s_barrier\n"
  "v_mfma_f32_16x16x32_bf16 v[164:167], v[60:63], a[84:87], v[164:167]\n"
  "v_perm_b32 v56, v69, v65, s69\n"
  "v_perm_b32 v57, v77, v73, s69\n"
  "v_mfma_f32_16x16x32_bf16 v[196:199], v[60:63], a[88:91], v[196:199]\n"
  "v_perm_b32 v58, v85, v81, s69\n"
  "v_perm_b32 v59, v93, v89, s69\n"
  "buffer_load_dwordx4 v[140:143], v5, s[44:47], s72 offen\n"
  "v_mfma_f32_16x16x32_bf16 v[228:231], v[60:63], a[92:95], v[228:231]\n"
  "v_mfma_f32_16x16x32_bf16 a[4:7], v[60:63], a[96:99], a[4:7]\n"
  "buffer_load_dwordx4 v[144:147], v6, s[44:47], s72 offen\n"
  "ds_read_b128 a[64:67], v12\n"
  "v_mfma_f32_16x16x32_bf16 a[36:39], v[60:63], a[100:103], a[36:39]\n"
  "v_mfma_f32_16x16x32_bf16 v[168:171], v[56:59], a[84:87], v[168:171]\n"
  "v_perm_b32 v60, v69, v65, s70\n"
  "v_perm_b32 v61, v77, v73, s70\n"
  "buffer_load_dwordx4 v[148:151], v7, s[44:47], s72 offen\n"
  "v_mfma_f32_16x16x32_bf16 v[200:203], v[56:59], a[88:91], v[200:203]\n"
  "v_perm_b32 v62, v85, v81, s70\n"
  "v_perm_b32 v63, v93, v89, s70\n"
  "v_mfma_f32_16x16x32_bf16 v[232:235], v[56:59], a[92:95], v[232:235]\n"
  "buffer_load_dwordx4 v[152:155], v8, s[44:47], s72 offen\n"
  "v_mfma_f32_16x16x32_bf16 a[8:11], v[56:59], a[96:99], a[8:11]\n"
  "ds_read_b128 a[68:71], v12 offset:4096\n"
  "v_mfma_f32_16x16x32_bf16 a[40:43], v[56:59], a[100:103], a[40:43]\n"
  "buffer_load_dwordx4 v[156:159], v9, s[44:47], s72 offen\n"
  "v_mfma_f32_16x16x32_bf16 v[172:175], v[60:63], a[84:87], v[172:175]\n"
  "v_perm_b32 v56, v70, v66, s69\n"
  "v_perm_b32 v57, v78, v74, s69\n"
  "v_mfma_f32_16x16x32_bf16 v[204:207], v[60:63], a[88:91], v[204:207]\n"
  "v_perm_b32 v58, v86, v82, s69\n"
  "v_perm_b32 v59, v94, v90, s69\n"
  "v_mfma_f32_16x16x32_bf16 v[236:239], v[60:63], a[92:95], v[236:239]\n"
  "v_mfma_f32_16x16x32_bf16 a[12:15], v[60:63], a[96:99], a[12:15]\n"
  "ds_read_b128 a[72:75], v12 offset:8192\n"
  "v_mfma_f32_16x16x32_bf16 a[44:47], v[60:63], a[100:103], a[44:47]\n"
  "v_mfma_f32_16x16x32_bf16 v[176:179], v[56:59], a[84:87], v[176:179]\n"
  "v_perm_b32 v60, v70, v66, s70\n"
  "v_perm_b32 v61, v78, v74, s70\n"
  "v_mfma_f32_16x16x32_bf16 v[208:211], v[56:59], a[88:91], v[208:211]\n"
  "v_perm_b32 v62, v86, v82, s70\n"
  "v_perm_b32 v63, v94, v90, s70\n"
  "v_mfma_f32_16x16x32_bf16 v[240:243], v[56:59], a[92:95], v[240:243]\n"
  "v_mfma_f32_16x16x32_bf16 a[16:19], v[56:59], a[96:99], a[16:19]\n"
  "ds_read_b128 a[76:79], v12 offset:12288\n"
  "v_mfma_f32_16x16x32_bf16 a[48:51], v[56:59], a[100:103], a[48:51]\n"
  "v_mfma_f32_16x16x32_bf16 v[180:183], v[60:63], a[84:87], v[180:183]\n"
  "v_perm_b32 v56, v71, v67, s69\n"
  "v_perm_b32 v57, v79, v75, s69\n"
  "v_mfma_f32_16x16x32_bf16 v[212:215], v[60:63], a[88:91], v[212:215]\n"
  "v_perm_b32 v58, v87, v83, s69\n"
  "v_perm_b32 v59, v95, v91, s69\n"
  "v_mfma_f32_16x16x32_bf16 v[244:247], v[60:63], a[92:95], v[244:247]\n"
  "v_mfma_f32_16x16x32_bf16 a[20:23], v[60:63], a[96:99], a[20:23]\n"
  "ds_read_b128 a[80:83], v12 offset:16384\n"
  "v_mfma_f32_16x16x32_bf16 a[52:55], v[60:63], a[100:103], a[52:55]\n"
  "v_mfma_f32_16x16x32_bf16 v[184:187], v[56:59], a[84:87], v[184:187]\n"
  "v_perm_b32 v60, v71, v67, s70\n"
  "v_perm_b32 v61, v79, v75, s70\n"
  "v_mfma_f32_16x16x32_bf16 v[216:219], v[56:59], a[88:91], v[216:219]\n"
  "v_perm_b32 v62, v87, v83, s70\n"
  "v_perm_b32 v63, v95, v91, s70\n"
  "v_mfma_f32_16x16x32_bf16 v[248:251], v[56:59], a[92:95], v[248:251]\n"
  "s_xor_b32 s72, s67, 0x180000\n"
  "buffer_load_dwordx4 v[64:67], v2, s[44:47], s72 offen\n"
  "v_mfma_f32_16x16x32_bf16 a[24:27], v[56:59], a[96:99], a[24:27]\n"
  "buffer_load_dwordx4 v[68:71], v3, s[44:47], s72 offen\n"
  "v_mfma_f32_16x16x32_bf16 a[56:59], v[56:59], a[100:103], a[56:59]\n"
  "s_waitcnt vmcnt(10)\n"
  "buffer_load_dwordx4 v[72:75], v4, s[44:47], s72 offen\n"
  "v_mfma_f32_16x16x32_bf16 v[188:191], v[60:63], a[84:87], v[188:191]\n"
  "v_perm_b32 v56, v100, v96, s69\n"
  "v_perm_b32 v57, v108, v104, s69\n"
  "buffer_load_dwordx4 v[76:79], v5, s[44:47], s72 offen\n"
  "v_mfma_f32_16x16x32_bf16 v[220:223], v[60:63], a[88:91], v[220:223]\n"
  "v_perm_b32 v58, v116, v112, s69\n"
  "v_perm_b32 v59, v124, v120, s69\n"
  "buffer_load_dwordx4 v[80:83], v6, s[44:47], s72 offen\n"
  "v_mfma_f32_16x16x32_bf16 v[252:255], v[60:63], a[92:95], v[252:255]\n"
  "buffer_load_dwordx4 v[84:87], v7, s[44:47], s72 offen\n"
  "v_mfma_f32_16x16x32_bf16 a[28:31], v[60:63], a[96:99], a[28:31]\n"
  "s_waitcnt lgkmcnt(0)\n"
  "buffer_load_dwordx4 v[88:91], v8, s[44:47], s72 offen\n"
  "v_mfma_f32_16x16x32_bf16 a[60:63], v[60:63], a[100:103], a[60:63]\n"
  "buffer_load_dwordx4 v[92:95], v9, s[44:47], s72 offen\n"
  "v_mfma_f32_16x16x32_bf16 v[160:163], v[56:59], a[64:67], v[160:163]\n"
  "v_perm_b32 v60, v100, v96, s70\n"
  "v_perm_b32 v61, v108, v104, s70\n"
  "v_mfma_f32_16x16x32_bf16 v[192:195], v[56:59], a[68:71], v[192:195]\n"
  "v_perm_b32 v62, v116, v112, s70\n"
  "v_perm_b32 v63, v124, v120, s70\n"
  "v_mfma_f32_16x16x32_bf16 v[224:227], v[56:59], a[72:75], v[224:227]\n"
  "s_add_u32 m0, s96, 0xa000\n"
  "s_xor_b32 s73, s68, 0x200\n"
  "buffer_load_dwordx4 v10, s[32:35], s73 offen lds\n"
  "v_mfma_f32_16x16x32_bf16 a[0:3], v[56:59], a[76:79], a[0:3]\n"
  "v_xor_b32 v12, 0x5040, v11\n"
  "v_mfma_f32_16x16x32_bf16 a[32:35], v[56:59], a[80:83], a[32:35]\n"
  "s_add_u32 m0, s96, 0xb000\n"
  "s_xor_b32 s73, s68, 0x4200\n"
  "buffer_load_dwordx4 v10, s[32:35], s73 offen lds\n"
  "v_mfma_f32_16x16x32_bf16 v[164:167], v[60:63], a[64:67], v[164:167]\n"
  "v_perm_b32 v56, v101, v97, s69\n"
  "v_perm_b32 v57, v109, v105, s69\n"
  "s_add_u32 m0, s96, 0xc000\n"
  "s_xor_b32 s73, s68, 0x8200\n"
  "buffer_load_dwordx4 v10, s[32:35], s73 offen lds\n"
  "v_mfma_f32_16x16x32_bf16 v[196:199], v[60:63], a[68:71], v[196:199]\n"
  "v_perm_b32 v58, v117, v113, s69\n"
  "v_perm_b32 v59, v125, v121, s69\n"
  "v_mfma_f32_16x16x32_bf16 v[228:231], v[60:63], a[72:75], v[228:231]\n"
  "s_add_u32 m0, s96, 0xd000\n"
  "s_xor_b32 s73, s68, 0xc200\n"
  "buffer_load_dwordx4 v10, s[32:35], s73 offen lds\n"
  "v_mfma_f32_16x16x32_bf16 a[4:7], v[60:63], a[76:79], a[4:7]\n"
  "ds_read_b128 a[84:87], v12\n"
  "v_mfma_f32_16x16x32_bf16 a[36:39], v[60:63], a[80:83], a[36:39]\n"
  "s_add_u32 m0, s96, 0xe000\n"
  "s_xor_b32 s73, s68, 0x10200\n"
  "buffer_load_dwordx4 v10, s[32:35], s73 offen lds\n"
  "v_mfma_f32_16x16x32_bf16 v[168:171], v[56:59], a[64:67], v[168:171]\n"
  "v_perm_b32 v60, v101, v97, s70\n"
  "v_perm_b32 v61, v109, v105, s70\n"
  "s_add_u32 m0, s77, 0x1000\n"
  "s_nop 0\n"
  "buffer_load_dwordx4 v16, s[36:39], s92 offen offset:3072 sc1 nt lds\n"
  "v_mfma_f32_16x16x32_bf16 v[200:203], v[56:59], a[68:71], v[200:203]\n"
  "v_perm_b32 v62, v117, v113, s70\n"
  "v_perm_b32 v63, v125, v121, s70\n"
  "v_mfma_f32_16x16x32_bf16 v[232:235], v[56:59], a[72:75], v[232:235]\n"
  "s_add_u32 m0, s77, 0x2000\n"
  "s_nop 0\n"
  "buffer_load_dwordx4 v13, s[36:39], s93 offen sc1 nt lds\n"
  "v_mfma_f32_16x16x32_bf16 a[8:11], v[56:59], a[76:79], a[8:11]\n"
  "ds_read_b128 a[88:91], v12 offset:4096\n"
  "v_mfma_f32_16x16x32_bf16 a[40:43], v[56:59], a[80:83], a[40:43]\n"
  "buffer_load_dwordx4 v14, s[36:39], s93 offen offset:1024 sc1 nt lds\n"
  "v_mfma_f32_16x16x32_bf16 v[172:175], v[60:63], a[64:67], v[172:175]\n"
  "v_perm_b32 v56, v102, v98, s69\n"
  "v_perm_b32 v57, v110, v106, s69\n"
  "v_mfma_f32_16x16x32_bf16 v[204:207], v[60:63], a[68:71], v[204:207]\n"
  "v_perm_b32 v58, v118, v114, s69\n"
  "v_perm_b32 v59, v126, v122, s69\n"
  "v_mfma_f32_16x16x32_bf16 v[236:239], v[60:63], a[72:75], v[236:239]\n"
  "v_mfma_f32_16x16x32_bf16 a[12:15], v[60:63], a[76:79], a[12:15]\n"
  "ds_read_b128 a[92:95], v12 offset:8192\n"
  "v_mfma_f32_16x16x32_bf16 a[44:47], v[60:63], a[80:83], a[44:47]\n"
  "v_mfma_f32_16x16x32_bf16 v[176:179], v[56:59], a[64:67], v[176:179]\n"
  "v_perm_b32 v60, v102, v98, s70\n"
  "v_perm_b32 v61, v110, v106, s70\n"
  "v_mfma_f32_16x16x32_bf16 v[208:211], v[56:59], a[68:71], v[208:211]\n"
  "v_perm_b32 v62, v118, v114, s70\n"
  "v_perm_b32 v63, v126, v122, s70\n"
  "v_mfma_f32_16x16x32_bf16 v[240:243], v[56:59], a[72:75], v[240:243]\n"
  "v_mfma_f32_16x16x32_bf16 a[16:19], v[56:59], a[76:79], a[16:19]\n"
  "ds_read_b128 a[96:99], v12 offset:12288\n"
  "v_mfma_f32_16x16x32_bf16 a[48:51], v[56:59], a[80:83], a[48:51]\n"
  "v_mfma_f32_16x16x32_bf16 v[180:183], v[60:63], a[64:67], v[180:183]\n"
  "v_perm_b32 v56, v103, v99, s69\n"
  "v_perm_b32 v57, v111, v107, s69\n"
  "v_mfma_f32_16x16x32_bf16 v[212:215], v[60:63], a[68:71], v[212:215]\n"
  "v_perm_b32 v58, v119, v115, s69\n"
  "v_perm_b32 v59, v127, v123, s69\n"
  "v_mfma_f32_16x16x32_bf16 v[244:247], v[60:63], a[72:75], v[244:247]\n"
  "v_mfma_f32_16x16x32_bf16 a[20:23], v[60:63], a[76:79], a[20:23]\n"
  "ds_read_b128 a[100:103], v12 offset:16384\n"
  "v_mfma_f32_16x16x32_bf16 a[52:55], v[60:63], a[80:83], a[52:55]\n"
  "v_mfma_f32_16x16x32_bf16 v[184:187], v[56:59], a[64:67], v[184:187]\n"
  "v_perm_b32 v60, v103, v99, s70\n"
  "v_perm_b32 v61, v111, v107, s70\n"
  "v_mfma_f32_16x16x32_bf16 v[216:219], v[56:59], a[68:71], v[216:219]\n"
  "v_perm_b32 v62, v119, v115, s70\n"
  "v_perm_b32 v63, v127, v123, s70\n"
  "v_mfma_f32_16x16x32_bf16 v[248:251], v[56:59], a[72:75], v[248:251]\n"
  "v_mfma_f32_16x16x32_bf16 a[24:27], v[56:59], a[76:79], a[24:27]\n"
  "v_mfma_f32_16x16x32_bf16 a[56:59], v[56:59], a[80:83], a[56:59]\n"
  "s_waitcnt vmcnt(16)\n"
  "v_mfma_f32_16x16x32_bf16 v[188:191], v[60:63], a[64:67], v[188:191]\n"
  "v_perm_b32 v56, v132, v128, s69\n"
  "v_perm_b32 v57, v140, v136, s69\n"
  "v_mfma_f32_16x16x32_bf16 v[220:223], v[60:63], a[68:71], v[220:223]\n"
  "v_perm_b32 v58, v148, v144, s69\n"
  "v_perm_b32 v59, v156, v152, s69\n"
  "v_mfma_f32_16x16x32_bf16 v[252:255], v[60:63], a[72:75], v[252:255]\n"
  "v_mfma_f32_16x16x32_bf16 a[28:31], v[60:63], a[76:79], a[28:31]\n"
  "s_waitcnt lgkmcnt(0)\n"
  "v_mfma_f32_16x16x32_bf16 a[60:63], v[60:63], a[80:83], a[60:63]\n"
  "v_mfma_f32_16x16x32_bf16 v[160:163], v[56:59], a[84:87], v[160:163]\n"
  "v_perm_b32 v60, v132, v128, s70\n"
  "v_perm_b32 v61, v140, v136, s70\n"
  "v_mfma_f32_16x16x32_bf16 v[192:195], v[56:59], a[88:91], v[192:195]\n"
  "v_perm_b32 v62, v148, v144, s70\n"
  "v_perm_b32 v63, v156, v152, s70\n"
  "v_mfma_f32_16x16x32_bf16 v[224:227], v[56:59], a[92:95], v[224:227]\n"
  "v_mfma_f32_16x16x32_bf16 a[0:3], v[56:59], a[96:99], a[0:3]\n"
  "v_xor_b32 v12, 0x5080, v11\n"
  "v_mfma_f32_16x16x32_bf16 a[32:35], v[56:59], a[100:103], a[32:35]\n"
  "v_mfma_f32_16x16x32_bf16 v[164:167], v[60:63], a[84:87], v[164:167]\n"
  "v_perm_b32 v56, v133, v129, s69\n"
  "v_perm_b32 v57, v141, v137, s69\n"
  "v_mfma_f32_16x16x32_bf16 v[196:199], v[60:63], a[88:91], v[196:199]\n"
  "v_perm_b32 v58, v149, v145, s69\n"
  "v_perm_b32 v59, v157, v153, s69\n"
  "v_mfma_f32_16x16x32_bf16 v[228:231], v[60:63], a[92:95], v[228:231]\n"
  "v_mfma_f32_16x16x32_bf16 a[4:7], v[60:63], a[96:99], a[4:7]\n"
  "ds_read_b128 a[64:67], v12\n"
  "v_mfma_f32_16x16x32_bf16 a[36:39], v[60:63], a[100:103], a[36:39]\n"
  "v_mfma_f32_16x16x32_bf16 v[168:171], v[56:59], a[84:87], v[168:171]\n"
  "v_perm_b32 v60, v133, v129, s70\n"
  "v_perm_b32 v61, v141, v137, s70\n"
  "v_mfma_f32_16x16x32_bf16 v[200:203], v[56:59], a[88:91], v[200:203]\n"
  "v_perm_b32 v62, v149, v145, s70\n"
  "v_perm_b32 v63, v157, v153, s70\n"
  "v_mfma_f32_16x16x32_bf16 v[232:235], v[56:59], a[92:95], v[232:235]\n"
  "v_mfma_f32_16x16x32_bf16 a[8:11], v[56:59], a[96:99], a[8:11]\n"
  "ds_read_b128 a[68:71], v12 offset:4096\n"
  "v_mfma_f32_16x16x32_bf16 a[40:43], v[56:59], a[100:103], a[40:43]\n"
  "v_mfma_f32_16x16x32_bf16 v[172:175], v[60:63], a[84:87], v[172:175]\n"
  "v_perm_b32 v56, v134, v130, s69\n"
  "v_perm_b32 v57, v142, v138, s69\n"
  "v_mfma_f32_16x16x32_bf16 v[204:207], v[60:63], a[88:91], v[204:207]\n"
  "v_perm_b32 v58, v150, v146, s69\n"
  "v_perm_b32 v59, v158, v154, s69\n"
  "v_mfma_f32_16x16x32_bf16 v[236:239], v[60:63], a[92:95], v[236:239]\n"
  "v_mfma_f32_16x16x32_bf16 a[12:15], v[60:63], a[96:99], a[12:15]\n"
  "ds_read_b128 a[72:75], v12 offset:8192\n"
  "v_mfma_f32_16x16x32_bf16 a[44:47], v[60:63], a[100:103], a[44:47]\n"
  "v_mfma_f32_16x16x32_bf16 v[176:179], v[56:59], a[84:87], v[176:179]\n"
  "v_perm_b32 v60, v134, v130, s70\n"
  "v_perm_b32 v61, v142, v138, s70\n"
  "v_mfma_f32_16x16x32_bf16 v[208:211], v[56:59], a[88:91], v[208:211]\n"
  "v_perm_b32 v62, v150, v146, s70\n"
  "v_perm_b32 v63, v158, v154, s70\n"
  "v_mfma_f32_16x16x32_bf16 v[240:243], v[56:59], a[92:95], v[240:243]\n"
  "s_xor_b32 s72, s67, 0x1c0000\n"
  "buffer_load_dwordx4 v[96:99], v2, s[44:47], s72 offen\n"
  "v_mfma_f32_16x16x32_bf16 a[16:19], v[56:59], a[96:99], a[16:19]\n"
  "ds_read_b128 a[76:79], v12 offset:12288\n"
  "v_mfma_f32_16x16x32_bf16 a[48:51], v[56:59], a[100:103], a[48:51]\n"
  "buffer_load_dwordx4 v[100:103], v3, s[44:47], s72 offen\n"
  "v_mfma_f32_16x16x32_bf16 v[180:183], v[60:63], a[84:87], v[180:183]\n"
  "v_perm_b32 v56, v135, v131, s69\n"
  "v_perm_b32 v57, v143, v139, s69\n"
  "v_mfma_f32_16x16x32_bf16 v[212:215], v[60:63], a[88:91], v[212:215]\n"
  "v_perm_b32 v58, v151, v147, s69\n"
  "v_perm_b32 v59, v159, v155, s69\n"
  "buffer_load_dwordx4 v[104:107], v4, s[44:47], s72 offen\n"
  "v_mfma_f32_16x16x32_bf16 v[244:247], v[60:63], a[92:95], v[244:247]\n"
  "v_mfma_f32_16x16x32_bf16 a[20:23], v[60:63], a[96:99], a[20:23]\n"
  "buffer_load_dwordx4 v[108:111], v5, s[44:47], s72 offen\n"
  "ds_read_b128 a[80:83], v12 offset:16384\n"
  "v_mfma_f32_16x16x32_bf16 a[52:55], v[60:63], a[100:103], a[52:55]\n"
  "v_mfma_f32_16x16x32_bf16 v[184:187], v[56:59], a[84:87], v[184:187]\n"
  "v_perm_b32 v60, v135, v131, s70\n"
  "v_perm_b32 v61, v143, v139, s70\n"
  "buffer_load_dwordx4 v[112:115], v6, s[44:47], s72 offen\n"
  "v_mfma_f32_16x16x32_bf16 v[216:219], v[56:59], a[88:91], v[216:219]\n"
  "v_perm_b32 v62, v151, v147, s70\n"
  "v_perm_b32 v63, v159, v155, s70\n"
  "v_mfma_f32_16x16x32_bf16 v[248:251], v[56:59], a[92:95], v[248:251]\n"
  "buffer_load_dwordx4 v[116:119], v7, s[44:47], s72 offen\n"
  "v_mfma_f32_16x16x32_bf16 a[24:27], v[56:59], a[96:99], a[24:27]\n"
  "v_mfma_f32_16x16x32_bf16 a[56:59], v[56:59], a[100:103], a[56:59]\n"
  "s_waitcnt vmcnt(14)\n"
  "buffer_load_dwordx4 v[120:123], v8, s[44:47], s72 offen\n"
  "v_mfma_f32_16x16x32_bf16 v[188:191], v[60:63], a[84:87], v[188:191]\n"
  "v_perm_b32 v56, v68, v64, s69\n"
  "v_perm_b32 v57, v76, v72, s69\n"
  "v_mfma_f32_16x16x32_bf16 v[220:223], v[60:63], a[88:91], v[220:223]\n"
  "v_perm_b32 v58, v84, v80, s69\n"
  "v_perm_b32 v59, v92, v88, s69\n"
  "buffer_load_dwordx4 v[124:127], v9, s[44:47], s72 offen\n"
  "v_mfma_f32_16x16x32_bf16 v[252:255], v[60:63], a[92:95], v[252:255]\n"
  "v_mfma_f32_16x16x32_bf16 a[28:31], v[60:63], a[96:99], a[28:31]\n"
  "s_waitcnt lgkmcnt(0)\n"
  "v_mfma_f32_16x16x32_bf16 a[60:63], v[60:63], a[100:103], a[60:63]\n"
  "v_mfma_f32_16x16x32_bf16 v[160:163], v[56:59], a[64:67], v[160:163]\n"
  "v_perm_b32 v60, v68, v64, s70\n"
  "v_perm_b32 v61, v76, v72, s70\n"
  "s_xor_b32 s72, s67, 0x200000\n"
  "buffer_load_dwordx4 v[128:131], v2, s[44:47], s72 offen\n"
  "v_mfma_f32_16x16x32_bf16 v[192:195], v[56:59], a[68:71], v[192:195]\n"
  "v_perm_b32 v62, v84, v80, s70\n"
  "v_perm_b32 v63, v92, v88, s70\n"
  "v_mfma_f32_16x16x32_bf16 v[224:227], v[56:59], a[72:75], v[224:227]\n"
  "buffer_load_dwordx4 v[132:135], v3, s[44:47], s72 offen\n"
  "v_mfma_f32_16x16x32_bf16 a[0:3], v[56:59], a[76:79], a[0:3]\n"
  "v_xor_b32 v12, 0x50c0, v11\n"
  "v_mfma_f32_16x16x32_bf16 a[32:35], v[56:59], a[80:83], a[32:35]\n"
  "buffer_load_dwordx4 v[136:139], v4, s[44:47], s72 offen\n"
  "v_mfma_f32_16x16x32_bf16 v[164:167], v[60:63], a[64:67], v[164:167]\n"
  "v_perm_b32 v56, v69, v65, s69\n"
  "v_perm_b32 v57, v77, v73, s69\n"
  "v_mfma_f32_16x16x32_bf16 v[196:199], v[60:63], a[68:71], v[196:199]\n"
  "v_perm_b32 v58, v85, v81, s69\n"
  "v_perm_b32 v59, v93, v89, s69\n"
  "buffer_load_dwordx4 v[140:143], v5, s[44:47], s72 offen\n"
  "v_mfma_f32_16x16x32_bf16 v[228:231], v[60:63], a[72:75], v[228:231]\n"
  "v_mfma_f32_16x16x32_bf16 a[4:7], v[60:63], a[76:79], a[4:7]\n"
  "buffer_load_dwordx4 v[144:147], v6, s[44:47], s72 offen\n"
  "ds_read_b128 a[84:87], v12\n"
  "v_mfma_f32_16x16x32_bf16 a[36:39], v[60:63], a[80:83], a[36:39]\n"
  "v_mfma_f32_16x16x32_bf16 v[168:171], v[56:59], a[64:67], v[168:171]\n"
  "v_perm_b32 v60, v69, v65, s70\n"
  "v_perm_b32 v61, v77, v73, s70\n"
  "buffer_load_dwordx4 v[148:151], v7, s[44:47], s72 offen\n"
  "v_mfma_f32_16x16x32_bf16 v[200:203], v[56:59], a[68:71], v[200:203]\n"
  "v_perm_b32 v62, v85, v81, s70\n"
  "v_perm_b32 v63, v93, v89, s70\n"
  "v_mfma_f32_16x16x32_bf16 v[232:235], v[56:59], a[72:75], v[232:235]\n"
  "buffer_load_dwordx4 v[152:155], v8, s[44:47], s72 offen\n"
  "v_mfma_f32_16x16x32_bf16 a[8:11], v[56:59], a[76:79], a[8:11]\n"
  "ds_read_b128 a[88:91], v12 offset:4096\n"
  "v_mfma_f32_16x16x32_bf16 a[40:43], v[56:59], a[80:83], a[40:43]\n"
  "buffer_load_dwordx4 v[156:159], v9, s[44:47], s72 offen\n"
  "v_mfma_f32_16x16x32_bf16 v[172:175], v[60:63], a[64:67], v[172:175]\n"
  "v_perm_b32 v56, v70, v66, s69\n"
  "v_perm_b32 v57, v78, v74, s69\n"
  "v_mfma_f32_16x16x32_bf16 v[204:207], v[60:63], a[68:71], v[204:207]\n"
  "v_perm_b32 v58, v86, v82, s69\n"
  "v_perm_b32 v59, v94, v90, s69\n"
  "v_mfma_f32_16x16x32_bf16 v[236:239], v[60:63], a[72:75], v[236:239]\n"
  "v_mfma_f32_16x16x32_bf16 a[12:15], v[60:63], a[76:79], a[12:15]\n"
  "ds_read_b128 a[92:95], v12 offset:8192\n"
  "v_mfma_f32_16x16x32_bf16 a[44:47], v[60:63], a[80:83], a[44:47]\n"
  "v_mfma_f32_16x16x32_bf16 v[176:179], v[56:59], a[64:67], v[176:179]\n"
  "v_perm_b32 v60, v70, v66, s70\n"
  "v_perm_b32 v61, v78, v74, s70\n"
  "v_mfma_f32_16x16x32_bf16 v[208:211], v[56:59], a[68:71], v[208:211]\n"
  "v_perm_b32 v62, v86, v82, s70\n"
  "v_perm_b32 v63, v94, v90, s70\n"
  "v_mfma_f32_16x16x32_bf16 v[240:243], v[56:59], a[72:75], v[240:243]\n"
  "v_mfma_f32_16x16x32_bf16 a[16:19], v[56:59], a[76:79], a[16:19]\n"
  "ds_read_b128 a[96:99], v12 offset:12288\n"
  "v_mfma_f32_16x16x32_bf16 a[48:51], v[56:59], a[80:83], a[48:51]\n"
  "v_mfma_f32_16x16x32_bf16 v[180:183], v[60:63], a[64:67], v[180:183]\n"
  "v_perm_b32 v56, v71, v67, s69\n"
  "v_perm_b32 v57, v79, v75, s69\n"
  "v_mfma_f32_16x16x32_bf16 v[212:215], v[60:63], a[68:71], v[212:215]\n"
  "v_perm_b32 v58, v87, v83, s69\n"
  "v_perm_b32 v59, v95, v91, s69\n"
  "v_mfma_f32_16x16x32_bf16 v[244:247], v[60:63], a[72:75], v[244:247]\n"
  "v_mfma_f32_16x16x32_bf16 a[20:23], v[60:63], a[76:79], a[20:23]\n"
  "ds_read_b128 a[100:103], v12 offset:16384\n"
  "v_mfma_f32_16x16x32_bf16 a[52:55], v[60:63], a[80:83], a[52:55]\n"
  "v_mfma_f32_16x16x32_bf16 v[184:187], v[56:59], a[64:67], v[184:187]\n"
  "v_perm_b32 v60, v71, v67, s70\n"
  "v_perm_b32 v61, v79, v75, s70\n"
  "v_mfma_f32_16x16x32_bf16 v[216:219], v[56:59], a[68:71], v[216:219]\n"
  "v_perm_b32 v62, v87, v83, s70\n"
  "v_perm_b32 v63, v95, v91, s70\n"
  "v_mfma_f32_16x16x32_bf16 v[248:251], v[56:59], a[72:75], v[248:251]\n"
  "s_xor_b32 s72, s67, 0x240000\n"
  "buffer_load_dwordx4 v[64:67], v2, s[44:47], s72 offen\n"
  "v_mfma_f32_16x16x32_bf16 a[24:27], v[56:59], a[76:79], a[24:27]\n"
  "buffer_load_dwordx4 v[68:71], v3, s[44:47], s72 offen\n"
  "v_mfma_f32_16x16x32_bf16 a[56:59], v[56:59], a[80:83], a[56:59]\n"
  "s_waitcnt vmcnt(10)\n"
  "buffer_load_dwordx4 v[72:75], v4, s[44:47], s72 offen\n"
  "v_mfma_f32_16x16x32_bf16 v[188:191], v[60:63], a[64:67], v[188:191]\n"
  "v_perm_b32 v56, v100, v96, s69\n"
  "v_perm_b32 v57, v108, v104, s69\n"
  "buffer_load_dwordx4 v[76:79], v5, s[44:47], s72 offen\n"
  "v_mfma_f32_16x16x32_bf16 v[220:223], v[60:63], a[68:71], v[220:223]\n"
  "v_perm_b32 v58, v116, v112, s69\n"
  "v_perm_b32 v59, v124, v120, s69\n"
  "buffer_load_dwordx4 v[80:83], v6, s[44:47], s72 offen\n"
  "v_mfma_f32_16x16x32_bf16 v[252:255], v[60:63], a[72:75], v[252:255]\n"
  "buffer_load_dwordx4 v[84:87], v7, s[44:47], s72 offen\n"
  "v_mfma_f32_16x16x32_bf16 a[28:31], v[60:63], a[76:79], a[28:31]\n"
  "s_waitcnt lgkmcnt(0)\n"
  "buffer_load_dwordx4 v[88:91], v8, s[44:47], s72 offen\n"
  "v_mfma_f32_16x16x32_bf16 a[60:63], v[60:63], a[80:83], a[60:63]\n"
  "buffer_load_dwordx4 v[92:95], v9, s[44:47], s72 offen\n"
  "v_mfma_f32_16x16x32_bf16 v[160:163], v[56:59], a[84:87], v[160:163]\n"
  "v_perm_b32 v60, v100, v96, s70\n"
  "v_perm_b32 v61, v108, v104, s70\n"
  "v_mfma_f32_16x16x32_bf16 v[192:195], v[56:59], a[88:91], v[192:195]\n"
  "v_perm_b32 v62, v116, v112, s70\n"
  "v_perm_b32 v63, v124, v120, s70\n"
  "v_mfma_f32_16x16x32_bf16 v[224:227], v[56:59], a[92:95], v[224:227]\n"
  "s_add_u32 m0, s96, 0xf000\n"
  "s_xor_b32 s73, s68, 0x300\n"
  "buffer_load_dwordx4 v10, s[32:35], s73 offen lds\n"
  "v_mfma_f32_16x16x32_bf16 a[0:3], v[56:59], a[96:99], a[0:3]\n"
  "v_xor_b32 v12, 0xa000, v11\n"
  "v_mfma_f32_16x16x32_bf16 a[32:35], v[56:59], a[100:103], a[32:35]\n"
  "s_barrier\n"
  "s_add_u32 m0, s96, 0x10000\n"
  "s_xor_b32 s73, s68, 0x4300\n"
  "buffer_load_dwordx4 v10, s[32:35], s73 offen lds\n"
  "v_mfma_f32_16x16x32_bf16 v[164:167], v[60:63], a[84:87], v[164:167]\n"
  "v_perm_b32 v56, v101, v97, s69\n"
  "v_perm_b32 v57, v109, v105, s69\n"
  "s_add_u32 m0, s96, 0x11000\n"
  "s_xor_b32 s73, s68, 0x8300\n"
  "buffer_load_dwordx4 v10, s[32:35], s73 offen lds\n"
  "v_mfma_f32_16x16x32_bf16 v[196:199], v[60:63], a[88:91], v[196:199]\n"
  "v_perm_b32 v58, v117, v113, s69\n"
  "v_perm_b32 v59, v125, v121, s69\n"
  "v_mfma_f32_16x16x32_bf16 v[228:231], v[60:63], a[92:95], v[228:231]\n"
  "s_add_u32 m0, s96, 0x12000\n"
  "s_xor_b32 s73, s68, 0xc300\n"
  "buffer_load_dwordx4 v10, s[32:35], s73 offen lds\n"
  "v_mfma_f32_16x16x32_bf16 a[4:7], v[60:63], a[96:99], a[4:7]\n"
  "ds_read_b128 a[64:67], v12\n"
  "v_mfma_f32_16x16x32_bf16 a[36:39], v[60:63], a[100:103], a[36:39]\n"
  "s_add_u32 m0, s96, 0x13000\n"
  "s_xor_b32 s73, s68, 0x10300\n"
  "buffer_load_dwordx4 v10, s[32:35], s73 offen lds\n"
  "v_mfma_f32_16x16x32_bf16 v[168:171], v[56:59], a[84:87], v[168:171]\n"
  "v_perm_b32 v60, v101, v97, s70\n"
  "v_perm_b32 v61, v109, v105, s70\n"
  "s_add_u32 m0, s77, 0x2000\n"
  "s_nop 0\n"
  "buffer_load_dwordx4 v15, s[36:39], s93 offen offset:2048 sc1 nt lds\n"
  "v_mfma_f32_16x16x32_bf16 v[200:203], v[56:59], a[88:91], v[200:203]\n"
  "v_perm_b32 v62, v117, v113, s70\n"
  "v_perm_b32 v63, v125, v121, s70\n"
  "v_mfma_f32_16x16x32_bf16 v[232:235], v[56:59], a[92:95], v[232:235]\n"
  "buffer_load_dwordx4 v16, s[36:39], s93 offen offset:3072 sc1 nt lds\n"
  "v_mfma_f32_16x16x32_bf16 a[8:11], v[56:59], a[96:99], a[8:11]\n"
  "ds_read_b128 a[68:71], v12 offset:4096\n"
  "v_mfma_f32_16x16x32_bf16 a[40:43], v[56:59], a[100:103], a[40:43]\n"
  "s_add_u32 m0, s77, 0x3000\n"
  "s_nop 0\n"
  "buffer_load_dwordx4 v13, s[36:39], s94 offen sc1 nt lds\n"
  "v_mfma_f32_16x16x32_bf16 v[172:175], v[60:63], a[84:87], v[172:175]\n"
  "v_perm_b32 v56, v102, v98, s69\n"
  "v_perm_b32 v57, v110, v106, s69\n"
  "v_mfma_f32_16x16x32_bf16 v[204:207], v[60:63], a[88:91], v[204:207]\n"
  "v_perm_b32 v58, v118, v114, s69\n"
  "v_perm_b32 v59, v126, v122, s69\n"
  "v_mfma_f32_16x16x32_bf16 v[236:239], v[60:63], a[92:95], v[236:239]\n"
  "v_mfma_f32_16x16x32_bf16 a[12:15], v[60:63], a[96:99], a[12:15]\n"
  "ds_read_b128 a[72:75], v12 offset:8192\n"
  "v_mfma_f32_16x16x32_bf16 a[44:47], v[60:63], a[100:103], a[44:47]\n"
  "v_mfma_f32_16x16x32_bf16 v[176:179], v[56:59], a[84:87], v[176:179]\n"
  "v_perm_b32 v60, v102, v98, s70\n"
  "v_perm_b32 v61, v110, v106, s70\n"
  "v_mfma_f32_16x16x32_bf16 v[208:211], v[56:59], a[88:91], v[208:211]\n"
  "v_perm_b32 v62, v118, v114, s70\n"
  "v_perm_b32 v63, v126, v122, s70\n"
  "v_mfma_f32_16x16x32_bf16 v[240:243], v[56:59], a[92:95], v[240:243]\n"
  "v_mfma_f32_16x16x32_bf16 a[16:19], v[56:59], a[96:99], a[16:19]\n"
  "ds_read_b128 a[76:79], v12 offset:12288\n"
  "v_mfma_f32_16x16x32_bf16 a[48:51], v[56:59], a[100:103], a[48:51]\n"
  "v_mfma_f32_16x16x32_bf16 v[180:183], v[60:63], a[84:87], v[180:183]\n"
  "v_perm_b32 v56, v103, v99, s69\n"
  "v_perm_b32 v57, v111, v107, s69\n"
  "v_mfma_f32_16x16x32_bf16 v[212:215], v[60:63], a[88:91], v[212:215]\n"
  "v_perm_b32 v58, v119, v115, s69\n"
  "v_perm_b32 v59, v127, v123, s69\n"
  "v_mfma_f32_16x16x32_bf16 v[244:247], v[60:63], a[92:95], v[244:247]\n"
  "v_mfma_f32_16x16x32_bf16 a[20:23], v[60:63], a[96:99], a[20:23]\n"
  "ds_read_b128 a[80:83], v12 offset:16384\n"
  "v_mfma_f32_16x16x32_bf16 a[52:55], v[60:63], a[100:103], a[52:55]\n"
  "v_mfma_f32_16x16x32_bf16 v[184:187], v[56:59], a[84:87], v[184:187]\n"
  "v_perm_b32 v60, v103, v99, s70\n"
  "v_perm_b32 v61, v111, v107, s70\n"
  "v_mfma_f32_16x16x32_bf16 v[216:219], v[56:59], a[88:91], v[216:219]\n"
  "v_perm_b32 v62, v119, v115, s70\n"
  "v_perm_b32 v63, v127, v123, s70\n"
  "v_mfma_f32_16x16x32_bf16 v[248:251], v[56:59], a[92:95], v[248:251]\n"
  "v_mfma_f32_16x16x32_bf16 a[24:27], v[56:59], a[96:99], a[24:27]\n"
  "v_mfma_f32_16x16x32_bf16 a[56:59], v[56:59], a[100:103], a[56:59]\n"
  "s_waitcnt vmcnt(16)\n"
  "v_mfma_f32_16x16x32_bf16 v[188:191], v[60:63], a[84:87], v[188:191]\n"
  "v_perm_b32 v56, v132, v128, s69\n"
  "v_perm_b32 v57, v140, v136, s69\n"
  "v_mfma_f32_16x16x32_bf16 v[220:223], v[60:63], a[88:91], v[220:223]\n"
  "v_perm_b32 v58, v148, v144, s69\n"
  "v_perm_b32 v59, v156, v152, s69\n"
  "v_mfma_f32_16x16x32_bf16 v[252:255], v[60:63], a[92:95], v[252:255]\n"
  "v_mfma_f32_16x16x32_bf16 a[28:31], v[60:63], a[96:99], a[28:31]\n"
  "s_waitcnt lgkmcnt(0)\n"
  "v_mfma_f32_16x16x32_bf16 a[60:63], v[60:63], a[100:103], a[60:63]\n"
  "v_mfma_f32_16x16x32_bf16 v[160:163], v[56:59], a[64:67], v[160:163]\n"
  "v_perm_b32 v60, v132, v128, s70\n"
  "v_perm_b32 v61, v140, v136, s70\n"
  "v_mfma_f32_16x16x32_bf16 v[192:195], v[56:59], a[68:71], v[192:195]\n"
  "v_perm_b32 v62, v148, v144, s70\n"
  "v_perm_b32 v63, v156, v152, s70\n"
  "v_mfma_f32_16x16x32_bf16 v[224:227], v[56:59], a[72:75], v[224:227]\n"
  "v_mfma_f32_16x16x32_bf16 a[0:3], v[56:59], a[76:79], a[0:3]\n"
  "v_xor_b32 v12, 0xa040, v11\n"
  "v_mfma_f32_16x16x32_bf16 a[32:35], v[56:59], a[80:83], a[32:35]\n"
  "v_mfma_f32_16x16x32_bf16 v[164:167], v[60:63], a[64:67], v[164:167]\n"
  "v_perm_b32 v56, v133, v129, s69\n"
  "v_perm_b32 v57, v141, v137, s69\n"
  "v_mfma_f32_16x16x32_bf16 v[196:199], v[60:63], a[68:71], v[196:199]\n"
  "v_perm_b32 v58, v149, v145, s69\n"
  "v_perm_b32 v59, v157, v153, s69\n"
  "v_mfma_f32_16x16x32_bf16 v[228:231], v[60:63], a[72:75], v[228:231]\n"
  "v_mfma_f32_16x16x32_bf16 a[4:7], v[60:63], a[76:79], a[4:7]\n"
  "ds_read_b128 a[84:87], v12\n"
  "v_mfma_f32_16x16x32_bf16 a[36:39], v[60:63], a[80:83], a[36:39]\n"
  "v_mfma_f32_16x16x32_bf16 v[168:171], v[56:59], a[64:67], v[168:171]\n"
  "v_perm_b32 v60, v133, v129, s70\n"
  "v_perm_b32 v61, v141, v137, s70\n"
  "v_mfma_f32_16x16x32_bf16 v[200:203], v[56:59], a[68:71], v[200:203]\n"
  "v_perm_b32 v62, v149, v145, s70\n"
  "v_perm_b32 v63, v157, v153, s70\n"
  "v_mfma_f32_16x16x32_bf16 v[232:235], v[56:59], a[72:75], v[232:235]\n"
  "v_mfma_f32_16x16x32_bf16 a[8:11], v[56:59], a[76:79], a[8:11]\n"
  "ds_read_b128 a[88:91], v12 offset:4096\n"
  "v_mfma_f32_16x16x32_bf16 a[40:43], v[56:59], a[80:83], a[40:43]\n"
  "v_mfma_f32_16x16x32_bf16 v[172:175], v[60:63], a[64:67], v[172:175]\n"
  "v_perm_b32 v56, v134, v130, s69\n"
  "v_perm_b32 v57, v142, v138, s69\n"
  "v_mfma_f32_16x16x32_bf16 v[204:207], v[60:63], a[68:71], v[204:207]\n"
  "v_perm_b32 v58, v150, v146, s69\n"
  "v_perm_b32 v59, v158, v154, s69\n"
  "v_mfma_f32_16x16x32_bf16 v[236:239], v[60:63], a[72:75], v[236:239]\n"
  "v_mfma_f32_16x16x32_bf16 a[12:15], v[60:63], a[76:79], a[12:15]\n"
  "ds_read_b128 a[92:95], v12 offset:8192\n"
  "v_mfma_f32_16x16x32_bf16 a[44:47], v[60:63], a[80:83], a[44:47]\n"
  "v_mfma_f32_16x16x32_bf16 v[176:179], v[56:59], a[64:67], v[176:179]\n"
  "v_perm_b32 v60, v134, v130, s70\n"
  "v_perm_b32 v61, v142, v138, s70\n"
  "v_mfma_f32_16x16x32_bf16 v[208:211], v[56:59], a[68:71], v[208:211]\n"
  "v_perm_b32 v62, v150, v146, s70\n"
  "v_perm_b32 v63, v158, v154, s70\n"
  "v_mfma_f32_16x16x32_bf16 v[240:243], v[56:59], a[72:75], v[240:243]\n"
  "s_xor_b32 s72, s67, 0x280000\n"
  "buffer_load_dwordx4 v[96:99], v2, s[44:47], s72 offen\n"
  "v_mfma_f32_16x16x32_bf16 a[16:19], v[56:59], a[76:79], a[16:19]\n"
  "ds_read_b128 a[96:99], v12 offset:12288\n"
  "v_mfma_f32_16x16x32_bf16 a[48:51], v[56:59], a[80:83], a[48:51]\n"
  "buffer_load_dwordx4 v[100:103], v3, s[44:47], s72 offen\n"
  "v_mfma_f32_16x16x32_bf16 v[180:183], v[60:63], a[64:67], v[180:183]\n"
  "v_perm_b32 v56, v135, v131, s69\n"
  "v_perm_b32 v57, v143, v139, s69\n"
  "v_mfma_f32_16x16x32_bf16 v[212:215], v[60:63], a[68:71], v[212:215]\n"
  "v_perm_b32 v58, v151, v147, s69\n"
  "v_perm_b32 v59, v159, v155, s69\n"
  "buffer_load_dwordx4 v[104:107], v4, s[44:47], s72 offen\n"
  "v_mfma_f32_16x16x32_bf16 v[244:247], v[60:63], a[72:75], v[244:247]\n"
  "v_mfma_f32_16x16x32_bf16 a[20:23], v[60:63], a[76:79], a[20:23]\n"
  "buffer_load_dwordx4 v[108:111], v5, s[44:47], s72 offen\n"
  "ds_read_b128 a[100:103], v12 offset:16384\n"
  "v_mfma_f32_16x16x32_bf16 a[52:55], v[60:63], a[80:83], a[52:55]\n"
  "v_mfma_f32_16x16x32_bf16 v[184:187], v[56:59], a[64:67], v[184:187]\n"
  "v_perm_b32 v60, v135, v131, s70\n"
  "v_perm_b32 v61, v143, v139, s70\n"
  "buffer_load_dwordx4 v[112:115], v6, s[44:47], s72 offen\n"
  "v_mfma_f32_16x16x32_bf16 v[216:219], v[56:59], a[68:71], v[216:219]\n"
  "v_perm_b32 v62, v151, v147, s70\n"
  "v_perm_b32 v63, v159, v155, s70\n"
  "v_mfma_f32_16x16x32_bf16 v[248:251], v[56:59], a[72:75], v[248:251]\n"
  "buffer_load_dwordx4 v[116:119], v7, s[44:47], s72 offen\n"
  "v_mfma_f32_16x16x32_bf16 a[24:27], v[56:59], a[76:79], a[24:27]\n"
  "v_mfma_f32_16x16x32_bf16 a[56:59], v[56:59], a[80:83], a[56:59]\n"
  "s_waitcnt vmcnt(14)\n"
  "buffer_load_dwordx4 v[120:123], v8, s[44:47], s72 offen\n"
  "v_mfma_f32_16x16x32_bf16 v[188:191], v[60:63], a[64:67], v[188:191]\n"
  "v_perm_b32 v56, v68, v64, s69\n"
  "v_perm_b32 v57, v76, v72, s69\n"
  "v_mfma_f32_16x16x32_bf16 v[220:223], v[60:63], a[68:71], v[220:223]\n"
  "v_perm_b32 v58, v84, v80, s69\n"
  "v_perm_b32 v59, v92, v88, s69\n"
  "buffer_load_dwordx4 v[124:127], v9, s[44:47], s72 offen\n"
  "v_mfma_f32_16x16x32_bf16 v[252:255], v[60:63], a[72:75], v[252:255]\n"
  "v_mfma_f32_16x16x32_bf16 a[28:31], v[60:63], a[76:79], a[28:31]\n"
  "s_waitcnt lgkmcnt(0)\n"
  "v_mfma_f32_16x16x32_bf16 a[60:63], v[60:63], a[80:83], a[60:63]\n"
  "v_mfma_f32_16x16x32_bf16 v[160:163], v[56:59], a[84:87], v[160:163]\n"
  "v_perm_b32 v60, v68, v64, s70\n"
  "v_perm_b32 v61, v76, v72, s70\n"
  "s_xor_b32 s72, s67, 0x2c0000\n"
  "buffer_load_dwordx4 v[128:131], v2, s[44:47], s72 offen\n"
  "v_mfma_f32_16x16x32_bf16 v[192:195], v[56:59], a[88:91], v[192:195]\n"
  "v_perm_b32 v62, v84, v80, s70\n"
  "v_perm_b32 v63, v92, v88, s70\n"
  "v_mfma_f32_16x16x32_bf16 v[224:227], v[56:59], a[92:95], v[224:227]\n"
  "buffer_load_dwordx4 v[132:135], v3, s[44:47], s72 offen\n"
  "v_mfma_f32_16x16x32_bf16 a[0:3], v[56:59], a[96:99], a[0:3]\n"
  "v_xor_b32 v12, 0xa080, v11\n"
  "v_mfma_f32_16x16x32_bf16 a[32:35], v[56:59], a[100:103], a[32:35]\n"
  "buffer_load_dwordx4 v[136:139], v4, s[44:47], s72 offen\n"
  "v_mfma_f32_16x16x32_bf16 v[164:167], v[60:63], a[84:87], v[164:167]\n"
  "v_perm_b32 v56, v69, v65, s69\n"
  "v_perm_b32 v57, v77, v73, s69\n"
  "v_mfma_f32_16x16x32_bf16 v[196:199], v[60:63], a[88:91], v[196:199]\n"
  "v_perm_b32 v58, v85, v81, s69\n"
  "v_perm_b32 v59, v93, v89, s69\n"
  "buffer_load_dwordx4 v[140:143], v5, s[44:47], s72 offen\n"
  "v_mfma_f32_16x16x32_bf16 v[228:231], v[60:63], a[92:95], v[228:231]\n"
  "v_mfma_f32_16x16x32_bf16 a[4:7], v[60:63], a[96:99], a[4:7]\n"
  "buffer_load_dwordx4 v[144:147], v6, s[44:47], s72 offen\n"
  "ds_read_b128 a[64:67], v12\n"
  "v_mfma_f32_16x16x32_bf16 a[36:39], v[60:63], a[100:103], a[36:39]\n"
  "v_mfma_f32_16x16x32_bf16 v[168:171], v[56:59], a[84:87], v[168:171]\n"
  "v_perm_b32 v60, v69, v65, s70\n"
  "v_perm_b32 v61, v77, v73, s70\n"
  "buffer_load_dwordx4 v[148:151], v7, s[44:47], s72 offen\n"
  "v_mfma_f32_16x16x32_bf16 v[200:203], v[56:59], a[88:91], v[200:203]\n"
  "v_perm_b32 v62, v85, v81, s70\n"
  "v_perm_b32 v63, v93, v89, s70\n"
  "v_mfma_f32_16x16x32_bf16 v[232:235], v[56:59], a[92:95], v[232:235]\n"
  "buffer_load_dwordx4 v[152:155], v8, s[44:47], s72 offen\n"
  "v_mfma_f32_16x16x32_bf16 a[8:11], v[56:59], a[96:99], a[8:11]\n"
  "ds_read_b128 a[68:71], v12 offset:4096\n"
  "v_mfma_f32_16x16x32_bf16 a[40:43], v[56:59], a[100:103], a[40:43]\n"
  "buffer_load_dwordx4 v[156:159], v9, s[44:47], s72 offen\n"
  "v_mfma_f32_16x16x32_bf16 v[172:175], v[60:63], a[84:87], v[172:175]\n"
  "v_perm_b32 v56, v70, v66, s69\n"
  "v_perm_b32 v57, v78, v74, s69\n"
  "v_mfma_f32_16x16x32_bf16 v[204:207], v[60:63], a[88:91], v[204:207]\n"
  "v_perm_b32 v58, v86, v82, s69\n"
  "v_perm_b32 v59, v94, v90, s69\n"
  "v_mfma_f32_16x16x32_bf16 v[236:239], v[60:63], a[92:95], v[236:239]\n"
  "v_mfma_f32_16x16x32_bf16 a[12:15], v[60:63], a[96:99], a[12:15]\n"
  "ds_read_b128 a[72:75], v12 offset:8192\n"
  "v_mfma_f32_16x16x32_bf16 a[44:47], v[60:63], a[100:103], a[44:47]\n"
  "v_mfma_f32_16x16x32_bf16 v[176:179], v[56:59], a[84:87], v[176:179]\n"
  "v_perm_b32 v60, v70, v66, s70\n"
  "v_perm_b32 v61, v78, v74, s70\n"
  "v_mfma_f32_16x16x32_bf16 v[208:211], v[56:59], a[88:91], v[208:211]\n"
  "v_perm_b32 v62, v86, v82, s70\n"
  "v_perm_b32 v63, v94, v90, s70\n"
  "v_mfma_f32_16x16x32_bf16 v[240:243], v[56:59], a[92:95], v[240:243]\n"
  "v_mfma_f32_16x16x32_bf16 a[16:19], v[56:59], a[96:99], a[16:19]\n"
  "ds_read_b128 a[76:79], v12 offset:12288\n"
  "v_mfma_f32_16x16x32_bf16 a[48:51], v[56:59], a[100:103], a[48:51]\n"
  "v_mfma_f32_16x16x32_bf16 v[180:183], v[60:63], a[84:87], v[180:183]\n"
  "v_perm_b32 v56, v71, v67, s69\n"
  "v_perm_b32 v57, v79, v75, s69\n"
  "v_mfma_f32_16x16x32_bf16 v[212:215], v[60:63], a[88:91], v[212:215]\n"
  "v_perm_b32 v58, v87, v83, s69\n"
  "v_perm_b32 v59, v95, v91, s69\n"
  "v_mfma_f32_16x16x32_bf16 v[244:247], v[60:63], a[92:95], v[244:247]\n"
  "v_mfma_f32_16x16x32_bf16 a[20:23], v[60:63], a[96:99], a[20:23]\n"
  "ds_read_b128 a[80:83], v12 offset:16384\n"
  "v_mfma_f32_16x16x32_bf16 a[52:55], v[60:63], a[100:103], a[52:55]\n"
  "v_mfma_f32_16x16x32_bf16 v[184:187], v[56:59], a[84:87], v[184:187]\n"
  "v_perm_b32 v60, v71, v67, s70\n"
  "v_perm_b32 v61, v79, v75, s70\n"
  "v_mfma_f32_16x16x32_bf16 v[216:219], v[56:59], a[88:91], v[216:219]\n"
  "v_perm_b32 v62, v87, v83, s70\n"
  "v_perm_b32 v63, v95, v91, s70\n"
  "v_mfma_f32_16x16x32_bf16 v[248:251], v[56:59], a[92:95], v[248:251]\n"
  "s_xor_b32 s72, s67, 0x300000\n"
  "buffer_load_dwordx4 v[64:67], v2, s[44:47], s72 offen\n"
  "v_mfma_f32_16x16x32_bf16 a[24:27], v[56:59], a[96:99], a[24:27]\n"
  "buffer_load_dwordx4 v[68:71], v3, s[44:47], s72 offen\n"
  "v_mfma_f32_16x16x32_bf16 a[56:59], v[56:59], a[100:103], a[56:59]\n"
  "s_waitcnt vmcnt(10)\n"
  "buffer_load_dwordx4 v[72:75], v4, s[44:47], s72 offen\n"
  "v_mfma_f32_16x16x32_bf16 v[188:191], v[60:63], a[84:87], v[188:191]\n"
  "v_perm_b32 v56, v100, v96, s69\n"
  "v_perm_b32 v57, v108, v104, s69\n"
  "buffer_load_dwordx4 v[76:79], v5, s[44:47], s72 offen\n"
  "v_mfma_f32_16x16x32_bf16 v[220:223], v[60:63], a[88:91], v[220:223]\n"
  "v_perm_b32 v58, v116, v112, s69\n"
  "v_perm_b32 v59, v124, v120, s69\n"
  "buffer_load_dwordx4 v[80:83], v6, s[44:47], s72 offen\n"
  "v_mfma_f32_16x16x32_bf16 v[252:255], v[60:63], a[92:95], v[252:255]\n"
  "buffer_load_dwordx4 v[84:87], v7, s[44:47], s72 offen\n"
  "v_mfma_f32_16x16x32_bf16 a[28:31], v[60:63], a[96:99], a[28:31]\n"
  "s_waitcnt lgkmcnt(0)\n"
  "buffer_load_dwordx4 v[88:91], v8, s[44:47], s72 offen\n"
  "v_mfma_f32_16x16x32_bf16 a[60:63], v[60:63], a[100:103], a[60:63]\n"
  "buffer_load_dwordx4 v[92:95], v9, s[44:47], s72 offen\n"
  "v_mfma_f32_16x16x32_bf16 v[160:163], v[56:59], a[64:67], v[160:163]\n"
  "v_perm_b32 v60, v100, v96, s70\n"
  "v_perm_b32 v61, v108, v104, s70\n"
  "v_mfma_f32_16x16x32_bf16 v[192:195], v[56:59], a[68:71], v[192:195]\n"
  "v_perm_b32 v62, v116, v112, s70\n"
  "v_perm_b32 v63, v124, v120, s70\n"
  "v_mfma_f32_16x16x32_bf16 v[224:227], v[56:59], a[72:75], v[224:227]\n"
  "buffer_load_dwordx4 v14, s[36:39], s94 offen offset:1024 sc1 nt lds\n"
  "v_mfma_f32_16x16x32_bf16 a[0:3], v[56:59], a[76:79], a[0:3]\n"
  "v_xor_b32 v12, 0xa0c0, v11\n"
  "v_mfma_f32_16x16x32_bf16 a[32:35], v[56:59], a[80:83], a[32:35]\n"
  "buffer_load_dwordx4 v15, s[36:39], s94 offen offset:2048 sc1 nt lds\n"
  "v_mfma_f32_16x16x32_bf16 v[164:167], v[60:63], a[64:67], v[164:167]\n"
  "v_perm_b32 v56, v101, v97, s69\n"
  "v_perm_b32 v57, v109, v105, s69\n"
  "buffer_load_dwordx4 v16, s[36:39], s94 offen offset:3072 sc1 nt lds\n"
  "v_mfma_f32_16x16x32_bf16 v[196:199], v[60:63], a[68:71], v[196:199]\n"
  "v_perm_b32 v58, v117, v113, s69\n"
  "v_perm_b32 v59, v125, v121, s69\n"
  "v_mfma_f32_16x16x32_bf16 v[228:231], v[60:63], a[72:75], v[228:231]\n"
  "s_add_u32 m0, s77, 0x4000\n"
  "s_nop 0\n"
  "buffer_load_dwordx4 v13, s[36:39], s95 offen sc1 nt lds\n"
  "v_mfma_f32_16x16x32_bf16 a[4:7], v[60:63], a[76:79], a[4:7]\n"
  "ds_read_b128 a[84:87], v12\n"
  "v_mfma_f32_16x16x32_bf16 a[36:39], v[60:63], a[80:83], a[36:39]\n"
  "buffer_load_dwordx4 v14, s[36:39], s95 offen offset:1024 sc1 nt lds\n"
  "v_mfma_f32_16x16x32_bf16 v[168:171], v[56:59], a[64:67], v[168:171]\n"
  "v_perm_b32 v60, v101, v97, s70\n"
  "v_perm_b32 v61, v109, v105, s70\n"
  "buffer_load_dwordx4 v15, s[36:39], s95 offen offset:2048 sc1 nt lds\n"
  "v_mfma_f32_16x16x32_bf16 v[200:203], v[56:59], a[68:71], v[200:203]\n"
  "v_perm_b32 v62, v117, v113, s70\n"
  "v_perm_b32 v63, v125, v121, s70\n"
  "v_mfma_f32_16x16x32_bf16 v[232:235], v[56:59], a[72:75], v[232:235]\n"
  "buffer_load_dwordx4 v16, s[36:39], s95 offen offset:3072 sc1 nt lds\n"
  "v_mfma_f32_16x16x32_bf16 a[8:11], v[56:59], a[76:79], a[8:11]\n"
  "ds_read_b128 a[88:91], v12 offset:4096\n"
  "v_mfma_f32_16x16x32_bf16 a[40:43], v[56:59], a[80:83], a[40:43]\n"
  "s_add_u32 m0, s76, 0\n"
  "s_nop 0\n"
  "buffer_load_dwordx4 v17, s[40:43], 0 offen sc1 nt lds\n"
  "v_mfma_f32_16x16x32_bf16 v[172:175], v[60:63], a[64:67], v[172:175]\n"
  "v_perm_b32 v56, v102, v98, s69\n"
  "v_perm_b32 v57, v110, v106, s69\n"
  "v_mfma_f32_16x16x32_bf16 v[204:207], v[60:63], a[68:71], v[204:207]\n"
  "v_perm_b32 v58, v118, v114, s69\n"
  "v_perm_b32 v59, v126, v122, s69\n"
  "v_mfma_f32_16x16x32_bf16 v[236:239], v[60:63], a[72:75], v[236:239]\n"
  "v_mfma_f32_16x16x32_bf16 a[12:15], v[60:63], a[76:79], a[12:15]\n"
  "ds_read_b128 a[92:95], v12 offset:8192\n"
  "v_mfma_f32_16x16x32_bf16 a[44:47], v[60:63], a[80:83], a[44:47]\n"
  "v_mfma_f32_16x16x32_bf16 v[176:179], v[56:59], a[64:67], v[176:179]\n"
  "v_perm_b32 v60, v102, v98, s70\n"
  "v_perm_b32 v61, v110, v106, s70\n"
  "v_mfma_f32_16x16x32_bf16 v[208:211], v[56:59], a[68:71], v[208:211]\n"
  "v_perm_b32 v62, v118, v114, s70\n"
  "v_perm_b32 v63, v126, v122, s70\n"
  "v_mfma_f32_16x16x32_bf16 v[240:243], v[56:59], a[72:75], v[240:243]\n"
  "v_mfma_f32_16x16x32_bf16 a[16:19], v[56:59], a[76:79], a[16:19]\n"
  "ds_read_b128 a[96:99], v12 offset:12288\n"
  "v_mfma_f32_16x16x32_bf16 a[48:51], v[56:59], a[80:83], a[48:51]\n"
  "v_mfma_f32_16x16x32_bf16 v[180:183], v[60:63], a[64:67], v[180:183]\n"
  "v_perm_b32 v56, v103, v99, s69\n"
  "v_perm_b32 v57, v111, v107, s69\n"
  "v_mfma_f32_16x16x32_bf16 v[212:215], v[60:63], a[68:71], v[212:215]\n"
  "v_perm_b32 v58, v119, v115, s69\n"
  "v_perm_b32 v59, v127, v123, s69\n"
  "v_mfma_f32_16x16x32_bf16 v[244:247], v[60:63], a[72:75], v[244:247]\n"
  "v_mfma_f32_16x16x32_bf16 a[20:23], v[60:63], a[76:79], a[20:23]\n"
  "ds_read_b128 a[100:103], v12 offset:16384\n"
  "v_mfma_f32_16x16x32_bf16 a[52:55], v[60:63], a[80:83], a[52:55]\n"
  "v_mfma_f32_16x16x32_bf16 v[184:187], v[56:59], a[64:67], v[184:187]\n"
  "v_perm_b32 v60, v103, v99, s70\n"
  "v_perm_b32 v61, v111, v107, s70\n"
  "v_mfma_f32_16x16x32_bf16 v[216:219], v[56:59], a[68:71], v[216:219]\n"
  "v_perm_b32 v62, v119, v115, s70\n"
  "v_perm_b32 v63, v127, v123, s70\n"
  "v_mfma_f32_16x16x32_bf16 v[248:251], v[56:59], a[72:75], v[248:251]\n"
  "v_mfma_f32_16x16x32_bf16 a[24:27], v[56:59], a[76:79], a[24:27]\n"
  "v_mfma_f32_16x16x32_bf16 a[56:59], v[56:59], a[80:83], a[56:59]\n"
  "s_waitcnt vmcnt(16)\n"
  "v_mfma_f32_16x16x32_bf16 v[188:191], v[60:63], a[64:67], v[188:191]\n"
  "v_perm_b32 v56, v132, v128, s69\n"
  "v_perm_b32 v57, v140, v136, s69\n"
  "v_mfma_f32_16x16x32_bf16 v[220:223], v[60:63], a[68:71], v[220:223]\n"
  "v_perm_b32 v58, v148, v144, s69\n"
  "v_perm_b32 v59, v156, v152, s69\n"
  "v_mfma_f32_16x16x32_bf16 v[252:255], v[60:63], a[72:75], v[252:255]\n"
  "v_mfma_f32_16x16x32_bf16 a[28:31], v[60:63], a[76:79], a[28:31]\n"
  "s_waitcnt lgkmcnt(0)\n"
  "v_mfma_f32_16x16x32_bf16 a[60:63], v[60:63], a[80:83], a[60:63]\n"
  "v_mfma_f32_16x16x32_bf16 v[160:163], v[56:59], a[84:87], v[160:163]\n"
  "v_perm_b32 v60, v132, v128, s70\n"
  "v_perm_b32 v61, v140, v136, s70\n"
  "v_mfma_f32_16x16x32_bf16 v[192:195], v[56:59], a[88:91], v[192:195]\n"
  "v_perm_b32 v62, v148, v144, s70\n"
  "v_perm_b32 v63, v156, v152, s70\n"
  "v_mfma_f32_16x16x32_bf16 v[224:227], v[56:59], a[92:95], v[224:227]\n"
  "v_mfma_f32_16x16x32_bf16 a[0:3], v[56:59], a[96:99], a[0:3]\n"
  "v_xor_b32 v12, 0xf000, v11\n"
  "v_mfma_f32_16x16x32_bf16 a[32:35], v[56:59], a[100:103], a[32:35]\n"
  "s_barrier\n"
  "v_mfma_f32_16x16x32_bf16 v[164:167], v[60:63], a[84:87], v[164:167]\n"
  "v_perm_b32 v56, v133, v129, s69\n"
  "v_perm_b32 v57, v141, v137, s69\n"
  "v_mfma_f32_16x16x32_bf16 v[196:199], v[60:63], a[88:91], v[196:199]\n"
  "v_perm_b32 v58, v149, v145, s69\n"
  "v_perm_b32 v59, v157, v153, s69\n"
  "v_mfma_f32_16x16x32_bf16 v[228:231], v[60:63], a[92:95], v[228:231]\n"
  "v_mfma_f32_16x16x32_bf16 a[4:7], v[60:63], a[96:99], a[4:7]\n"
  "ds_read_b128 a[64:67], v12\n"
  "v_mfma_f32_16x16x32_bf16 a[36:39], v[60:63], a[100:103], a[36:39]\n"
  "v_mfma_f32_16x16x32_bf16 v[168:171], v[56:59], a[84:87], v[168:171]\n"
  "v_perm_b32 v60, v133, v129, s70\n"
  "v_perm_b32 v61, v141, v137, s70\n"
  "v_mfma_f32_16x16x32_bf16 v[200:203], v[56:59], a[88:91], v[200:203]\n"
  "v_perm_b32 v62, v149, v145, s70\n"
  "v_perm_b32 v63, v157, v153, s70\n"
  "v_mfma_f32_16x16x32_bf16 v[232:235], v[56:59], a[92:95], v[232:235]\n"
  "v_mfma_f32_16x16x32_bf16 a[8:11], v[56:59], a[96:99], a[8:11]\n"
  "ds_read_b128 a[68:71], v12 offset:4096\n"
  "v_mfma_f32_16x16x32_bf16 a[40:43], v[56:59], a[100:103], a[40:43]\n"
  "v_mfma_f32_16x16x32_bf16 v[172:175], v[60:63], a[84:87], v[172:175]\n"
  "v_perm_b32 v56, v134, v130, s69\n"
  "v_perm_b32 v57, v142, v138, s69\n"
  "v_mfma_f32_16x16x32_bf16 v[204:207], v[60:63], a[88:91], v[204:207]\n"
  "v_perm_b32 v58, v150, v146, s69\n"
  "v_perm_b32 v59, v158, v154, s69\n"
  "v_mfma_f32_16x16x32_bf16 v[236:239], v[60:63], a[92:95], v[236:239]\n"
  "v_mfma_f32_16x16x32_bf16 a[12:15], v[60:63], a[96:99], a[12:15]\n"
  "ds_read_b128 a[72:75], v12 offset:8192\n"
  "v_mfma_f32_16x16x32_bf16 a[44:47], v[60:63], a[100:103], a[44:47]\n"
  "v_mfma_f32_16x16x32_bf16 v[176:179], v[56:59], a[84:87], v[176:179]\n"
  "v_perm_b32 v60, v134, v130, s70\n"
  "v_perm_b32 v61, v142, v138, s70\n"
  "v_mfma_f32_16x16x32_bf16 v[208:211], v[56:59], a[88:91], v[208:211]\n"
  "v_perm_b32 v62, v150, v146, s70\n"
  "v_perm_b32 v63, v158, v154, s70\n"
  "v_mfma_f32_16x16x32_bf16 v[240:243], v[56:59], a[92:95], v[240:243]\n"
  "s_xor_b32 s72, s67, 0x340000\n"
  "buffer_load_dwordx4 v[96:99], v2, s[44:47], s72 offen\n"
  "v_mfma_f32_16x16x32_bf16 a[16:19], v[56:59], a[96:99], a[16:19]\n"
  "ds_read_b128 a[76:79], v12 offset:12288\n"
  "v_mfma_f32_16x16x32_bf16 a[48:51], v[56:59], a[100:103], a[48:51]\n"
  "buffer_load_dwordx4 v[100:103], v3, s[44:47], s72 offen\n"
  "v_mfma_f32_16x16x32_bf16 v[180:183], v[60:63], a[84:87], v[180:183]\n"
  "v_perm_b32 v56, v135, v131, s69\n"
  "v_perm_b32 v57, v143, v139, s69\n"
  "v_mfma_f32_16x16x32_bf16 v[212:215], v[60:63], a[88:91], v[212:215]\n"
  "v_perm_b32 v58, v151, v147, s69\n"
  "v_perm_b32 v59, v159, v155, s69\n"
  "buffer_load_dwordx4 v[104:107], v4, s[44:47], s72 offen\n"
  "v_mfma_f32_16x16x32_bf16 v[244:247], v[60:63], a[92:95], v[244:247]\n"
  "v_mfma_f32_16x16x32_bf16 a[20:23], v[60:63], a[96:99], a[20:23]\n"
  "buffer_load_dwordx4 v[108:111], v5, s[44:47], s72 offen\n"
  "ds_read_b128 a[80:83], v12 offset:16384\n"
  "v_mfma_f32_16x16x32_bf16 a[52:55], v[60:63], a[100:103], a[52:55]\n"
  "v_mfma_f32_16x16x32_bf16 v[184:187], v[56:59], a[84:87], v[184:187]\n"
  "v_perm_b32 v60, v135, v131, s70\n"
  "v_perm_b32 v61, v143, v139, s70\n"
  "buffer_load_dwordx4 v[112:115], v6, s[44:47], s72 offen\n"
  "v_mfma_f32_16x16x32_bf16 v[216:219], v[56:59], a[88:91], v[216:219]\n"
  "v_perm_b32 v62, v151, v147, s70\n"
  "v_perm_b32 v63, v159, v155, s70\n"
  "v_mfma_f32_16x16x32_bf16 v[248:251], v[56:59], a[92:95], v[248:251]\n"
  "buffer_load_dwordx4 v[116:119], v7, s[44:47], s72 offen\n"
  "v_mfma_f32_16x16x32_bf16 a[24:27], v[56:59], a[96:99], a[24:27]\n"
  "v_mfma_f32_16x16x32_bf16 a[56:59], v[56:59], a[100:103], a[56:59]\n"
  "s_waitcnt vmcnt(14)\n"
  "buffer_load_dwordx4 v[120:123], v8, s[44:47], s72 offen\n"
  "v_mfma_f32_16x16x32_bf16 v[188:191], v[60:63], a[84:87], v[188:191]\n"
  "v_perm_b32 v56, v68, v64, s69\n"
  "v_perm_b32 v57, v76, v72, s69\n"
  "v_mfma_f32_16x16x32_bf16 v[220:223], v[60:63], a[88:91], v[220:223]\n"
  "v_perm_b32 v58, v84, v80, s69\n"
  "v_perm_b32 v59, v92, v88, s69\n"
  "buffer_load_dwordx4 v[124:127], v9, s[44:47], s72 offen\n"
  "v_mfma_f32_16x16x32_bf16 v[252:255], v[60:63], a[92:95], v[252:255]\n"
  "v_mfma_f32_16x16x32_bf16 a[28:31], v[60:63], a[96:99], a[28:31]\n"
  "s_waitcnt lgkmcnt(0)\n"
  "v_mfma_f32_16x16x32_bf16 a[60:63], v[60:63], a[100:103], a[60:63]\n"
  "v_mfma_f32_16x16x32_bf16 v[160:163], v[56:59], a[64:67], v[160:163]\n"
  "v_perm_b32 v60, v68, v64, s70\n"
  "v_perm_b32 v61, v76, v72, s70\n"
  "s_xor_b32 s72, s67, 0x380000\n"
  "buffer_load_dwordx4 v[128:131], v2, s[44:47], s72 offen\n"
  "v_mfma_f32_16x16x32_bf16 v[192:195], v[56:59], a[68:71], v[192:195]\n"
  "v_perm_b32 v62, v84, v80, s70\n"
  "v_perm_b32 v63, v92, v88, s70\n"
  "v_mfma_f32_16x16x32_bf16 v[224:227], v[56:59], a[72:75], v[224:227]\n"
  "buffer_load_dwordx4 v[132:135], v3, s[44:47], s72 offen\n"
  "v_mfma_f32_16x16x32_bf16 a[0:3], v[56:59], a[76:79], a[0:3]\n"
  "v_xor_b32 v12, 0xf040, v11\n"
  "v_mfma_f32_16x16x32_bf16 a[32:35], v[56:59], a[80:83], a[32:35]\n"
  "buffer_load_dwordx4 v[136:139], v4, s[44:47], s72 offen\n"
  "v_mfma_f32_16x16x32_bf16 v[164:167], v[60:63], a[64:67], v[164:167]\n"
  "v_perm_b32 v56, v69, v65, s69\n"
  "v_perm_b32 v57, v77, v73, s69\n"
  "v_mfma_f32_16x16x32_bf16 v[196:199], v[60:63], a[68:71], v[196:199]\n"
  "v_perm_b32 v58, v85, v81, s69\n"
  "v_perm_b32 v59, v93, v89, s69\n"
  "buffer_load_dwordx4 v[140:143], v5, s[44:47], s72 offen\n"
  "v_mfma_f32_16x16x32_bf16 v[228:231], v[60:63], a[72:75], v[228:231]\n"
  "v_mfma_f32_16x16x32_bf16 a[4:7], v[60:63], a[76:79], a[4:7]\n"
  "buffer_load_dwordx4 v[144:147], v6, s[44:47], s72 offen\n"
  "ds_read_b128 a[84:87], v12\n"
  "v_mfma_f32_16x16x32_bf16 a[36:39], v[60:63], a[80:83], a[36:39]\n"
  "v_mfma_f32_16x16x32_bf16 v[168:171], v[56:59], a[64:67], v[168:171]\n"
  "v_perm_b32 v60, v69, v65, s70\n"
  "v_perm_b32 v61, v77, v73, s70\n"
  "buffer_load_dwordx4 v[148:151], v7, s[44:47], s72 offen\n"
  "v_mfma_f32_16x16x32_bf16 v[200:203], v[56:59], a[68:71], v[200:203]\n"
  "v_perm_b32 v62, v85, v81, s70\n"
  "v_perm_b32 v63, v93, v89, s70\n"
  "v_mfma_f32_16x16x32_bf16 v[232:235], v[56:59], a[72:75], v[232:235]\n"
  "buffer_load_dwordx4 v[152:155], v8, s[44:47], s72 offen\n"
  "v_mfma_f32_16x16x32_bf16 a[8:11], v[56:59], a[76:79], a[8:11]\n"
  "ds_read_b128 a[88:91], v12 offset:4096\n"
  "v_mfma_f32_16x16x32_bf16 a[40:43], v[56:59], a[80:83], a[40:43]\n"
  "buffer_load_dwordx4 v[156:159], v9, s[44:47], s72 offen\n"
  "v_mfma_f32_16x16x32_bf16 v[172:175], v[60:63], a[64:67], v[172:175]\n"
  "v_perm_b32 v56, v70, v66, s69\n"
  "v_perm_b32 v57, v78, v74, s69\n"
  "v_mfma_f32_16x16x32_bf16 v[204:207], v[60:63], a[68:71], v[204:207]\n"
  "v_perm_b32 v58, v86, v82, s69\n"
  "v_perm_b32 v59, v94, v90, s69\n"
  "v_mfma_f32_16x16x32_bf16 v[236:239], v[60:63], a[72:75], v[236:239]\n"
  "v_mfma_f32_16x16x32_bf16 a[12:15], v[60:63], a[76:79], a[12:15]\n"
  "ds_read_b128 a[92:95], v12 offset:8192\n"
  "v_mfma_f32_16x16x32_bf16 a[44:47], v[60:63], a[80:83], a[44:47]\n"
  "v_mfma_f32_16x16x32_bf16 v[176:179], v[56:59], a[64:67], v[176:179]\n"
  "v_perm_b32 v60, v70, v66, s70\n"
  "v_perm_b32 v61, v78, v74, s70\n"
  "v_mfma_f32_16x16x32_bf16 v[208:211], v[56:59], a[68:71], v[208:211]\n"
  "v_perm_b32 v62, v86, v82, s70\n"
  "v_perm_b32 v63, v94, v90, s70\n"
  "v_mfma_f32_16x16x32_bf16 v[240:243], v[56:59], a[72:75], v[240:243]\n"
  "v_mfma_f32_16x16x32_bf16 a[16:19], v[56:59], a[76:79], a[16:19]\n"
  "ds_read_b128 a[96:99], v12 offset:12288\n"
  "v_mfma_f32_16x16x32_bf16 a[48:51], v[56:59], a[80:83], a[48:51]\n"
  "v_mfma_f32_16x16x32_bf16 v[180:183], v[60:63], a[64:67], v[180:183]\n"
  "v_perm_b32 v56, v71, v67, s69\n"
  "v_perm_b32 v57, v79, v75, s69\n"
  "v_mfma_f32_16x16x32_bf16 v[212:215], v[60:63], a[68:71], v[212:215]\n"
  "v_perm_b32 v58, v87, v83, s69\n"
  "v_perm_b32 v59, v95, v91, s69\n"
  "v_mfma_f32_16x16x32_bf16 v[244:247], v[60:63], a[72:75], v[244:247]\n"
  "v_mfma_f32_16x16x32_bf16 a[20:23], v[60:63], a[76:79], a[20:23]\n"
  "ds_read_b128 a[100:103], v12 offset:16384\n"
  "v_mfma_f32_16x16x32_bf16 a[52:55], v[60:63], a[80:83], a[52:55]\n"
  "v_mfma_f32_16x16x32_bf16 v[184:187], v[56:59], a[64:67], v[184:187]\n"
  "v_perm_b32 v60, v71, v67, s70\n"
  "v_perm_b32 v61, v79, v75, s70\n"
  "v_mfma_f32_16x16x32_bf16 v[216:219], v[56:59], a[68:71], v[216:219]\n"
  "v_perm_b32 v62, v87, v83, s70\n"
  "v_perm_b32 v63, v95, v91, s70\n"
  "v_mfma_f32_16x16x32_bf16 v[248:251], v[56:59], a[72:75], v[248:251]\n"
  "s_xor_b32 s72, s67, 0x3c0000\n"
  "buffer_load_dwordx4 v[64:67], v2, s[44:47], s72 offen\n"
  "v_mfma_f32_16x16x32_bf16 a[24:27], v[56:59], a[76:79], a[24:27]\n"
  "buffer_load_dwordx4 v[68:71], v3, s[44:47], s72 offen\n"
  "v_mfma_f32_16x16x32_bf16 a[56:59], v[56:59], a[80:83], a[56:59]\n"
  "s_waitcnt vmcnt(10)\n"
  "buffer_load_dwordx4 v[72:75], v4, s[44:47], s72 offen\n"
  "v_mfma_f32_16x16x32_bf16 v[188:191], v[60:63], a[64:67], v[188:191]\n"
  "v_perm_b32 v56, v100, v96, s69\n"
  "v_perm_b32 v57, v108, v104, s69\n"
  "buffer_load_dwordx4 v[76:79], v5, s[44:47], s72 offen\n"
  "v_mfma_f32_16x16x32_bf16 v[220:223], v[60:63], a[68:71], v[220:223]\n"
  "v_perm_b32 v58, v116, v112, s69\n"
  "v_perm_b32 v59, v124, v120, s69\n"
  "buffer_load_dwordx4 v[80:83], v6, s[44:47], s72 offen\n"
  "v_mfma_f32_16x16x32_bf16 v[252:255], v[60:63], a[72:75], v[252:255]\n"
  "buffer_load_dwordx4 v[84:87], v7, s[44:47], s72 offen\n"
  "v_mfma_f32_16x16x32_bf16 a[28:31], v[60:63], a[76:79], a[28:31]\n"
  "s_waitcnt lgkmcnt(0)\n"
  "buffer_load_dwordx4 v[88:91], v8, s[44:47], s72 offen\n"
  "v_mfma_f32_16x16x32_bf16 a[60:63], v[60:63], a[80:83], a[60:63]\n"
  "buffer_load_dwordx4 v[92:95], v9, s[44:47], s72 offen\n"
  "v_mfma_f32_16x16x32_bf16 v[160:163], v[56:59], a[84:87], v[160:163]\n"
  "v_perm_b32 v60, v100, v96, s70\n"
  "v_perm_b32 v61, v108, v104, s70\n"
  "v_mfma_f32_16x16x32_bf16 v[192:195], v[56:59], a[88:91], v[192:195]\n"
  "v_perm_b32 v62, v116, v112, s70\n"
  "v_perm_b32 v63, v124, v120, s70\n"
  "v_mfma_f32_16x16x32_bf16 v[224:227], v[56:59], a[92:95], v[224:227]\n"
  "buffer_load_dwordx4 v18, s[40:43], 0 offen offset:1024 sc1 nt lds\n"
  "v_mfma_f32_16x16x32_bf16 a[0:3], v[56:59], a[96:99], a[0:3]\n"
  "v_xor_b32 v12, 0xf080, v11\n"
  "v_mfma_f32_16x16x32_bf16 a[32:35], v[56:59], a[100:103], a[32:35]\n"
  "buffer_load_dwordx4 v17, s[40:43], 0 offen offset:2048 sc1 nt lds\n"
  "v_mfma_f32_16x16x32_bf16 v[164:167], v[60:63], a[84:87], v[164:167]\n"
  "v_perm_b32 v56, v101, v97, s69\n"
  "v_perm_b32 v57, v109, v105, s69\n"
  "buffer_load_dwordx4 v18, s[40:43], 0 offen offset:3072 sc1 nt lds\n"
  "v_mfma_f32_16x16x32_bf16 v[196:199], v[60:63], a[88:91], v[196:199]\n"
  "v_perm_b32 v58, v117, v113, s69\n"
  "v_perm_b32 v59, v125, v121, s69\n"
  "v_mfma_f32_16x16x32_bf16 v[228:231], v[60:63], a[92:95], v[228:231]\n"
  "s_add_u32 m0, s76, 0x4000\n"
  "s_nop 0\n"
  "buffer_load_dwordx4 v17, s[40:43], s92 offen sc1 nt lds\n"
  "v_mfma_f32_16x16x32_bf16 a[4:7], v[60:63], a[96:99], a[4:7]\n"
  "ds_read_b128 a[64:67], v12\n"
  "v_mfma_f32_16x16x32_bf16 a[36:39], v[60:63], a[100:103], a[36:39]\n"
  "buffer_load_dwordx4 v18, s[40:43], s92 offen offset:1024 sc1 nt lds\n"
  "v_mfma_f32_16x16x32_bf16 v[168:171], v[56:59], a[84:87], v[168:171]\n"
  "v_perm_b32 v60, v101, v97, s70\n"
  "v_perm_b32 v61, v109, v105, s70\n"
  "buffer_load_dwordx4 v17, s[40:43], s92 offen offset:2048 sc1 nt lds\n"
  "v_mfma_f32_16x16x32_bf16 v[200:203], v[56:59], a[88:91], v[200:203]\n"
  "v_perm_b32 v62, v117, v113, s70\n"
  "v_perm_b32 v63, v125, v121, s70\n"
  "v_mfma_f32_16x16x32_bf16 v[232:235], v[56:59], a[92:95], v[232:235]\n"
  "buffer_load_dwordx4 v18, s[40:43], s92 offen offset:3072 sc1 nt lds\n"
  "v_mfma_f32_16x16x32_bf16 a[8:11], v[56:59], a[96:99], a[8:11]\n"
  "ds_read_b128 a[68:71], v12 offset:4096\n"
  "v_mfma_f32_16x16x32_bf16 a[40:43], v[56:59], a[100:103], a[40:43]\n"
  "v_mfma_f32_16x16x32_bf16 v[172:175], v[60:63], a[84:87], v[172:175]\n"
  "v_perm_b32 v56, v102, v98, s69\n"
  "v_perm_b32 v57, v110, v106, s69\n"
  "v_mfma_f32_16x16x32_bf16 v[204:207], v[60:63], a[88:91], v[204:207]\n"
  "v_perm_b32 v58, v118, v114, s69\n"
  "v_perm_b32 v59, v126, v122, s69\n"
  "v_mfma_f32_16x16x32_bf16 v[236:239], v[60:63], a[92:95], v[236:239]\n"
  "v_mfma_f32_16x16x32_bf16 a[12:15], v[60:63], a[96:99], a[12:15]\n"
  "ds_read_b128 a[72:75], v12 offset:8192\n"
  "v_mfma_f32_16x16x32_bf16 a[44:47], v[60:63], a[100:103], a[44:47]\n"
  "v_mfma_f32_16x16x32_bf16 v[176:179], v[56:59], a[84:87], v[176:179]\n"
  "v_perm_b32 v60, v102, v98, s70\n"
  "v_perm_b32 v61, v110, v106, s70\n"
  "v_mfma_f32_16x16x32_bf16 v[208:211], v[56:59], a[88:91], v[208:211]\n"
  "v_perm_b32 v62, v118, v114, s70\n"
  "v_perm_b32 v63, v126, v122, s70\n"
  "v_mfma_f32_16x16x32_bf16 v[240:243], v[56:59], a[92:95], v[240:243]\n"
  "v_mfma_f32_16x16x32_bf16 a[16:19], v[56:59], a[96:99], a[16:19]\n"
  "ds_read_b128 a[76:79], v12 offset:12288\n"
  "v_mfma_f32_16x16x32_bf16 a[48:51], v[56:59], a[100:103], a[48:51]\n"
  "v_mfma_f32_16x16x32_bf16 v[180:183], v[60:63], a[84:87], v[180:183]\n"
  "v_perm_b32 v56, v103, v99, s69\n"
  "v_perm_b32 v57, v111, v107, s69\n"
  "v_mfma_f32_16x16x32_bf16 v[212:215], v[60:63], a[88:91], v[212:215]\n"
  "v_perm_b32 v58, v119, v115, s69\n"
  "v_perm_b32 v59, v127, v123, s69\n"
  "v_mfma_f32_16x16x32_bf16 v[244:247], v[60:63], a[92:95], v[244:247]\n"
  "v_mfma_f32_16x16x32_bf16 a[20:23], v[60:63], a[96:99], a[20:23]\n"
  "ds_read_b128 a[80:83], v12 offset:16384\n"
  "v_mfma_f32_16x16x32_bf16 a[52:55], v[60:63], a[100:103], a[52:55]\n"
  "v_mfma_f32_16x16x32_bf16 v[184:187], v[56:59], a[84:87], v[184:187]\n"
  "v_perm_b32 v60, v103, v99, s70\n"
  "v_perm_b32 v61, v111, v107, s70\n"
  "v_mfma_f32_16x16x32_bf16 v[216:219], v[56:59], a[88:91], v[216:219]\n"
  "v_perm_b32 v62, v119, v115, s70\n"
  "v_perm_b32 v63, v127, v123, s70\n"
  "v_mfma_f32_16x16x32_bf16 v[248:251], v[56:59], a[92:95], v[248:251]\n"
  "v_mfma_f32_16x16x32_bf16 a[24:27], v[56:59], a[96:99], a[24:27]\n"
  "v_xor_b32 v49, 16, v19\n"
  "v_xor_b32 v50, 32, v19\n"
  "v_mfma_f32_16x16x32_bf16 a[56:59], v[56:59], a[100:103], a[56:59]\n"
  "s_waitcnt vmcnt(15)\n"
  "v_mfma_f32_16x16x32_bf16 v[188:191], v[60:63], a[84:87], v[188:191]\n"
  "v_perm_b32 v56, v132, v128, s69\n"
  "v_perm_b32 v57, v140, v136, s69\n"
  "v_mfma_f32_16x16x32_bf16 v[220:223], v[60:63], a[88:91], v[220:223]\n"
  "v_perm_b32 v58, v148, v144, s69\n"
  "v_perm_b32 v59, v156, v152, s69\n"
  "v_mfma_f32_16x16x32_bf16 v[252:255], v[60:63], a[92:95], v[252:255]\n"
  "v_mfma_f32_16x16x32_bf16 a[28:31], v[60:63], a[96:99], a[28:31]\n"
  "s_waitcnt lgkmcnt(0)\n"
  "v_mfma_f32_16x16x32_bf16 a[60:63], v[60:63], a[100:103], a[60:63]\n"
  "v_xor_b32 v51, 48, v19\n"
  "v_mfma_f32_16x16x32_bf16 v[160:163], v[56:59], a[64:67], v[160:163]\n"
  "v_perm_b32 v60, v132, v128, s70\n"
  "v_perm_b32 v61, v140, v136, s70\n"
  "v_mfma_f32_16x16x32_bf16 v[192:195], v[56:59], a[68:71], v[192:195]\n"
  "v_perm_b32 v62, v148, v144, s70\n"
  "v_perm_b32 v63, v156, v152, s70\n"
  "v_mfma_f32_16x16x32_bf16 v[224:227], v[56:59], a[72:75], v[224:227]\n"
  "ds_read_b128 a[104:107], v19\n"
  "ds_read_b128 a[108:111], v49\n"
  "v_mfma_f32_16x16x32_bf16 a[0:3], v[56:59], a[76:79], a[0:3]\n"
  "v_xor_b32 v12, 0xf0c0, v11\n"
  "v_mfma_f32_16x16x32_bf16 a[32:35], v[56:59], a[80:83], a[32:35]\n"
  "v_mfma_f32_16x16x32_bf16 v[164:167], v[60:63], a[64:67], v[164:167]\n"
  "v_perm_b32 v56, v133, v129, s69\n"
  "v_perm_b32 v57, v141, v137, s69\n"
  "v_mfma_f32_16x16x32_bf16 v[196:199], v[60:63], a[68:71], v[196:199]\n"
  "v_perm_b32 v58, v149, v145, s69\n"
  "v_perm_b32 v59, v157, v153, s69\n"
  "v_mfma_f32_16x16x32_bf16 v[228:231], v[60:63], a[72:75], v[228:231]\n"
  "ds_read_b128 a[112:115], v50\n"
  "ds_read_b128 a[116:119], v51\n"
  "v_mfma_f32_16x16x32_bf16 a[4:7], v[60:63], a[76:79], a[4:7]\n"
  "ds_read_b128 a[84:87], v12\n"
  "v_mfma_f32_16x16x32_bf16 a[36:39], v[60:63], a[80:83], a[36:39]\n"
  "v_mfma_f32_16x16x32_bf16 v[168:171], v[56:59], a[64:67], v[168:171]\n"
  "v_perm_b32 v60, v133, v129, s70\n"
  "v_perm_b32 v61, v141, v137, s70\n"
  "v_mfma_f32_16x16x32_bf16 v[200:203], v[56:59], a[68:71], v[200:203]\n"
  "v_perm_b32 v62, v149, v145, s70\n"
  "v_perm_b32 v63, v157, v153, s70\n"
  "v_mfma_f32_16x16x32_bf16 v[232:235], v[56:59], a[72:75], v[232:235]\n"
  "ds_read_b128 a[120:123], v19 offset:4096\n"
  "ds_read_b128 a[124:127], v49 offset:4096\n"
  "v_mfma_f32_16x16x32_bf16 a[8:11], v[56:59], a[76:79], a[8:11]\n"
  "ds_read_b128 a[88:91], v12 offset:4096\n"
  "v_mfma_f32_16x16x32_bf16 a[40:43], v[56:59], a[80:83], a[40:43]\n"
  "v_mfma_f32_16x16x32_bf16 v[172:175], v[60:63], a[64:67], v[172:175]\n"
  "v_perm_b32 v56, v134, v130, s69\n"
  "v_perm_b32 v57, v142, v138, s69\n"
  "v_mfma_f32_16x16x32_bf16 v[204:207], v[60:63], a[68:71], v[204:207]\n"
  "v_perm_b32 v58, v150, v146, s69\n"
  "v_perm_b32 v59, v158, v154, s69\n"
  "v_mfma_f32_16x16x32_bf16 v[236:239], v[60:63], a[72:75], v[236:239]\n"
  "ds_read_b128 a[128:131], v50 offset:4096\n"
  "ds_read_b128 a[132:135], v51 offset:4096\n"
  "v_mfma_f32_16x16x32_bf16 a[12:15], v[60:63], a[76:79], a[12:15]\n"
  "ds_read_b128 a[92:95], v12 offset:8192\n"
  "v_mfma_f32_16x16x32_bf16 a[44:47], v[60:63], a[80:83], a[44:47]\n"
  "v_mfma_f32_16x16x32_bf16 v[176:179], v[56:59], a[64:67], v[176:179]\n"
  "v_perm_b32 v60, v134, v130, s70\n"
  "v_perm_b32 v61, v142, v138, s70\n"
  "v_mfma_f32_16x16x32_bf16 v[208:211], v[56:59], a[68:71], v[208:211]\n"
  "v_perm_b32 v62, v150, v146, s70\n"
  "v_perm_b32 v63, v158, v154, s70\n"
  "v_mfma_f32_16x16x32_bf16 v[240:243], v[56:59], a[72:75], v[240:243]\n"
  "ds_read_b128 a[136:139], v19 offset:8192\n"
  "ds_read_b128 a[140:143], v49 offset:8192\n"
  "v_mfma_f32_16x16x32_bf16 a[16:19], v[56:59], a[76:79], a[16:19]\n"
  "ds_read_b128 a[96:99], v12 offset:12288\n"
  "v_mfma_f32_16x16x32_bf16 a[48:51], v[56:59], a[80:83], a[48:51]\n"
  "v_mfma_f32_16x16x32_bf16 v[180:183], v[60:63], a[64:67], v[180:183]\n"
  "v_perm_b32 v56, v135, v131, s69\n"
  "v_perm_b32 v57, v143, v139, s69\n"
  "v_mfma_f32_16x16x32_bf16 v[212:215], v[60:63], a[68:71], v[212:215]\n"
  "v_perm_b32 v58, v151, v147, s69\n"
  "v_perm_b32 v59, v159, v155, s69\n"
  "v_mfma_f32_16x16x32_bf16 v[244:247], v[60:63], a[72:75], v[244:247]\n"
  "ds_read_b128 a[144:147], v50 offset:8192\n"
  "ds_read_b128 a[148:151], v51 offset:8192\n"
  "v_mfma_f32_16x16x32_bf16 a[20:23], v[60:63], a[76:79], a[20:23]\n"
  "ds_read_b128 a[100:103], v12 offset:16384\n"
  "v_mfma_f32_16x16x32_bf16 a[52:55], v[60:63], a[80:83], a[52:55]\n"
  "v_mfma_f32_16x16x32_bf16 v[184:187], v[56:59], a[64:67], v[184:187]\n"
  "v_perm_b32 v60, v135, v131, s70\n"
  "v_perm_b32 v61, v143, v139, s70\n"
  "v_mfma_f32_16x16x32_bf16 v[216:219], v[56:59], a[68:71], v[216:219]\n"
  "v_perm_b32 v62, v151, v147, s70\n"
  "v_perm_b32 v63, v159, v155, s70\n"
  "v_mfma_f32_16x16x32_bf16 v[248:251], v[56:59], a[72:75], v[248:251]\n"
  "ds_read_b128 a[152:155], v19 offset:12288\n"
  "ds_read_b128 a[156:159], v49 offset:12288\n"
  "v_mfma_f32_16x16x32_bf16 a[24:27], v[56:59], a[76:79], a[24:27]\n"
  "ds_read_b128 a[160:163], v50 offset:12288\n"
  "ds_read_b128 a[164:167], v51 offset:12288\n"
  "v_mfma_f32_16x16x32_bf16 a[56:59], v[56:59], a[80:83], a[56:59]\n"
  "s_waitcnt vmcnt(7)\n"
  "v_mfma_f32_16x16x32_bf16 v[188:191], v[60:63], a[64:67], v[188:191]\n"
  "v_perm_b32 v96, v68, v64, s69\n"
  "v_perm_b32 v97, v76, v72, s69\n"
  "v_mfma_f32_16x16x32_bf16 v[220:223], v[60:63], a[68:71], v[220:223]\n"
  "v_perm_b32 v98, v84, v80, s69\n"
  "v_perm_b32 v99, v92, v88, s69\n"
  "v_mfma_f32_16x16x32_bf16 v[252:255], v[60:63], a[72:75], v[252:255]\n"
  "ds_read_b128 a[168:171], v19 offset:16384\n"
  "ds_read_b128 a[172:175], v49 offset:16384\n"
  "v_mfma_f32_16x16x32_bf16 a[28:31], v[60:63], a[76:79], a[28:31]\n"
  "ds_read_b128 a[176:179], v50 offset:16384\n"
  "ds_read_b128 a[180:183], v51 offset:16384\n"
  "v_mfma_f32_16x16x32_bf16 a[60:63], v[60:63], a[80:83], a[60:63]\n"
  "s_branch Lj_%=\n"
  "Lv2_%=:\n"
  "s_waitcnt vmcnt(13)\n"
  "s_barrier\n"
  "ds_read_b128 a[64:67], v12\n"
  "ds_read_b128 a[68:71], v12 offset:4096\n"
  "ds_read_b128 a[72:75], v12 offset:8192\n"
  "ds_read_b128 a[76:79], v12 offset:12288\n"
  "ds_read_b128 a[80:83], v12 offset:16384\n"
  "buffer_load_dwordx4 v[96:99], v2, s[44:47], s72 offen\n"
  "buffer_load_dwordx4 v[100:103], v3, s[44:47], s72 offen\n"
  "buffer_load_dwordx4 v[104:107], v4, s[44:47], s72 offen\n"
  "buffer_load_dwordx4 v[108:111], v5, s[44:47], s72 offen\n"
  "buffer_load_dwordx4 v[112:115], v6, s[44:47], s72 offen\n"
  "buffer_load_dwordx4 v[116:119], v7, s[44:47], s72 offen\n"
  "buffer_load_dwordx4 v[120:123], v8, s[44:47], s72 offen\n"
  "buffer_load_dwordx4 v[124:127], v9, s[44:47], s72 offen\n"
  "s_xor_b32 s72, s67, 0x80000\n"
  "buffer_load_dwordx4 v[128:131], v2, s[44:47], s72 offen\n"
  "s_waitcnt vmcnt(20)\n"
  "v_perm_b32 v56, v68, v64, s69\n"
  "s_waitcnt vmcnt(18)\n"
  "v_perm_b32 v57, v76, v72, s69\n"
  "s_waitcnt vmcnt(16)\n"
  "v_perm_b32 v58, v84, v80, s69\n"
  "s_waitcnt vmcnt(14)\n"
  "v_perm_b32 v59, v92, v88, s69\n"
  "s_waitcnt vmcnt(13)\n"
  "v_readfirstlane_b32 s90, v54\n"
  "v_readfirstlane_b32 s91, v55\n"
  "s_add_u32 s90, s90, 0x7f4a7c15\n"
  "s_addc_u32 s91, s91, 0x9e3779b9\n"
  "s_waitcnt lgkmcnt(4)\n"
  "v_mfma_f32_16x16x32_bf16 v[160:163], v[56:59], a[64:67], 0\n"
  "v_perm_b32 v60, v68, v64, s70\n"
  "v_perm_b32 v61, v76, v72, s70\n"
  "s_waitcnt lgkmcnt(3)\n"
  "v_mfma_f32_16x16x32_bf16 v[192:195], v[56:59], a[68:71], 0\n"
  "v_perm_b32 v62, v84, v80, s70\n"
  "v_perm_b32 v63, v92, v88, s70\n"
  "s_waitcnt lgkmcnt(2)\n"
  "v_mfma_f32_16x16x32_bf16 v[224:227], v[56:59], a[72:75], 0\n"
  "s_add_u32 m0, s96, 0x5000\n"
  "s_xor_b32 s73, s68, 0x100\n"
  "buffer_load_dwordx4 v10, s[32:35], s73 offen lds\n"
  "s_waitcnt lgkmcnt(1)\n"
  "v_mfma_f32_16x16x32_bf16 a[0:3], v[56:59], a[76:79], 0\n"
  "v_xor_b32 v12, 64, v11\n"
  "s_waitcnt lgkmcnt(0)\n"
  "v_mfma_f32_16x16x32_bf16 a[32:35], v[56:59], a[80:83], 0\n"
  "s_add_u32 m0, s96, 0x6000\n"
  "s_xor_b32 s73, s68, 0x4100\n"
  "buffer_load_dwordx4 v10, s[32:35], s73 offen lds\n"
  "v_mfma_f32_16x16x32_bf16 v[164:167], v[60:63], a[64:67], 0\n"
  "v_perm_b32 v56, v69, v65, s69\n"
  "v_perm_b32 v57, v77, v73, s69\n"
  "s_add_u32 m0, s96, 0x7000\n"
  "s_xor_b32 s73, s68, 0x8100\n"
  "buffer_load_dwordx4 v10, s[32:35], s73 offen lds\n"
  "v_mfma_f32_16x16x32_bf16 v[196:199], v[60:63], a[68:71], 0\n"
  "v_perm_b32 v58, v85, v81, s69\n"
  "v_perm_b32 v59, v93, v89, s69\n"
  "v_mfma_f32_16x16x32_bf16 v[228:231], v[60:63], a[72:75], 0\n"
  "s_add_u32 m0, s96, 0x8000\n"
  "s_xor_b32 s73, s68, 0xc100\n"
  "buffer_load_dwordx4 v10, s[32:35], s73 offen lds\n"
  "v_mfma_f32_16x16x32_bf16 a[4:7], v[60:63], a[76:79], 0\n"
  "ds_read_b128 a[84:87], v12\n"
  "v_mfma_f32_16x16x32_bf16 a[36:39], v[60:63], a[80:83], 0\n"
  "s_add_u32 m0, s96, 0x9000\n"
  "s_xor_b32 s73, s68, 0x10100\n"
  "buffer_load_dwordx4 v10, s[32:35], s73 offen lds\n"
  "v_mfma_f32_16x16x32_bf16 v[168:171], v[56:59], a[64:67], 0\n"
  "v_perm_b32 v60, v69, v65, s70\n"
  "v_perm_b32 v61, v77, v73, s70\n"
  "v_mfma_f32_16x16x32_bf16 v[200:203], v[56:59], a[68:71], 0\n"
  "v_perm_b32 v62, v85, v81, s70\n"
  "v_perm_b32 v63, v93, v89, s70\n"
  "v_mfma_f32_16x16x32_bf16 v[232:235], v[56:59], a[72:75], 0\n"
  "v_mfma_f32_16x16x32_bf16 a[8:11], v[56:59], a[76:79], 0\n"
  "ds_read_b128 a[88:91], v12 offset:4096\n"
  "v_mfma_f32_16x16x32_bf16 a[40:43], v[56:59], a[80:83], 0\n"
  "v_mfma_f32_16x16x32_bf16 v[172:175], v[60:63], a[64:67], 0\n"
  "v_perm_b32 v56, v70, v66, s69\n"
  "v_perm_b32 v57, v78, v74, s69\n"
  "v_mfma_f32_16x16x32_bf16 v[204:207], v[60:63], a[68:71], 0\n"
  "v_perm_b32 v58, v86, v82, s69\n"
  "v_perm_b32 v59, v94, v90, s69\n"
  "v_mfma_f32_16x16x32_bf16 v[236:239], v[60:63], a[72:75], 0\n"
  "v_mfma_f32_16x16x32_bf16 a[12:15], v[60:63], a[76:79], 0\n"
  "ds_read_b128 a[92:95], v12 offset:8192\n"
  "v_mfma_f32_16x16x32_bf16 a[44:47], v[60:63], a[80:83], 0\n"
  "v_mfma_f32_16x16x32_bf16 v[176:179], v[56:59], a[64:67], 0\n"
  "v_perm_b32 v60, v70, v66, s70\n"
  "v_perm_b32 v61, v78, v74, s70\n"
  "v_mfma_f32_16x16x32_bf16 v[208:211], v[56:59], a[68:71], 0\n"
  "v_perm_b32 v62, v86, v82, s70\n"
  "v_perm_b32 v63, v94, v90, s70\n"
  "v_mfma_f32_16x16x32_bf16 v[240:243], v[56:59], a[72:75], 0\n"
  "v_mfma_f32_16x16x32_bf16 a[16:19], v[56:59], a[76:79], 0\n"
  "ds_read_b128 a[96:99], v12 offset:12288\n"
  "v_mfma_f32_16x16x32_bf16 a[48:51], v[56:59], a[80:83], 0\n"
  "buffer_load_dwordx4 v[132:135], v3, s[44:47], s72 offen\n"
  "v_mfma_f32_16x16x32_bf16 v[180:183], v[60:63], a[64:67], 0\n"
  "v_perm_b32 v56, v71, v67, s69\n"
  "v_perm_b32 v57, v79, v75, s69\n"
  "v_mfma_f32_16x16x32_bf16 v[212:215], v[60:63], a[68:71], 0\n"
  "v_perm_b32 v58, v87, v83, s69\n"
  "v_perm_b32 v59, v95, v91, s69\n"
  "buffer_load_dwordx4 v[136:139], v4, s[44:47], s72 offen\n"
  "v_mfma_f32_16x16x32_bf16 v[244:247], v[60:63], a[72:75], 0\n"
  "v_mfma_f32_16x16x32_bf16 a[20:23], v[60:63], a[76:79], 0\n"
  "buffer_load_dwordx4 v[140:143], v5, s[44:47], s72 offen\n"
  "ds_read_b128 a[100:103], v12 offset:16384\n"
  "v_mfma_f32_16x16x32_bf16 a[52:55], v[60:63], a[80:83], 0\n"
  "v_mfma_f32_16x16x32_bf16 v[184:187], v[56:59], a[64:67], 0\n"
  "v_perm_b32 v60, v71, v67, s70\n"
  "v_perm_b32 v61, v79, v75, s70\n"
  "buffer_load_dwordx4 v[144:147], v6, s[44:47], s72 offen\n"
  "v_mfma_f32_16x16x32_bf16 v[216:219], v[56:59], a[68:71], 0\n"
  "v_perm_b32 v62, v87, v83, s70\n"
  "v_perm_b32 v63, v95, v91, s70\n"
  "v_mfma_f32_16x16x32_bf16 v[248:251], v[56:59], a[72:75], 0\n"
  "buffer_load_dwordx4 v[148:151], v7, s[44:47], s72 offen\n"
  "v_mfma_f32_16x16x32_bf16 a[24:27], v[56:59], a[76:79], 0\n"
  "v_mfma_f32_16x16x32_bf16 a[56:59], v[56:59], a[80:83], 0\n"
  "s_waitcnt vmcnt(11)\n"
  "buffer_load_dwordx4 v[152:155], v8, s[44:47], s72 offen\n"
  "v_mfma_f32_16x16x32_bf16 v[188:191], v[60:63], a[64:67], 0\n"
  "v_perm_b32 v56, v100, v96, s69\n"
  "v_perm_b32 v57, v108, v104, s69\n"
  "v_mfma_f32_16x16x32_bf16 v[220:223], v[60:63], a[68:71], 0\n"
  "v_perm_b32 v58, v116, v112, s69\n"
  "v_perm_b32 v59, v124, v120, s69\n"
  "buffer_load_dwordx4 v[156:159], v9, s[44:47], s72 offen\n"
  "v_mfma_f32_16x16x32_bf16 v[252:255], v[60:63], a[72:75], 0\n"
  "v_mfma_f32_16x16x32_bf16 a[28:31], v[60:63], a[76:79], 0\n"
  "s_waitcnt lgkmcnt(0)\n"
  "v_mfma_f32_16x16x32_bf16 a[60:63], v[60:63], a[80:83], 0\n"
  "v_mfma_f32_16x16x32_bf16 v[160:163], v[56:59], a[84:87], v[160:163]\n"
  "v_perm_b32 v60, v100, v96, s70\n"
  "v_perm_b32 v61, v108, v104, s70\n"
  "s_xor_b32 s72, s67, 0xc0000\n"
  "buffer_load_dwordx4 v[64:67], v2, s[44:47], s72 offen\n"
  "v_mfma_f32_16x16x32_bf16 v[192:195], v[56:59], a[88:91], v[192:195]\n"
  "v_perm_b32 v62, v116, v112, s70\n"
  "v_perm_b32 v63, v124, v120, s70\n"
  "v_mfma_f32_16x16x32_bf16 v[224:227], v[56:59], a[92:95], v[224:227]\n"
  "buffer_load_dwordx4 v[68:71], v3, s[44:47], s72 offen\n"
  "v_mfma_f32_16x16x32_bf16 a[0:3], v[56:59], a[96:99], a[0:3]\n"
  "v_xor_b32 v12, 0x80, v11\n"
  "v_mfma_f32_16x16x32_bf16 a[32:35], v[56:59], a[100:103], a[32:35]\n"
  "buffer_load_dwordx4 v[72:75], v4, s[44:47], s72 offen\n"
  "v_mfma_f32_16x16x32_bf16 v[164:167], v[60:63], a[84:87], v[164:167]\n"
  "v_perm_b32 v56, v101, v97, s69\n"
  "v_perm_b32 v57, v109, v105, s69\n"
  "v_mfma_f32_16x16x32_bf16 v[196:199], v[60:63], a[88:91], v[196:199]\n"
  "v_perm_b32 v58, v117, v113, s69\n"
  "v_perm_b32 v59, v125, v121, s69\n"
  "buffer_load_dwordx4 v[76:79], v5, s[44:47], s72 offen\n"
  "v_mfma_f32_16x16x32_bf16 v[228:231], v[60:63], a[92:95], v[228:231]\n"
  "v_mfma_f32_16x16x32_bf16 a[4:7], v[60:63], a[96:99], a[4:7]\n"
  "buffer_load_dwordx4 v[80:83], v6, s[44:47], s72 offen\n"
  "ds_read_b128 a[64:67], v12\n"
  "v_mfma_f32_16x16x32_bf16 a[36:39], v[60:63], a[100:103], a[36:39]\n"
  "v_mfma_f32_16x16x32_bf16 v[168:171], v[56:59], a[84:87], v[168:171]\n"
  "v_perm_b32 v60, v101, v97, s70\n"
  "v_perm_b32 v61, v109, v105, s70\n"
  "buffer_load_dwordx4 v[84:87], v7, s[44:47], s72 offen\n"
  "v_mfma_f32_16x16x32_bf16 v[200:203], v[56:59], a[88:91], v[200:203]\n"
  "v_perm_b32 v62, v117, v113, s70\n"
  "v_perm_b32 v63, v125, v121, s70\n"
  "v_mfma_f32_16x16x32_bf16 v[232:235], v[56:59], a[92:95], v[232:235]\n"
  "buffer_load_dwordx4 v[88:91], v8, s[44:47], s72 offen\n"
  "v_mfma_f32_16x16x32_bf16 a[8:11], v[56:59], a[96:99], a[8:11]\n"
  "ds_read_b128 a[68:71], v12 offset:4096\n"
  "v_mfma_f32_16x16x32_bf16 a[40:43], v[56:59], a[100:103], a[40:43]\n"
  "buffer_load_dwordx4 v[92:95], v9, s[44:47], s72 offen\n"
  "v_mfma_f32_16x16x32_bf16 v[172:175], v[60:63], a[84:87], v[172:175]\n"
  "v_perm_b32 v56, v102, v98, s69\n"
  "v_perm_b32 v57, v110, v106, s69\n"
  "v_mfma_f32_16x16x32_bf16 v[204:207], v[60:63], a[88:91], v[204:207]\n"
  "v_perm_b32 v58, v118, v114, s69\n"
  "v_perm_b32 v59, v126, v122, s69\n"
  "v_mfma_f32_16x16x32_bf16 v[236:239], v[60:63], a[92:95], v[236:239]\n"
  "v_mfma_f32_16x16x32_bf16 a[12:15], v[60:63], a[96:99], a[12:15]\n"
  "ds_read_b128 a[72:75], v12 offset:8192\n"
  "v_mfma_f32_16x16x32_bf16 a[44:47], v[60:63], a[100:103], a[44:47]\n"
  "v_mfma_f32_16x16x32_bf16 v[176:179], v[56:59], a[84:87], v[176:179]\n"
  "v_perm_b32 v60, v102, v98, s70\n"
  "v_perm_b32 v61, v110, v106, s70\n"
  "v_mfma_f32_16x16x32_bf16 v[208:211], v[56:59], a[88:91], v[208:211]\n"
  "v_perm_b32 v62, v118, v114, s70\n"
  "v_perm_b32 v63, v126, v122, s70\n"
  "v_mfma_f32_16x16x32_bf16 v[240:243], v[56:59], a[92:95], v[240:243]\n"
  "v_mfma_f32_16x16x32_bf16 a[16:19], v[56:59], a[96:99], a[16:19]\n"
  "ds_read_b128 a[76:79], v12 offset:12288\n"
  "v_mfma_f32_16x16x32_bf16 a[48:51], v[56:59], a[100:103], a[48:51]\n"
  "v_mfma_f32_16x16x32_bf16 v[180:183], v[60:63], a[84:87], v[180:183]\n"
  "v_perm_b32 v56, v103, v99, s69\n"
  "v_perm_b32 v57, v111, v107, s69\n"
  "v_mfma_f32_16x16x32_bf16 v[212:215], v[60:63], a[88:91], v[212:215]\n"
  "v_perm_b32 v58, v119, v115, s69\n"
  "v_perm_b32 v59, v127, v123, s69\n"
  "v_mfma_f32_16x16x32_bf16 v[244:247], v[60:63], a[92:95], v[244:247]\n"
  "v_mfma_f32_16x16x32_bf16 a[20:23], v[60:63], a[96:99], a[20:23]\n"
  "ds_read_b128 a[80:83], v12 offset:16384\n"
  "v_mfma_f32_16x16x32_bf16 a[52:55], v[60:63], a[100:103], a[52:55]\n"
  "v_mfma_f32_16x16x32_bf16 v[184:187], v[56:59], a[84:87], v[184:187]\n"
  "v_perm_b32 v60, v103, v99, s70\n"
  "v_perm_b32 v61, v111, v107, s70\n"
  "v_mfma_f32_16x16x32_bf16 v[216:219], v[56:59], a[88:91], v[216:219]\n"
  "v_perm_b32 v62, v119, v115, s70\n"
  "v_perm_b32 v63, v127, v123, s70\n"
  "v_mfma_f32_16x16x32_bf16 v[248:251], v[56:59], a[92:95], v[248:251]\n"
  "s_xor_b32 s72, s67, 0x100000\n"
  "buffer_load_dwordx4 v[96:99], v2, s[44:47], s72 offen\n"
  "v_mfma_f32_16x16x32_bf16 a[24:27], v[56:59], a[96:99], a[24:27]\n"
  "buffer_load_dwordx4 v[100:103], v3, s[44:47], s72 offen\n"
  "v_mfma_f32_16x16x32_bf16 a[56:59], v[56:59], a[100:103], a[56:59]\n"
  "s_waitcnt vmcnt(10)\n"
  "buffer_load_dwordx4 v[104:107], v4, s[44:47], s72 offen\n"
  "v_mfma_f32_16x16x32_bf16 v[188:191], v[60:63], a[84:87], v[188:191]\n"
  "v_perm_b32 v56, v132, v128, s69\n"
  "v_perm_b32 v57, v140, v136, s69\n"
  "buffer_load_dwordx4 v[108:111], v5, s[44:47], s72 offen\n"
  "v_mfma_f32_16x16x32_bf16 v[220:223], v[60:63], a[88:91], v[220:223]\n"
  "v_perm_b32 v58, v148, v144, s69\n"
  "v_perm_b32 v59, v156, v152, s69\n"
  "buffer_load_dwordx4 v[112:115], v6, s[44:47], s72 offen\n"
  "v_mfma_f32_16x16x32_bf16 v[252:255], v[60:63], a[92:95], v[252:255]\n"
  "buffer_load_dwordx4 v[116:119], v7, s[44:47], s72 offen\n"
  "v_mfma_f32_16x16x32_bf16 a[28:31], v[60:63], a[96:99], a[28:31]\n"
  "s_waitcnt lgkmcnt(0)\n"
  "buffer_load_dwordx4 v[120:123], v8, s[44:47], s72 offen\n"
  "v_mfma_f32_16x16x32_bf16 a[60:63], v[60:63], a[100:103], a[60:63]\n"
  "buffer_load_dwordx4 v[124:127], v9, s[44:47], s72 offen\n"
  "v_mfma_f32_16x16x32_bf16 v[160:163], v[56:59], a[64:67], v[160:163]\n"
  "v_perm_b32 v60, v132, v128, s70\n"
  "v_perm_b32 v61, v140, v136, s70\n"
  "v_mfma_f32_16x16x32_bf16 v[192:195], v[56:59], a[68:71], v[192:195]\n"
  "v_perm_b32 v62, v148, v144, s70\n"
  "v_perm_b32 v63, v156, v152, s70\n"
  "v_mfma_f32_16x16x32_bf16 v[224:227], v[56:59], a[72:75], v[224:227]\n"
  "s_add_u32 m0, s77, 0x1000\n"
  "s_nop 0\n"
  "buffer_load_dwordx4 v13, s[36:39], s92 offen sc1 nt lds\n"
  "v_mfma_f32_16x16x32_bf16 a[0:3], v[56:59], a[76:79], a[0:3]\n"
  "v_xor_b32 v12, 0xc0, v11\n"
  "v_mfma_f32_16x16x32_bf16 a[32:35], v[56:59], a[80:83], a[32:35]\n"
  "buffer_load_dwordx4 v14, s[36:39], s92 offen offset:1024 sc1 nt lds\n"
  "v_mfma_f32_16x16x32_bf16 v[164:167], v[60:63], a[64:67], v[164:167]\n"
  "v_perm_b32 v56, v133, v129, s69\n"
  "v_perm_b32 v57, v141, v137, s69\n"
  "buffer_load_dwordx4 v15, s[36:39], s92 offen offset:2048 sc1 nt lds\n"
  "v_mfma_f32_16x16x32_bf16 v[196:199], v[60:63], a[68:71], v[196:199]\n"
  "v_perm_b32 v58, v149, v145, s69\n"
  "v_perm_b32 v59, v157, v153, s69\n"
  "v_mfma_f32_16x16x32_bf16 v[228:231], v[60:63], a[72:75], v[228:231]\n"
  "v_mfma_f32_16x16x32_bf16 a[4:7], v[60:63], a[76:79], a[4:7]\n"
  "ds_read_b128 a[84:87], v12\n"
  "v_mfma_f32_16x16x32_bf16 a[36:39], v[60:63], a[80:83], a[36:39]\n"
  "v_mfma_f32_16x16x32_bf16 v[168:171], v[56:59], a[64:67], v[168:171]\n"
  "v_perm_b32 v60, v133, v129, s70\n"
  "v_perm_b32 v61, v141, v137, s70\n"
  "v_mfma_f32_16x16x32_bf16 v[200:203], v[56:59], a[68:71], v[200:203]\n"
  "v_perm_b32 v62, v149, v145, s70\n"
  "v_perm_b32 v63, v157, v153, s70\n"
  "v_mfma_f32_16x16x32_bf16 v[232:235], v[56:59], a[72:75], v[232:235]\n"
  "v_mfma_f32_16x16x32_bf16 a[8:11], v[56:59], a[76:79], a[8:11]\n"
  "ds_read_b128 a[88:91], v12 offset:4096\n"
  "v_mfma_f32_16x16x32_bf16 a[40:43], v[56:59], a[80:83], a[40:43]\n"
  "v_mfma_f32_16x16x32_bf16 v[172:175], v[60:63], a[64:67], v[172:175]\n"
  "v_perm_b32 v56, v134, v130, s69\n"
  "v_perm_b32 v57, v142, v138, s69\n"
  "v_mfma_f32_16x16x32_bf16 v[204:207], v[60:63], a[68:71], v[204:207]\n"
  "v_perm_b32 v58, v150, v146, s69\n"
  "v_perm_b32 v59, v158, v154, s69\n"
  "v_mfma_f32_16x16x32_bf16 v[236:239], v[60:63], a[72:75], v[236:239]\n"
  "v_mfma_f32_16x16x32_bf16 a[12:15], v[60:63], a[76:79], a[12:15]\n"
  "ds_read_b128 a[92:95], v12 offset:8192\n"
  "v_mfma_f32_16x16x32_bf16 a[44:47], v[60:63], a[80:83], a[44:47]\n"
  "v_mfma_f32_16x16x32_bf16 v[176:179], v[56:59], a[64:67], v[176:179]\n"
  "v_perm_b32 v60, v134, v130, s70\n"
  "v_perm_b32 v61, v142, v138, s70\n"
  "v_mfma_f32_16x16x32_bf16 v[208:211], v[56:59], a[68:71], v[208:211]\n"
  "v_perm_b32 v62, v150, v146, s70\n"
  "v_perm_b32 v63, v158, v154, s70\n"
  "v_mfma_f32_16x16x32_bf16 v[240:243], v[56:59], a[72:75], v[240:243]\n"
  "v_mfma_f32_16x16x32_bf16 a[16:19], v[56:59], a[76:79], a[16:19]\n"
  "ds_read_b128 a[96:99], v12 offset:12288\n"
  "v_mfma_f32_16x16x32_bf16 a[48:51], v[56:59], a[80:83], a[48:51]\n"
  "v_mfma_f32_16x16x32_bf16 v[180:183], v[60:63], a[64:67], v[180:183]\n"
  "v_perm_b32 v56, v135, v131, s69\n"
  "v_perm_b32 v57, v143, v139, s69\n"
  "v_mfma_f32_16x16x32_bf16 v[212:215], v[60:63], a[68:71], v[212:215]\n"
  "v_perm_b32 v58, v151, v147, s69\n"
  "v_perm_b32 v59, v159, v155, s69\n"
  "v_mfma_f32_16x16x32_bf16 v[244:247], v[60:63], a[72:75], v[244:247]\n"
  "v_mfma_f32_16x16x32_bf16 a[20:23], v[60:63], a[76:79], a[20:23]\n"
  "ds_read_b128 a[100:103], v12 offset:16384\n"
  "v_mfma_f32_16x16x32_bf16 a[52:55], v[60:63], a[80:83], a[52:55]\n"
  "v_mfma_f32_16x16x32_bf16 v[184:187], v[56:59], a[64:67], v[184:187]\n"
  "v_perm_b32 v60, v135, v131, s70\n"
  "v_perm_b32 v61, v143, v139, s70\n"
  "v_mfma_f32_16x16x32_bf16 v[216:219], v[56:59], a[68:71], v[216:219]\n"
  "v_perm_b32 v62, v151, v147, s70\n"
  "v_perm_b32 v63, v159, v155, s70\n"
  "v_mfma_f32_16x16x32_bf16 v[248:251], v[56:59], a[72:75], v[248:251]\n"
  "v_mfma_f32_16x16x32_bf16 a[24:27], v[56:59], a[76:79], a[24:27]\n"
  "v_mfma_f32_16x16x32_bf16 a[56:59], v[56:59], a[80:83], a[56:59]\n"
  "s_waitcnt vmcnt(11)\n"
  "v_mfma_f32_16x16x32_bf16 v[188:191], v[60:63], a[64:67], v[188:191]\n"
  "v_perm_b32 v56, v68, v64, s69\n"
  "v_perm_b32 v57, v76, v72, s69\n"
  "v_mfma_f32_16x16x32_bf16 v[220:223], v[60:63], a[68:71], v[220:223]\n"
  "v_perm_b32 v58, v84, v80, s69\n"
  "v_perm_b32 v59, v92, v88, s69\n"
  "v_mfma_f32_16x16x32_bf16 v[252:255], v[60:63], a[72:75], v[252:255]\n"
  "v_mfma_f32_16x16x32_bf16 a[28:31], v[60:63], a[76:79], a[28:31]\n"
  "s_waitcnt lgkmcnt(0)\n"
  "v_mfma_f32_16x16x32_bf16 a[60:63], v[60:63], a[80:83], a[60:63]\n"
  "v_mfma_f32_16x16x32_bf16 v[160:163], v[56:59], a[84:87], v[160:163]\n"
  "v_perm_b32 v60, v68, v64, s70\n"
  "v_perm_b32 v61, v76, v72, s70\n"
  "v_mfma_f32_16x16x32_bf16 v[192:195], v[56:59], a[88:91], v[192:195]\n"
  "v_perm_b32 v62, v84, v80, s70\n"
  "v_perm_b32 v63, v92, v88, s70\n"
  "v_mfma_f32_16x16x32_bf16 v[224:227], v[56:59], a[92:95], v[224:227]\n"
  "v_mfma_f32_16x16x32_bf16 a[0:3], v[56:59], a[96:99], a[0:3]\n"
  "v_xor_b32 v12, 0x5000, v11\n"
  "v_mfma_f32_16x16x32_bf16 a[32:35], v[56:59], a[100:103], a[32:35]\n"
  "s_barrier\n"
  "v_mfma_f32_16x16x32_bf16 v[164:167], v[60:63], a[84:87], v[164:167]\n"
  "v_perm_b32 v56, v69, v65, s69\n"
  "v_perm_b32 v57, v77, v73, s69\n"
  "v_mfma_f32_16x16x32_bf16 v[196:199], v[60:63], a[88:91], v[196:199]\n"
  "v_perm_b32 v58, v85, v81, s69\n"
  "v_perm_b32 v59, v93, v89, s69\n"
  "v_mfma_f32_16x16x32_bf16 v[228:231], v[60:63], a[92:95], v[228:231]\n"
  "v_mfma_f32_16x16x32_bf16 a[4:7], v[60:63], a[96:99], a[4:7]\n"
  "ds_read_b128 a[64:67], v12\n"
  "v_mfma_f32_16x16x32_bf16 a[36:39], v[60:63], a[100:103], a[36:39]\n"
  "v_mfma_f32_16x16x32_bf16 v[168:171], v[56:59], a[84:87], v[168:171]\n"
  "v_perm_b32 v60, v69, v65, s70\n"
  "v_perm_b32 v61, v77, v73, s70\n"
  "v_mfma_f32_16x16x32_bf16 v[200:203], v[56:59], a[88:91], v[200:203]\n"
  "v_perm_b32 v62, v85, v81, s70\n"
  "v_perm_b32 v63, v93, v89, s70\n"
  "v_mfma_f32_16x16x32_bf16 v[232:235], v[56:59], a[92:95], v[232:235]\n"
  "v_mfma_f32_16x16x32_bf16 a[8:11], v[56:59], a[96:99], a[8:11]\n"
  "ds_read_b128 a[68:71], v12 offset:4096\n"
  "v_mfma_f32_16x16x32_bf16 a[40:43], v[56:59], a[100:103], a[40:43]\n"
  "v_mfma_f32_16x16x32_bf16 v[172:175], v[60:63], a[84:87], v[172:175]\n"
  "v_perm_b32 v56, v70, v66, s69\n"
  "v_perm_b32 v57, v78, v74, s69\n"
  "v_mfma_f32_16x16x32_bf16 v[204:207], v[60:63], a[88:91], v[204:207]\n"
  "v_perm_b32 v58, v86, v82, s69\n"
  "v_perm_b32 v59, v94, v90, s69\n"
  "v_mfma_f32_16x16x32_bf16 v[236:239], v[60:63], a[92:95], v[236:239]\n"
  "v_mfma_f32_16x16x32_bf16 a[12:15], v[60:63], a[96:99], a[12:15]\n"
  "ds_read_b128 a[72:75], v12 offset:8192\n"
  "v_mfma_f32_16x16x32_bf16 a[44:47], v[60:63], a[100:103], a[44:47]\n"
  "v_mfma_f32_16x16x32_bf16 v[176:179], v[56:59], a[84:87], v[176:179]\n"
  "v_perm_b32 v60, v70, v66, s70\n"
  "v_perm_b32 v61, v78, v74, s70\n"
  "v_mfma_f32_16x16x32_bf16 v[208:211], v[56:59], a[88:91], v[208:211]\n"
  "v_perm_b32 v62, v86, v82, s70\n"
  "v_perm_b32 v63, v94, v90, s70\n"
  "v_mfma_f32_16x16x32_bf16 v[240:243], v[56:59], a[92:95], v[240:243]\n"
  "s_xor_b32 s72, s67, 0x140000\n"
  "buffer_load_dwordx4 v[128:131], v2, s[44:47], s72 offen\n"
  "v_mfma_f32_16x16x32_bf16 a[16:19], v[56:59], a[96:99], a[16:19]\n"
  "ds_read_b128 a[76:79], v12 offset:12288\n"
  "v_mfma_f32_16x16x32_bf16 a[48:51], v[56:59], a[100:103], a[48:51]\n"
  "buffer_load_dwordx4 v[132:135], v3, s[44:47], s72 offen\n"
  "v_mfma_f32_16x16x32_bf16 v[180:183], v[60:63], a[84:87], v[180:183]\n"
  "v_perm_b32 v56, v71, v67, s69\n"
  "v_perm_b32 v57, v79, v75, s69\n"
  "v_mfma_f32_16x16x32_bf16 v[212:215], v[60:63], a[88:91], v[212:215]\n"
  "v_perm_b32 v58, v87, v83, s69\n"
  "v_perm_b32 v59, v95, v91, s69\n"
  "buffer_load_dwordx4 v[136:139], v4, s[44:47], s72 offen\n"
  "v_mfma_f32_16x16x32_bf16 v[244:247], v[60:63], a[92:95], v[244:247]\n"
  "v_mfma_f32_16x16x32_bf16 a[20:23], v[60:63], a[96:99], a[20:23]\n"
  "buffer_load_dwordx4 v[140:143], v5, s[44:47], s72 offen\n"
  "ds_read_b128 a[80:83], v12 offset:16384\n"
  "v_mfma_f32_16x16x32_bf16 a[52:55], v[60:63], a[100:103], a[52:55]\n"
  "v_mfma_f32_16x16x32_bf16 v[184:187], v[56:59], a[84:87], v[184:187]\n"
  "v_perm_b32 v60, v71, v67, s70\n"
  "v_perm_b32 v61, v79, v75, s70\n"
  "buffer_load_dwordx4 v[144:147], v6, s[44:47], s72 offen\n"
  "v_mfma_f32_16x16x32_bf16 v[216:219], v[56:59], a[88:91], v[216:219]\n"
  "v_perm_b32 v62, v87, v83, s70\n"
  "v_perm_b32 v63, v95, v91, s70\n"
  "v_mfma_f32_16x16x32_bf16 v[248:251], v[56:59], a[92:95], v[248:251]\n"
  "buffer_load_dwordx4 v[148:151], v7, s[44:47], s72 offen\n"
  "v_mfma_f32_16x16x32_bf16 a[24:27], v[56:59], a[96:99], a[24:27]\n"
  "v_mfma_f32_16x16x32_bf16 a[56:59], v[56:59], a[100:103], a[56:59]\n"
  "s_waitcnt vmcnt(9)\n"
  "buffer_load_dwordx4 v[152:155], v8, s[44:47], s72 offen\n"
  "v_mfma_f32_16x16x32_bf16 v[188:191], v[60:63], a[84:87], v[188:191]\n"
  "v_perm_b32 v56, v100, v96, s69\n"
  "v_perm_b32 v57, v108, v104, s69\n"
  "v_mfma_f32_16x16x32_bf16 v[220:223], v[60:63], a[88:91], v[220:223]\n"
  "v_perm_b32 v58, v116, v112, s69\n"
  "v_perm_b32 v59, v124, v120, s69\n"
  "buffer_load_dwordx4 v[156:159], v9, s[44:47], s72 offen\n"
  "v_mfma_f32_16x16x32_bf16 v[252:255], v[60:63], a[92:95], v[252:255]\n"
  "v_mfma_f32_16x16x32_bf16 a[28:31], v[60:63], a[96:99], a[28:31]\n"
  "s_waitcnt lgkmcnt(0)\n"
  "v_mfma_f32_16x16x32_bf16 a[60:63], v[60:63], a[100:103], a[60:63]\n"
  "v_mfma_f32_16x16x32_bf16 v[160:163], v[56:59], a[64:67], v[160:163]\n"
  "v_perm_b32 v60, v100, v96, s70\n"
  "v_perm_b32 v61, v108, v104, s70\n"
  "s_xor_b32 s72, s67, 0x180000\n"
  "buffer_load_dwordx4 v[64:67], v2, s[44:47], s72 offen\n"
  "v_mfma_f32_16x16x32_bf16 v[192:195], v[56:59], a[68:71], v[192:195]\n"
  "v_perm_b32 v62, v116, v112, s70\n"
  "v_perm_b32 v63, v124, v120, s70\n"
  "v_mfma_f32_16x16x32_bf16 v[224:227], v[56:59], a[72:75], v[224:227]\n"
  "buffer_load_dwordx4 v[68:71], v3, s[44:47], s72 offen\n"
  "v_mfma_f32_16x16x32_bf16 a[0:3], v[56:59], a[76:79], a[0:3]\n"
  "v_xor_b32 v12, 0x5040, v11\n"
  "v_mfma_f32_16x16x32_bf16 a[32:35], v[56:59], a[80:83], a[32:35]\n"
  "buffer_load_dwordx4 v[72:75], v4, s[44:47], s72 offen\n"
  "v_mfma_f32_16x16x32_bf16 v[164:167], v[60:63], a[64:67], v[164:167]\n"
  "v_perm_b32 v56, v101, v97, s69\n"
  "v_perm_b32 v57, v109, v105, s69\n"
  "v_mfma_f32_16x16x32_bf16 v[196:199], v[60:63], a[68:71], v[196:199]\n"
  "v_perm_b32 v58, v117, v113, s69\n"
  "v_perm_b32 v59, v125, v121, s69\n"
  "buffer_load_dwordx4 v[76:79], v5, s[44:47], s72 offen\n"
  "v_mfma_f32_16x16x32_bf16 v[228:231], v[60:63], a[72:75], v[228:231]\n"
  "v_mfma_f32_16x16x32_bf16 a[4:7], v[60:63], a[76:79], a[4:7]\n"
  "buffer_load_dwordx4 v[80:83], v6, s[44:47], s72 offen\n"
  "ds_read_b128 a[84:87], v12\n"
  "v_mfma_f32_16x16x32_bf16 a[36:39], v[60:63], a[80:83], a[36:39]\n"
  "v_mfma_f32_16x16x32_bf16 v[168:171], v[56:59], a[64:67], v[168:171]\n"
  "v_perm_b32 v60, v101, v97, s70\n"
  "v_perm_b32 v61, v109, v105, s70\n"
  "buffer_load_dwordx4 v[84:87], v7, s[44:47], s72 offen\n"
  "v_mfma_f32_16x16x32_bf16 v[200:203], v[56:59], a[68:71], v[200:203]\n"
  "v_perm_b32 v62, v117, v113, s70\n"
  "v_perm_b32 v63, v125, v121, s70\n"
  "v_mfma_f32_16x16x32_bf16 v[232:235], v[56:59], a[72:75], v[232:235]\n"
  "buffer_load_dwordx4 v[88:91], v8, s[44:47], s72 offen\n"
  "v_mfma_f32_16x16x32_bf16 a[8:11], v[56:59], a[76:79], a[8:11]\n"
  "ds_read_b128 a[88:91], v12 offset:4096\n"
  "v_mfma_f32_16x16x32_bf16 a[40:43], v[56:59], a[80:83], a[40:43]\n"
  "buffer_load_dwordx4 v[92:95], v9, s[44:47], s72 offen\n"
  "v_mfma_f32_16x16x32_bf16 v[172:175], v[60:63], a[64:67], v[172:175]\n"
  "v_perm_b32 v56, v102, v98, s69\n"
  "v_perm_b32 v57, v110, v106, s69\n"
  "v_mfma_f32_16x16x32_bf16 v[204:207], v[60:63], a[68:71], v[204:207]\n"
  "v_perm_b32 v58, v118, v114, s69\n"
  "v_perm_b32 v59, v126, v122, s69\n"
  "v_mfma_f32_16x16x32_bf16 v[236:239], v[60:63], a[72:75], v[236:239]\n"
  "v_mfma_f32_16x16x32_bf16 a[12:15], v[60:63], a[76:79], a[12:15]\n"
  "ds_read_b128 a[92:95], v12 offset:8192\n"
  "v_mfma_f32_16x16x32_bf16 a[44:47], v[60:63], a[80:83], a[44:47]\n"
  "v_mfma_f32_16x16x32_bf16 v[176:179], v[56:59], a[64:67], v[176:179]\n"
  "v_perm_b32 v60, v102, v98, s70\n"
  "v_perm_b32 v61, v110, v106, s70\n"
  "v_mfma_f32_16x16x32_bf16 v[208:211], v[56:59], a[68:71], v[208:211]\n"
  "v_perm_b32 v62, v118, v114, s70\n"
  "v_perm_b32 v63, v126, v122, s70\n"
  "v_mfma_f32_16x16x32_bf16 v[240:243], v[56:59], a[72:75], v[240:243]\n"
  "v_mfma_f32_16x16x32_bf16 a[16:19], v[56:59], a[76:79], a[16:19]\n"
  "ds_read_b128 a[96:99], v12 offset:12288\n"
  "v_mfma_f32_16x16x32_bf16 a[48:51], v[56:59], a[80:83], a[48:51]\n"
  "v_mfma_f32_16x16x32_bf16 v[180:183], v[60:63], a[64:67], v[180:183]\n"
  "v_perm_b32 v56, v103, v99, s69\n"
  "v_perm_b32 v57, v111, v107, s69\n"
  "v_mfma_f32_16x16x32_bf16 v[212:215], v[60:63], a[68:71], v[212:215]\n"
  "v_perm_b32 v58, v119, v115, s69\n"
  "v_perm_b32 v59, v127, v123, s69\n"
  "v_mfma_f32_16x16x32_bf16 v[244:247], v[60:63], a[72:75], v[244:247]\n"
  "v_mfma_f32_16x16x32_bf16 a[20:23], v[60:63], a[76:79], a[20:23]\n"
  "ds_read_b128 a[100:103], v12 offset:16384\n"
  "v_mfma_f32_16x16x32_bf16 a[52:55], v[60:63], a[80:83], a[52:55]\n"
  "v_mfma_f32_16x16x32_bf16 v[184:187], v[56:59], a[64:67], v[184:187]\n"
  "v_perm_b32 v60, v103, v99, s70\n"
  "v_perm_b32 v61, v111, v107, s70\n"
  "v_mfma_f32_16x16x32_bf16 v[216:219], v[56:59], a[68:71], v[216:219]\n"
  "v_perm_b32 v62, v119, v115, s70\n"
  "v_perm_b32 v63, v127, v123, s70\n"
  "v_mfma_f32_16x16x32_bf16 v[248:251], v[56:59], a[72:75], v[248:251]\n"
  "s_xor_b32 s72, s67, 0x1c0000\n"
  "buffer_load_dwordx4 v[96:99], v2, s[44:47], s72 offen\n"
  "v_mfma_f32_16x16x32_bf16 a[24:27], v[56:59], a[76:79], a[24:27]\n"
  "buffer_load_dwordx4 v[100:103], v3, s[44:47], s72 offen\n"
  "v_mfma_f32_16x16x32_bf16 a[56:59], v[56:59], a[80:83], a[56:59]\n"
  "s_waitcnt vmcnt(10)\n"
  "buffer_load_dwordx4 v[104:107], v4, s[44:47], s72 offen\n"
  "v_mfma_f32_16x16x32_bf16 v[188:191], v[60:63], a[64:67], v[188:191]\n"
  "v_perm_b32 v56, v132, v128, s69\n"
  "v_perm_b32 v57, v140, v136, s69\n"
  "buffer_load_dwordx4 v[108:111], v5, s[44:47], s72 offen\n"
  "v_mfma_f32_16x16x32_bf16 v[220:223], v[60:63], a[68:71], v[220:223]\n"
  "v_perm_b32 v58, v148, v144, s69\n"
  "v_perm_b32 v59, v156, v152, s69\n"
  "buffer_load_dwordx4 v[112:115], v6, s[44:47], s72 offen\n"
  "v_mfma_f32_16x16x32_bf16 v[252:255], v[60:63], a[72:75], v[252:255]\n"
  "buffer_load_dwordx4 v[116:119], v7, s[44:47], s72 offen\n"
  "v_mfma_f32_16x16x32_bf16 a[28:31], v[60:63], a[76:79], a[28:31]\n"
  "s_waitcnt lgkmcnt(0)\n"
  "buffer_load_dwordx4 v[120:123], v8, s[44:47], s72 offen\n"
  "v_mfma_f32_16x16x32_bf16 a[60:63], v[60:63], a[80:83], a[60:63]\n"
  "buffer_load_dwordx4 v[124:127], v9, s[44:47], s72 offen\n"
  "v_mfma_f32_16x16x32_bf16 v[160:163], v[56:59], a[84:87], v[160:163]\n"
  "v_perm_b32 v60, v132, v128, s70\n"
  "v_perm_b32 v61, v140, v136, s70\n"
  "v_mfma_f32_16x16x32_bf16 v[192:195], v[56:59], a[88:91], v[192:195]\n"
  "v_perm_b32 v62, v148, v144, s70\n"
  "v_perm_b32 v63, v156, v152, s70\n"
  "v_mfma_f32_16x16x32_bf16 v[224:227], v[56:59], a[92:95], v[224:227]\n"
  "s_add_u32 m0, s96, 0xa000\n"
  "s_xor_b32 s73, s68, 0x200\n"
  "buffer_load_dwordx4 v10, s[32:35], s73 offen lds\n"
  "v_mfma_f32_16x16x32_bf16 a[0:3], v[56:59], a[96:99], a[0:3]\n"
  "v_xor_b32 v12, 0x5080, v11\n"
  "v_mfma_f32_16x16x32_bf16 a[32:35], v[56:59], a[100:103], a[32:35]\n"
  "s_add_u32 m0, s96, 0xb000\n"
  "s_xor_b32 s73, s68, 0x4200\n"
  "buffer_load_dwordx4 v10, s[32:35], s73 offen lds\n"
  "v_mfma_f32_16x16x32_bf16 v[164:167], v[60:63], a[84:87], v[164:167]\n"
  "v_perm_b32 v56, v133, v129, s69\n"
  "v_perm_b32 v57, v141, v137, s69\n"
  "s_add_u32 m0, s96, 0xc000\n"
  "s_xor_b32 s73, s68, 0x8200\n"
  "buffer_load_dwordx4 v10, s[32:35], s73 offen lds\n"
  "v_mfma_f32_16x16x32_bf16 v[196:199], v[60:63], a[88:91], v[196:199]\n"
  "v_perm_b32 v58, v149, v145, s69\n"
  "v_perm_b32 v59, v157, v153, s69\n"
  "v_mfma_f32_16x16x32_bf16 v[228:231], v[60:63], a[92:95], v[228:231]\n"
  "s_add_u32 m0, s96, 0xd000\n"
  "s_xor_b32 s73, s68, 0xc200\n"
  "buffer_load_dwordx4 v10, s[32:35], s73 offen lds\n"
  "v_mfma_f32_16x16x32_bf16 a[4:7], v[60:63], a[96:99], a[4:7]\n"
  "ds_read_b128 a[64:67], v12\n"
  "v_mfma_f32_16x16x32_bf16 a[36:39], v[60:63], a[100:103], a[36:39]\n"
  "s_add_u32 m0, s96, 0xe000\n"
  "s_xor_b32 s73, s68, 0x10200\n"
  "buffer_load_dwordx4 v10, s[32:35], s73 offen lds\n"
  "v_mfma_f32_16x16x32_bf16 v[168:171], v[56:59], a[84:87], v[168:171]\n"
  "v_perm_b32 v60, v133, v129, s70\n"
  "v_perm_b32 v61, v141, v137, s70\n"
  "s_add_u32 m0, s77, 0x1000\n"
  "s_nop 0\n"
  "buffer_load_dwordx4 v16, s[36:39], s92 offen offset:3072 sc1 nt lds\n"
  "v_mfma_f32_16x16x32_bf16 v[200:203], v[56:59], a[88:91], v[200:203]\n"
  "v_perm_b32 v62, v149, v145, s70\n"
  "v_perm_b32 v63, v157, v153, s70\n"
  "v_mfma_f32_16x16x32_bf16 v[232:235], v[56:59], a[92:95], v[232:235]\n"
  "s_add_u32 m0, s77, 0x2000\n"
  "s_nop 0\n"
  "buffer_load_dwordx4 v13, s[36:39], s93 offen sc1 nt lds\n"
  "v_mfma_f32_16x16x32_bf16 a[8:11], v[56:59], a[96:99], a[8:11]\n"
  "ds_read_b128 a[68:71], v12 offset:4096\n"
  "v_mfma_f32_16x16x32_bf16 a[40:43], v[56:59], a[100:103], a[40:43]\n"
  "buffer_load_dwordx4 v14, s[36:39], s93 offen offset:1024 sc1 nt lds\n"
  "v_mfma_f32_16x16x32_bf16 v[172:175], v[60:63], a[84:87], v[172:175]\n"
  "v_perm_b32 v56, v134, v130, s69\n"
  "v_perm_b32 v57, v142, v138, s69\n"
  "v_mfma_f32_16x16x32_bf16 v[204:207], v[60:63], a[88:91], v[204:207]\n"
  "v_perm_b32 v58, v150, v146, s69\n"
  "v_perm_b32 v59, v158, v154, s69\n"
  "v_mfma_f32_16x16x32_bf16 v[236:239], v[60:63], a[92:95], v[236:239]\n"
  "v_mfma_f32_16x16x32_bf16 a[12:15], v[60:63], a[96:99], a[12:15]\n"
  "ds_read_b128 a[72:75], v12 offset:8192\n"
  "v_mfma_f32_16x16x32_bf16 a[44:47], v[60:63], a[100:103], a[44:47]\n"
  "v_mfma_f32_16x16x32_bf16 v[176:179], v[56:59], a[84:87], v[176:179]\n"
  "v_perm_b32 v60, v134, v130, s70\n"
  "v_perm_b32 v61, v142, v138, s70\n"
  "v_mfma_f32_16x16x32_bf16 v[208:211], v[56:59], a[88:91], v[208:211]\n"
  "v_perm_b32 v62, v150, v146, s70\n"
  "v_perm_b32 v63, v158, v154, s70\n"
  "v_mfma_f32_16x16x32_bf16 v[240:243], v[56:59], a[92:95], v[240:243]\n"
  "v_mfma_f32_16x16x32_bf16 a[16:19], v[56:59], a[96:99], a[16:19]\n"
  "ds_read_b128 a[76:79], v12 offset:12288\n"
  "v_mfma_f32_16x16x32_bf16 a[48:51], v[56:59], a[100:103], a[48:51]\n"
  "v_mfma_f32_16x16x32_bf16 v[180:183], v[60:63], a[84:87], v[180:183]\n"
  "v_perm_b32 v56, v135, v131, s69\n"
  "v_perm_b32 v57, v143, v139, s69\n"
  "v_mfma_f32_16x16x32_bf16 v[212:215], v[60:63], a[88:91], v[212:215]\n"
  "v_perm_b32 v58, v151, v147, s69\n"
  "v_perm_b32 v59, v159, v155, s69\n"
  "v_mfma_f32_16x16x32_bf16 v[244:247], v[60:63], a[92:95], v[244:247]\n"
  "v_mfma_f32_16x16x32_bf16 a[20:23], v[60:63], a[96:99], a[20:23]\n"
  "ds_read_b128 a[80:83], v12 offset:16384\n"
  "v_mfma_f32_16x16x32_bf16 a[52:55], v[60:63], a[100:103], a[52:55]\n"
  "v_mfma_f32_16x16x32_bf16 v[184:187], v[56:59], a[84:87], v[184:187]\n"
  "v_perm_b32 v60, v135, v131, s70\n"
  "v_perm_b32 v61, v143, v139, s70\n"
  "v_mfma_f32_16x16x32_bf16 v[216:219], v[56:59], a[88:91], v[216:219]\n"
  "v_perm_b32 v62, v151, v147, s70\n"
  "v_perm_b32 v63, v159, v155, s70\n"
  "v_mfma_f32_16x16x32_bf16 v[248:251], v[56:59], a[92:95], v[248:251]\n"
  "v_mfma_f32_16x16x32_bf16 a[24:27], v[56:59], a[96:99], a[24:27]\n"
  "v_mfma_f32_16x16x32_bf16 a[56:59], v[56:59], a[100:103], a[56:59]\n"
  "s_waitcnt vmcnt(16)\n"
  "v_mfma_f32_16x16x32_bf16 v[188:191], v[60:63], a[84:87], v[188:191]\n"
  "v_perm_b32 v56, v68, v64, s69\n"
  "v_perm_b32 v57, v76, v72, s69\n"
  "v_mfma_f32_16x16x32_bf16 v[220:223], v[60:63], a[88:91], v[220:223]\n"
  "v_perm_b32 v58, v84, v80, s69\n"
  "v_perm_b32 v59, v92, v88, s69\n"
  "v_mfma_f32_16x16x32_bf16 v[252:255], v[60:63], a[92:95], v[252:255]\n"
  "v_mfma_f32_16x16x32_bf16 a[28:31], v[60:63], a[96:99], a[28:31]\n"
  "s_waitcnt lgkmcnt(0)\n"
  "v_mfma_f32_16x16x32_bf16 a[60:63], v[60:63], a[100:103], a[60:63]\n"
  "v_mfma_f32_16x16x32_bf16 v[160:163], v[56:59], a[64:67], v[160:163]\n"
  "v_perm_b32 v60, v68, v64, s70\n"
  "v_perm_b32 v61, v76, v72, s70\n"
  "v_mfma_f32_16x16x32_bf16 v[192:195], v[56:59], a[68:71], v[192:195]\n"
  "v_perm_b32 v62, v84, v80, s70\n"
  "v_perm_b32 v63, v92, v88, s70\n"
  "v_mfma_f32_16x16x32_bf16 v[224:227], v[56:59], a[72:75], v[224:227]\n"
  "v_mfma_f32_16x16x32_bf16 a[0:3], v[56:59], a[76:79], a[0:3]\n"
  "v_xor_b32 v12, 0x50c0, v11\n"
  "v_mfma_f32_16x16x32_bf16 a[32:35], v[56:59], a[80:83], a[32:35]\n"
  "v_mfma_f32_16x16x32_bf16 v[164:167], v[60:63], a[64:67], v[164:167]\n"
  "v_perm_b32 v56, v69, v65, s69\n"
  "v_perm_b32 v57, v77, v73, s69\n"
  "v_mfma_f32_16x16x32_bf16 v[196:199], v[60:63], a[68:71], v[196:199]\n"
  "v_perm_b32 v58, v85, v81, s69\n"
  "v_perm_b32 v59, v93, v89, s69\n"
  "v_mfma_f32_16x16x32_bf16 v[228:231], v[60:63], a[72:75], v[228:231]\n"
  "v_mfma_f32_16x16x32_bf16 a[4:7], v[60:63], a[76:79], a[4:7]\n"
  "ds_read_b128 a[84:87], v12\n"
  "v_mfma_f32_16x16x32_bf16 a[36:39], v[60:63], a[80:83], a[36:39]\n"
  "v_mfma_f32_16x16x32_bf16 v[168:171], v[56:59], a[64:67], v[168:171]\n"
  "v_perm_b32 v60, v69, v65, s70\n"
  "v_perm_b32 v61, v77, v73, s70\n"
  "v_mfma_f32_16x16x32_bf16 v[200:203], v[56:59], a[68:71], v[200:203]\n"
  "v_perm_b32 v62, v85, v81, s70\n"
  "v_perm_b32 v63, v93, v89, s70\n"
  "v_mfma_f32_16x16x32_bf16 v[232:235], v[56:59], a[72:75], v[232:235]\n"
  "v_mfma_f32_16x16x32_bf16 a[8:11], v[56:59], a[76:79], a[8:11]\n"
  "ds_read_b128 a[88:91], v12 offset:4096\n"
  "v_mfma_f32_16x16x32_bf16 a[40:43], v[56:59], a[80:83], a[40:43]\n"
  "v_mfma_f32_16x16x32_bf16 v[172:175], v[60:63], a[64:67], v[172:175]\n"
  "v_perm_b32 v56, v70, v66, s69\n"
  "v_perm_b32 v57, v78, v74, s69\n"
  "v_mfma_f32_16x16x32_bf16 v[204:207], v[60:63], a[68:71], v[204:207]\n"
  "v_perm_b32 v58, v86, v82, s69\n"
  "v_perm_b32 v59, v94, v90, s69\n"
  "v_mfma_f32_16x16x32_bf16 v[236:239], v[60:63], a[72:75], v[236:239]\n"
  "v_mfma_f32_16x16x32_bf16 a[12:15], v[60:63], a[76:79], a[12:15]\n"
  "ds_read_b128 a[92:95], v12 offset:8192\n"
  "v_mfma_f32_16x16x32_bf16 a[44:47], v[60:63], a[80:83], a[44:47]\n"
  "v_mfma_f32_16x16x32_bf16 v[176:179], v[56:59], a[64:67], v[176:179]\n"
  "v_perm_b32 v60, v70, v66, s70\n"
  "v_perm_b32 v61, v78, v74, s70\n"
  "v_mfma_f32_16x16x32_bf16 v[208:211], v[56:59], a[68:71], v[208:211]\n"
  "v_perm_b32 v62, v86, v82, s70\n"
  "v_perm_b32 v63, v94, v90, s70\n"
  "v_mfma_f32_16x16x32_bf16 v[240:243], v[56:59], a[72:75], v[240:243]\n"
  "s_xor_b32 s72, s67, 0x200000\n"
  "buffer_load_dwordx4 v[128:131], v2, s[44:47], s72 offen\n"
  "v_mfma_f32_16x16x32_bf16 a[16:19], v[56:59], a[76:79], a[16:19]\n"
  "ds_read_b128 a[96:99], v12 offset:12288\n"
  "v_mfma_f32_16x16x32_bf16 a[48:51], v[56:59], a[80:83], a[48:51]\n"
  "buffer_load_dwordx4 v[132:135], v3, s[44:47], s72 offen\n"
  "v_mfma_f32_16x16x32_bf16 v[180:183], v[60:63], a[64:67], v[180:183]\n"
  "v_perm_b32 v56, v71, v67, s69\n"
  "v_perm_b32 v57, v79, v75, s69\n"
  "v_mfma_f32_16x16x32_bf16 v[212:215], v[60:63], a[68:71], v[212:215]\n"
  "v_perm_b32 v58, v87, v83, s69\n"
  "v_perm_b32 v59, v95, v91, s69\n"
  "buffer_load_dwordx4 v[136:139], v4, s[44:47], s72 offen\n"
  "v_mfma_f32_16x16x32_bf16 v[244:247], v[60:63], a[72:75], v[244:247]\n"
  "v_mfma_f32_16x16x32_bf16 a[20:23], v[60:63], a[76:79], a[20:23]\n"
  "buffer_load_dwordx4 v[140:143], v5, s[44:47], s72 offen\n"
  "ds_read_b128 a[100:103], v12 offset:16384\n"
  "v_mfma_f32_16x16x32_bf16 a[52:55], v[60:63], a[80:83], a[52:55]\n"
  "v_mfma_f32_16x16x32_bf16 v[184:187], v[56:59], a[64:67], v[184:187]\n"
  "v_perm_b32 v60, v71, v67, s70\n"
  "v_perm_b32 v61, v79, v75, s70\n"
  "buffer_load_dwordx4 v[144:147], v6, s[44:47], s72 offen\n"
  "v_mfma_f32_16x16x32_bf16 v[216:219], v[56:59], a[68:71], v[216:219]\n"
  "v_perm_b32 v62, v87, v83, s70\n"
  "v_perm_b32 v63, v95, v91, s70\n"
  "v_mfma_f32_16x16x32_bf16 v[248:251], v[56:59], a[72:75], v[248:251]\n"
  "buffer_load_dwordx4 v[148:151], v7, s[44:47], s72 offen\n"
  "v_mfma_f32_16x16x32_bf16 a[24:27], v[56:59], a[76:79], a[24:27]\n"
  "v_mfma_f32_16x16x32_bf16 a[56:59], v[56:59], a[80:83], a[56:59]\n"
  "s_waitcnt vmcnt(14)\n"
  "buffer_load_dwordx4 v[152:155], v8, s[44:47], s72 offen\n"
  "v_mfma_f32_16x16x32_bf16 v[188:191], v[60:63], a[64:67], v[188:191]\n"
  "v_perm_b32 v56, v100, v96, s69\n"
  "v_perm_b32 v57, v108, v104, s69\n"
  "v_mfma_f32_16x16x32_bf16 v[220:223], v[60:63], a[68:71], v[220:223]\n"
  "v_perm_b32 v58, v116, v112, s69\n"
  "v_perm_b32 v59, v124, v120, s69\n"
  "buffer_load_dwordx4 v[156:159], v9, s[44:47], s72 offen\n"
  "v_mfma_f32_16x16x32_bf16 v[252:255], v[60:63], a[72:75], v[252:255]\n"
  "v_mfma_f32_16x16x32_bf16 a[28:31], v[60:63], a[76:79], a[28:31]\n"
  "s_waitcnt lgkmcnt(0)\n"
  "v_mfma_f32_16x16x32_bf16 a[60:63], v[60:63], a[80:83], a[60:63]\n"
  "v_mfma_f32_16x16x32_bf16 v[160:163], v[56:59], a[84:87], v[160:163]\n"
  "v_perm_b32 v60, v100, v96, s70\n"
  "v_perm_b32 v61, v108, v104, s70\n"
  "s_xor_b32 s72, s67, 0x240000\n"
  "buffer_load_dwordx4 v[64:67], v2, s[44:47], s72 offen\n"
  "v_mfma_f32_16x16x32_bf16 v[192:195], v[56:59], a[88:91], v[192:195]\n"
  "v_perm_b32 v62, v116, v112, s70\n"
  "v_perm_b32 v63, v124, v120, s70\n"
  "v_mfma_f32_16x16x32_bf16 v[224:227], v[56:59], a[92:95], v[224:227]\n"
  "buffer_load_dwordx4 v[68:71], v3, s[44:47], s72 offen\n"
  "v_mfma_f32_16x16x32_bf16 a[0:3], v[56:59], a[96:99], a[0:3]\n"
  "v_xor_b32 v12, 0xa000, v11\n"
  "v_mfma_f32_16x16x32_bf16 a[32:35], v[56:59], a[100:103], a[32:35]\n"
  "buffer_load_dwordx4 v[72:75], v4, s[44:47], s72 offen\n"
  "s_waitcnt vmcnt(14)\n"
  "s_barrier\n"
  "v_mfma_f32_16x16x32_bf16 v[164:167], v[60:63], a[84:87], v[164:167]\n"
  "v_perm_b32 v56, v101, v97, s69\n"
  "v_perm_b32 v57, v109, v105, s69\n"
  "v_mfma_f32_16x16x32_bf16 v[196:199], v[60:63], a[88:91], v[196:199]\n"
  "v_perm_b32 v58, v117, v113, s69\n"
  "v_perm_b32 v59, v125, v121, s69\n"
  "buffer_load_dwordx4 v[76:79], v5, s[44:47], s72 offen\n"
  "v_mfma_f32_16x16x32_bf16 v[228:231], v[60:63], a[92:95], v[228:231]\n"
  "v_mfma_f32_16x16x32_bf16 a[4:7], v[60:63], a[96:99], a[4:7]\n"
  "buffer_load_dwordx4 v[80:83], v6, s[44:47], s72 offen\n"
  "ds_read_b128 a[64:67], v12\n"
  "v_mfma_f32_16x16x32_bf16 a[36:39], v[60:63], a[100:103], a[36:39]\n"
  "v_mfma_f32_16x16x32_bf16 v[168:171], v[56:59], a[84:87], v[168:171]\n"
  "v_perm_b32 v60, v101, v97, s70\n"
  "v_perm_b32 v61, v109, v105, s70\n"
  "buffer_load_dwordx4 v[84:87], v7, s[44:47], s72 offen\n"
  "v_mfma_f32_16x16x32_bf16 v[200:203], v[56:59], a[88:91], v[200:203]\n"
  "v_perm_b32 v62, v117, v113, s70\n"
  "v_perm_b32 v63, v125, v121, s70\n"
  "v_mfma_f32_16x16x32_bf16 v[232:235], v[56:59], a[92:95], v[232:235]\n"
  "buffer_load_dwordx4 v[88:91], v8, s[44:47], s72 offen\n"
  "v_mfma_f32_16x16x32_bf16 a[8:11], v[56:59], a[96:99], a[8:11]\n"
  "ds_read_b128 a[68:71], v12 offset:4096\n"
  "v_mfma_f32_16x16x32_bf16 a[40:43], v[56:59], a[100:103], a[40:43]\n"
  "buffer_load_dwordx4 v[92:95], v9, s[44:47], s72 offen\n"
  "v_mfma_f32_16x16x32_bf16 v[172:175], v[60:63], a[84:87], v[172:175]\n"
  "v_perm_b32 v56, v102, v98, s69\n"
  "v_perm_b32 v57, v110, v106, s69\n"
  "v_mfma_f32_16x16x32_bf16 v[204:207], v[60:63], a[88:91], v[204:207]\n"
  "v_perm_b32 v58, v118, v114, s69\n"
  "v_perm_b32 v59, v126, v122, s69\n"
  "v_mfma_f32_16x16x32_bf16 v[236:239], v[60:63], a[92:95], v[236:239]\n"
  "v_mfma_f32_16x16x32_bf16 a[12:15], v[60:63], a[96:99], a[12:15]\n"
  "ds_read_b128 a[72:75], v12 offset:8192\n"
  "v_mfma_f32_16x16x32_bf16 a[44:47], v[60:63], a[100:103], a[44:47]\n"
  "v_mfma_f32_16x16x32_bf16 v[176:179], v[56:59], a[84:87], v[176:179]\n"
  "v_perm_b32 v60, v102, v98, s70\n"
  "v_perm_b32 v61, v110, v106, s70\n"
  "v_mfma_f32_16x16x32_bf16 v[208:211], v[56:59], a[88:91], v[208:211]\n"
  "v_perm_b32 v62, v118, v114, s70\n"
  "v_perm_b32 v63, v126, v122, s70\n"
  "v_mfma_f32_16x16x32_bf16 v[240:243], v[56:59], a[92:95], v[240:243]\n"
  "v_mfma_f32_16x16x32_bf16 a[16:19], v[56:59], a[96:99], a[16:19]\n"
  "ds_read_b128 a[76:79], v12 offset:12288\n"
  "v_mfma_f32_16x16x32_bf16 a[48:51], v[56:59], a[100:103], a[48:51]\n"
  "v_mfma_f32_16x16x32_bf16 v[180:183], v[60:63], a[84:87], v[180:183]\n"
  "v_perm_b32 v56, v103, v99, s69\n"
  "v_perm_b32 v57, v111, v107, s69\n"
  "v_mfma_f32_16x16x32_bf16 v[212:215], v[60:63], a[88:91], v[212:215]\n"
  "v_perm_b32 v58, v119, v115, s69\n"
  "v_perm_b32 v59, v127, v123, s69\n"
  "v_mfma_f32_16x16x32_bf16 v[244:247], v[60:63], a[92:95], v[244:247]\n"
  "v_mfma_f32_16x16x32_bf16 a[20:23], v[60:63], a[96:99], a[20:23]\n"
  "ds_read_b128 a[80:83], v12 offset:16384\n"
  "v_mfma_f32_16x16x32_bf16 a[52:55], v[60:63], a[100:103], a[52:55]\n"
  "v_mfma_f32_16x16x32_bf16 v[184:187], v[56:59], a[84:87], v[184:187]\n"
  "v_perm_b32 v60, v103, v99, s70\n"
  "v_perm_b32 v61, v111, v107, s70\n"
  "v_mfma_f32_16x16x32_bf16 v[216:219], v[56:59], a[88:91], v[216:219]\n"
  "v_perm_b32 v62, v119, v115, s70\n"
  "v_perm_b32 v63, v127, v123, s70\n"
  "v_mfma_f32_16x16x32_bf16 v[248:251], v[56:59], a[92:95], v[248:251]\n"
  "s_xor_b32 s72, s67, 0x280000\n"
  "buffer_load_dwordx4 v[96:99], v2, s[44:47], s72 offen\n"
  "v_mfma_f32_16x16x32_bf16 a[24:27], v[56:59], a[96:99], a[24:27]\n"
  "buffer_load_dwordx4 v[100:103], v3, s[44:47], s72 offen\n"
  "v_mfma_f32_16x16x32_bf16 a[56:59], v[56:59], a[100:103], a[56:59]\n"
  "s_waitcnt vmcnt(10)\n"
  "buffer_load_dwordx4 v[104:107], v4, s[44:47], s72 offen\n"
  "v_mfma_f32_16x16x32_bf16 v[188:191], v[60:63], a[84:87], v[188:191]\n"
  "v_perm_b32 v56, v132, v128, s69\n"
  "v_perm_b32 v57, v140, v136, s69\n"
  "buffer_load_dwordx4 v[108:111], v5, s[44:47], s72 offen\n"
  "v_mfma_f32_16x16x32_bf16 v[220:223], v[60:63], a[88:91], v[220:223]\n"
  "v_perm_b32 v58, v148, v144, s69\n"
  "v_perm_b32 v59, v156, v152, s69\n"
  "buffer_load_dwordx4 v[112:115], v6, s[44:47], s72 offen\n"
  "v_mfma_f32_16x16x32_bf16 v[252:255], v[60:63], a[92:95], v[252:255]\n"
  "buffer_load_dwordx4 v[116:119], v7, s[44:47], s72 offen\n"
  "v_mfma_f32_16x16x32_bf16 a[28:31], v[60:63], a[96:99], a[28:31]\n"
  "s_waitcnt lgkmcnt(0)\n"
  "buffer_load_dwordx4 v[120:123], v8, s[44:47], s72 offen\n"
  "v_mfma_f32_16x16x32_bf16 a[60:63], v[60:63], a[100:103], a[60:63]\n"
  "buffer_load_dwordx4 v[124:127], v9, s[44:47], s72 offen\n"
  "v_mfma_f32_16x16x32_bf16 v[160:163], v[56:59], a[64:67], v[160:163]\n"
  "v_perm_b32 v60, v132, v128, s70\n"
  "v_perm_b32 v61, v140, v136, s70\n"
  "v_mfma_f32_16x16x32_bf16 v[192:195], v[56:59], a[68:71], v[192:195]\n"
  "v_perm_b32 v62, v148, v144, s70\n"
  "v_perm_b32 v63, v156, v152, s70\n"
  "v_mfma_f32_16x16x32_bf16 v[224:227], v[56:59], a[72:75], v[224:227]\n"
  "s_add_u32 m0, s96, 0xf000\n"
  "s_xor_b32 s73, s68, 0x300\n"
  "buffer_load_dwordx4 v10, s[32:35], s73 offen lds\n"
  "v_mfma_f32_16x16x32_bf16 a[0:3], v[56:59], a[76:79], a[0:3]\n"
  "v_xor_b32 v12, 0xa040, v11\n"
  "v_mfma_f32_16x16x32_bf16 a[32:35], v[56:59], a[80:83], a[32:35]\n"
  "s_add_u32 m0, s96, 0x10000\n"
  "s_xor_b32 s73, s68, 0x4300\n"
  "buffer_load_dwordx4 v10, s[32:35], s73 offen lds\n"
  "v_mfma_f32_16x16x32_bf16 v[164:167], v[60:63], a[64:67], v[164:167]\n"
  "v_perm_b32 v56, v133, v129, s69\n"
  "v_perm_b32 v57, v141, v137, s69\n"
  "s_add_u32 m0, s96, 0x11000\n"
  "s_xor_b32 s73, s68, 0x8300\n"
  "buffer_load_dwordx4 v10, s[32:35], s73 offen lds\n"
  "v_mfma_f32_16x16x32_bf16 v[196:199], v[60:63], a[68:71], v[196:199]\n"
  "v_perm_b32 v58, v149, v145, s69\n"
  "v_perm_b32 v59, v157, v153, s69\n"
  "v_mfma_f32_16x16x32_bf16 v[228:231], v[60:63], a[72:75], v[228:231]\n"
  "s_add_u32 m0, s96, 0x12000\n"
  "s_xor_b32 s73, s68, 0xc300\n"
  "buffer_load_dwordx4 v10, s[32:35], s73 offen lds\n"
  "v_mfma_f32_16x16x32_bf16 a[4:7], v[60:63], a[76:79], a[4:7]\n"
  "ds_read_b128 a[84:87], v12\n"
  "v_mfma_f32_16x16x32_bf16 a[36:39], v[60:63], a[80:83], a[36:39]\n"
  "s_add_u32 m0, s96, 0x13000\n"
  "s_xor_b32 s73, s68, 0x10300\n"
  "buffer_load_dwordx4 v10, s[32:35], s73 offen lds\n"
  "v_mfma_f32_16x16x32_bf16 v[168:171], v[56:59], a[64:67], v[168:171]\n"
  "v_perm_b32 v60, v133, v129, s70\n"
  "v_perm_b32 v61, v141, v137, s70\n"
  "s_add_u32 m0, s77, 0x2000\n"
  "s_nop 0\n"
  "buffer_load_dwordx4 v15, s[36:39], s93 offen offset:2048 sc1 nt lds\n"
  "v_mfma_f32_16x16x32_bf16 v[200:203], v[56:59], a[68:71], v[200:203]\n"
  "v_perm_b32 v62, v149, v145, s70\n"
  "v_perm_b32 v63, v157, v153, s70\n"
  "v_mfma_f32_16x16x32_bf16 v[232:235], v[56:59], a[72:75], v[232:235]\n"
  "buffer_load_dwordx4 v16, s[36:39], s93 offen offset:3072 sc1 nt lds\n"
  "v_mfma_f32_16x16x32_bf16 a[8:11], v[56:59], a[76:79], a[8:11]\n"
  "ds_read_b128 a[88:91], v12 offset:4096\n"
  "v_mfma_f32_16x16x32_bf16 a[40:43], v[56:59], a[80:83], a[40:43]\n"
  "s_add_u32 m0, s77, 0x3000\n"
  "s_nop 0\n"
  "buffer_load_dwordx4 v13, s[36:39], s94 offen sc1 nt lds\n"
  "v_mfma_f32_16x16x32_bf16 v[172:175], v[60:63], a[64:67], v[172:175]\n"
  "v_perm_b32 v56, v134, v130, s69\n"
  "v_perm_b32 v57, v142, v138, s69\n"
  "v_mfma_f32_16x16x32_bf16 v[204:207], v[60:63], a[68:71], v[204:207]\n"
  "v_perm_b32 v58, v150, v146, s69\n"
  "v_perm_b32 v59, v158, v154, s69\n"
  "v_mfma_f32_16x16x32_bf16 v[236:239], v[60:63], a[72:75], v[236:239]\n"
  "v_mfma_f32_16x16x32_bf16 a[12:15], v[60:63], a[76:79], a[12:15]\n"
  "ds_read_b128 a[92:95], v12 offset:8192\n"
  "v_mfma_f32_16x16x32_bf16 a[44:47], v[60:63], a[80:83], a[44:47]\n"
  "v_mfma_f32_16x16x32_bf16 v[176:179], v[56:59], a[64:67], v[176:179]\n"
  "v_perm_b32 v60, v134, v130, s70\n"
  "v_perm_b32 v61, v142, v138, s70\n"
  "v_mfma_f32_16x16x32_bf16 v[208:211], v[56:59], a[68:71], v[208:211]\n"
  "v_perm_b32 v62, v150, v146, s70\n"
  "v_perm_b32 v63, v158, v154, s70\n"
  "v_mfma_f32_16x16x32_bf16 v[240:243], v[56:59], a[72:75], v[240:243]\n"
  "v_mfma_f32_16x16x32_bf16 a[16:19], v[56:59], a[76:79], a[16:19]\n"
  "ds_read_b128 a[96:99], v12 offset:12288\n"
  "v_mfma_f32_16x16x32_bf16 a[48:51], v[56:59], a[80:83], a[48:51]\n"
  "v_mfma_f32_16x16x32_bf16 v[180:183], v[60:63], a[64:67], v[180:183]\n"
  "v_perm_b32 v56, v135, v131, s69\n"
  "v_perm_b32 v57, v143, v139, s69\n"
  "v_mfma_f32_16x16x32_bf16 v[212:215], v[60:63], a[68:71], v[212:215]\n"
  "v_perm_b32 v58, v151, v147, s69\n"
  "v_perm_b32 v59, v159, v155, s69\n"
  "v_mfma_f32_16x16x32_bf16 v[244:247], v[60:63], a[72:75], v[244:247]\n"
  "v_mfma_f32_16x16x32_bf16 a[20:23], v[60:63], a[76:79], a[20:23]\n"
  "ds_read_b128 a[100:103], v12 offset:16384\n"
  "v_mfma_f32_16x16x32_bf16 a[52:55], v[60:63], a[80:83], a[52:55]\n"
  "v_mfma_f32_16x16x32_bf16 v[184:187], v[56:59], a[64:67], v[184:187]\n"
  "v_perm_b32 v60, v135, v131, s70\n"
  "v_perm_b32 v61, v143, v139, s70\n"
  "v_mfma_f32_16x16x32_bf16 v[216:219], v[56:59], a[68:71], v[216:219]\n"
  "v_perm_b32 v62, v151, v147, s70\n"
  "v_perm_b32 v63, v159, v155, s70\n"
  "v_mfma_f32_16x16x32_bf16 v[248:251], v[56:59], a[72:75], v[248:251]\n"
  "v_mfma_f32_16x16x32_bf16 a[24:27], v[56:59], a[76:79], a[24:27]\n"
  "v_mfma_f32_16x16x32_bf16 a[56:59], v[56:59], a[80:83], a[56:59]\n"
  "s_waitcnt vmcnt(16)\n"
  "v_mfma_f32_16x16x32_bf16 v[188:191], v[60:63], a[64:67], v[188:191]\n"
  "v_perm_b32 v56, v68, v64, s69\n"
  "v_perm_b32 v57, v76, v72, s69\n"
  "v_mfma_f32_16x16x32_bf16 v[220:223], v[60:63], a[68:71], v[220:223]\n"
  "v_perm_b32 v58, v84, v80, s69\n"
  "v_perm_b32 v59, v92, v88, s69\n"
  "v_mfma_f32_16x16x32_bf16 v[252:255], v[60:63], a[72:75], v[252:255]\n"
  "v_mfma_f32_16x16x32_bf16 a[28:31], v[60:63], a[76:79], a[28:31]\n"
  "s_waitcnt lgkmcnt(0)\n"
  "v_mfma_f32_16x16x32_bf16 a[60:63], v[60:63], a[80:83], a[60:63]\n"
  "v_mfma_f32_16x16x32_bf16 v[160:163], v[56:59], a[84:87], v[160:163]\n"
  "v_perm_b32 v60, v68, v64, s70\n"
  "v_perm_b32 v61, v76, v72, s70\n"
  "v_mfma_f32_16x16x32_bf16 v[192:195], v[56:59], a[88:91], v[192:195]\n"
  "v_perm_b32 v62, v84, v80, s70\n"
  "v_perm_b32 v63, v92, v88, s70\n"
  "v_mfma_f32_16x16x32_bf16 v[224:227], v[56:59], a[92:95], v[224:227]\n"
  "v_mfma_f32_16x16x32_bf16 a[0:3], v[56:59], a[96:99], a[0:3]\n"
  "v_xor_b32 v12, 0xa080, v11\n"
  "v_mfma_f32_16x16x32_bf16 a[32:35], v[56:59], a[100:103], a[32:35]\n"
  "v_mfma_f32_16x16x32_bf16 v[164:167], v[60:63], a[84:87], v[164:167]\n"
  "v_perm_b32 v56, v69, v65, s69\n"
  "v_perm_b32 v57, v77, v73, s69\n"
  "v_mfma_f32_16x16x32_bf16 v[196:199], v[60:63], a[88:91], v[196:199]\n"
  "v_perm_b32 v58, v85, v81, s69\n"
  "v_perm_b32 v59, v93, v89, s69\n"
  "v_mfma_f32_16x16x32_bf16 v[228:231], v[60:63], a[92:95], v[228:231]\n"
  "v_mfma_f32_16x16x32_bf16 a[4:7], v[60:63], a[96:99], a[4:7]\n"
  "ds_read_b128 a[64:67], v12\n"
  "v_mfma_f32_16x16x32_bf16 a[36:39], v[60:63], a[100:103], a[36:39]\n"
  "v_mfma_f32_16x16x32_bf16 v[168:171], v[56:59], a[84:87], v[168:171]\n"
  "v_perm_b32 v60, v69, v65, s70\n"
  "v_perm_b32 v61, v77, v73, s70\n"
  "v_mfma_f32_16x16x32_bf16 v[200:203], v[56:59], a[88:91], v[200:203]\n"
  "v_perm_b32 v62, v85, v81, s70\n"
  "v_perm_b32 v63, v93, v89, s70\n"
  "v_mfma_f32_16x16x32_bf16 v[232:235], v[56:59], a[92:95], v[232:235]\n"
  "v_mfma_f32_16x16x32_bf16 a[8:11], v[56:59], a[96:99], a[8:11]\n"
  "ds_read_b128 a[68:71], v12 offset:4096\n"
  "v_mfma_f32_16x16x32_bf16 a[40:43], v[56:59], a[100:103], a[40:43]\n"
  "v_mfma_f32_16x16x32_bf16 v[172:175], v[60:63], a[84:87], v[172:175]\n"
  "v_perm_b32 v56, v70, v66, s69\n"
  "v_perm_b32 v57, v78, v74, s69\n"
  "v_mfma_f32_16x16x32_bf16 v[204:207], v[60:63], a[88:91], v[204:207]\n"
  "v_perm_b32 v58, v86, v82, s69\n"
  "v_perm_b32 v59, v94, v90, s69\n"
  "v_mfma_f32_16x16x32_bf16 v[236:239], v[60:63], a[92:95], v[236:239]\n"
  "v_mfma_f32_16x16x32_bf16 a[12:15], v[60:63], a[96:99], a[12:15]\n"
  "ds_read_b128 a[72:75], v12 offset:8192\n"
  "v_mfma_f32_16x16x32_bf16 a[44:47], v[60:63], a[100:103], a[44:47]\n"
  "v_mfma_f32_16x16x32_bf16 v[176:179], v[56:59], a[84:87], v[176:179]\n"
  "v_perm_b32 v60, v70, v66, s70\n"
  "v_perm_b32 v61, v78, v74, s70\n"
  "v_mfma_f32_16x16x32_bf16 v[208:211], v[56:59], a[88:91], v[208:211]\n"
  "v_perm_b32 v62, v86, v82, s70\n"
  "v_perm_b32 v63, v94, v90, s70\n"
  "v_mfma_f32_16x16x32_bf16 v[240:243], v[56:59], a[92:95], v[240:243]\n"
  "s_xor_b32 s72, s67, 0x2c0000\n"
  "buffer_load_dwordx4 v[128:131], v2, s[44:47], s72 offen\n"
  "v_mfma_f32_16x16x32_bf16 a[16:19], v[56:59], a[96:99], a[16:19]\n"
  "ds_read_b128 a[76:79], v12 offset:12288\n"
  "v_mfma_f32_16x16x32_bf16 a[48:51], v[56:59], a[100:103], a[48:51]\n"
  "buffer_load_dwordx4 v[132:135], v3, s[44:47], s72 offen\n"
  "v_mfma_f32_16x16x32_bf16 v[180:183], v[60:63], a[84:87], v[180:183]\n"
  "v_perm_b32 v56, v71, v67, s69\n"
  "v_perm_b32 v57, v79, v75, s69\n"
  "v_mfma_f32_16x16x32_bf16 v[212:215], v[60:63], a[88:91], v[212:215]\n"
  "v_perm_b32 v58, v87, v83, s69\n"
  "v_perm_b32 v59, v95, v91, s69\n"
  "buffer_load_dwordx4 v[136:139], v4, s[44:47], s72 offen\n"
  "v_mfma_f32_16x16x32_bf16 v[244:247], v[60:63], a[92:95], v[244:247]\n"
  "v_mfma_f32_16x16x32_bf16 a[20:23], v[60:63], a[96:99], a[20:23]\n"
  "buffer_load_dwordx4 v[140:143], v5, s[44:47], s72 offen\n"
  "ds_read_b128 a[80:83], v12 offset:16384\n"
  "v_mfma_f32_16x16x32_bf16 a[52:55], v[60:63], a[100:103], a[52:55]\n"
  "v_mfma_f32_16x16x32_bf16 v[184:187], v[56:59], a[84:87], v[184:187]\n"
  "v_perm_b32 v60, v71, v67, s70\n"
  "v_perm_b32 v61, v79, v75, s70\n"
  "buffer_load_dwordx4 v[144:147], v6, s[44:47], s72 offen\n"
  "v_mfma_f32_16x16x32_bf16 v[216:219], v[56:59], a[88:91], v[216:219]\n"
  "v_perm_b32 v62, v87, v83, s70\n"
  "v_perm_b32 v63, v95, v91, s70\n"
  "v_mfma_f32_16x16x32_bf16 v[248:251], v[56:59], a[92:95], v[248:251]\n"
  "buffer_load_dwordx4 v[148:151], v7, s[44:47], s72 offen\n"
  "v_mfma_f32_16x16x32_bf16 a[24:27], v[56:59], a[96:99], a[24:27]\n"
  "v_mfma_f32_16x16x32_bf16 a[56:59], v[56:59], a[100:103], a[56:59]\n"
  "s_waitcnt vmcnt(14)\n"
  "buffer_load_dwordx4 v[152:155], v8, s[44:47], s72 offen\n"
  "v_mfma_f32_16x16x32_bf16 v[188:191], v[60:63], a[84:87], v[188:191]\n"
  "v_perm_b32 v56, v100, v96, s69\n"
  "v_perm_b32 v57, v108, v104, s69\n"
  "v_mfma_f32_16x16x32_bf16 v[220:223], v[60:63], a[88:91], v[220:223]\n"
  "v_perm_b32 v58, v116, v112, s69\n"
  "v_perm_b32 v59, v124, v120, s69\n"
  "buffer_load_dwordx4 v[156:159], v9, s[44:47], s72 offen\n"
  "v_mfma_f32_16x16x32_bf16 v[252:255], v[60:63], a[92:95], v[252:255]\n"
  "v_mfma_f32_16x16x32_bf16 a[28:31], v[60:63], a[96:99], a[28:31]\n"
  "s_waitcnt lgkmcnt(0)\n"
  "v_mfma_f32_16x16x32_bf16 a[60:63], v[60:63], a[100:103], a[60:63]\n"
  "v_mfma_f32_16x16x32_bf16 v[160:163], v[56:59], a[64:67], v[160:163]\n"
  "v_perm_b32 v60, v100, v96, s70\n"
  "v_perm_b32 v61, v108, v104, s70\n"
  "s_xor_b32 s72, s67, 0x300000\n"
  "buffer_load_dwordx4 v[64:67], v2, s[44:47], s72 offen\n"
  "v_mfma_f32_16x16x32_bf16 v[192:195], v[56:59], a[68:71], v[192:195]\n"
  "v_perm_b32 v62, v116, v112, s70\n"
  "v_perm_b32 v63, v124, v120, s70\n"
  "v_mfma_f32_16x16x32_bf16 v[224:227], v[56:59], a[72:75], v[224:227]\n"
  "buffer_load_dwordx4 v[68:71], v3, s[44:47], s72 offen\n"
  "v_mfma_f32_16x16x32_bf16 a[0:3], v[56:59], a[76:79], a[0:3]\n"
  "v_xor_b32 v12, 0xa0c0, v11\n"
  "v_mfma_f32_16x16x32_bf16 a[32:35], v[56:59], a[80:83], a[32:35]\n"
  "buffer_load_dwordx4 v[72:75], v4, s[44:47], s72 offen\n"
  "v_mfma_f32_16x16x32_bf16 v[164:167], v[60:63], a[64:67], v[164:167]\n"
  "v_perm_b32 v56, v101, v97, s69\n"
  "v_perm_b32 v57, v109, v105, s69\n"
  "v_mfma_f32_16x16x32_bf16 v[196:199], v[60:63], a[68:71], v[196:199]\n"
  "v_perm_b32 v58, v117, v113, s69\n"
  "v_perm_b32 v59, v125, v121, s69\n"
  "buffer_load_dwordx4 v[76:79], v5, s[44:47], s72 offen\n"
  "v_mfma_f32_16x16x32_bf16 v[228:231], v[60:63], a[72:75], v[228:231]\n"
  "v_mfma_f32_16x16x32_bf16 a[4:7], v[60:63], a[76:79], a[4:7]\n"
  "buffer_load_dwordx4 v[80:83], v6, s[44:47], s72 offen\n"
  "ds_read_b128 a[84:87], v12\n"
  "v_mfma_f32_16x16x32_bf16 a[36:39], v[60:63], a[80:83], a[36:39]\n"
  "v_mfma_f32_16x16x32_bf16 v[168:171], v[56:59], a[64:67], v[168:171]\n"
  "v_perm_b32 v60, v101, v97, s70\n"
  "v_perm_b32 v61, v109, v105, s70\n"
  "buffer_load_dwordx4 v[84:87], v7, s[44:47], s72 offen\n"
  "v_mfma_f32_16x16x32_bf16 v[200:203], v[56:59], a[68:71], v[200:203]\n"
  "v_perm_b32 v62, v117, v113, s70\n"
  "v_perm_b32 v63, v125, v121, s70\n"
  "v_mfma_f32_16x16x32_bf16 v[232:235], v[56:59], a[72:75], v[232:235]\n"
  "buffer_load_dwordx4 v[88:91], v8, s[44:47], s72 offen\n"
  "v_mfma_f32_16x16x32_bf16 a[8:11], v[56:59], a[76:79], a[8:11]\n"
  "ds_read_b128 a[88:91], v12 offset:4096\n"
  "v_mfma_f32_16x16x32_bf16 a[40:43], v[56:59], a[80:83], a[40:43]\n"
  "buffer_load_dwordx4 v[92:95], v9, s[44:47], s72 offen\n"
  "v_mfma_f32_16x16x32_bf16 v[172:175], v[60:63], a[64:67], v[172:175]\n"
  "v_perm_b32 v56, v102, v98, s69\n"
  "v_perm_b32 v57, v110, v106, s69\n"
  "v_mfma_f32_16x16x32_bf16 v[204:207], v[60:63], a[68:71], v[204:207]\n"
  "v_perm_b32 v58, v118, v114, s69\n"
  "v_perm_b32 v59, v126, v122, s69\n"
  "v_mfma_f32_16x16x32_bf16 v[236:239], v[60:63], a[72:75], v[236:239]\n"
  "v_mfma_f32_16x16x32_bf16 a[12:15], v[60:63], a[76:79], a[12:15]\n"
  "ds_read_b128 a[92:95], v12 offset:8192\n"
  "v_mfma_f32_16x16x32_bf16 a[44:47], v[60:63], a[80:83], a[44:47]\n"
  "v_mfma_f32_16x16x32_bf16 v[176:179], v[56:59], a[64:67], v[176:179]\n"
  "v_perm_b32 v60, v102, v98, s70\n"
  "v_perm_b32 v61, v110, v106, s70\n"
  "v_mfma_f32_16x16x32_bf16 v[208:211], v[56:59], a[68:71], v[208:211]\n"
  "v_perm_b32 v62, v118, v114, s70\n"
  "v_perm_b32 v63, v126, v122, s70\n"
  "v_mfma_f32_16x16x32_bf16 v[240:243], v[56:59], a[72:75], v[240:243]\n"
  "v_mfma_f32_16x16x32_bf16 a[16:19], v[56:59], a[76:79], a[16:19]\n"
  "ds_read_b128 a[96:99], v12 offset:12288\n"
  "v_mfma_f32_16x16x32_bf16 a[48:51], v[56:59], a[80:83], a[48:51]\n"
  "v_mfma_f32_16x16x32_bf16 v[180:183], v[60:63], a[64:67], v[180:183]\n"
  "v_perm_b32 v56, v103, v99, s69\n"
  "v_perm_b32 v57, v111, v107, s69\n"
  "v_mfma_f32_16x16x32_bf16 v[212:215], v[60:63], a[68:71], v[212:215]\n"
  "v_perm_b32 v58, v119, v115, s69\n"
  "v_perm_b32 v59, v127, v123, s69\n"
  "v_mfma_f32_16x16x32_bf16 v[244:247], v[60:63], a[72:75], v[244:247]\n"
  "v_mfma_f32_16x16x32_bf16 a[20:23], v[60:63], a[76:79], a[20:23]\n"
  "ds_read_b128 a[100:103], v12 offset:16384\n"
  "v_mfma_f32_16x16x32_bf16 a[52:55], v[60:63], a[80:83], a[52:55]\n"
  "v_mfma_f32_16x16x32_bf16 v[184:187], v[56:59], a[64:67], v[184:187]\n"
  "v_perm_b32 v60, v103, v99, s70\n"
  "v_perm_b32 v61, v111, v107, s70\n"
  "v_mfma_f32_16x16x32_bf16 v[216:219], v[56:59], a[68:71], v[216:219]\n"
  "v_perm_b32 v62, v119, v115, s70\n"
  "v_perm_b32 v63, v127, v123, s70\n"
  "v_mfma_f32_16x16x32_bf16 v[248:251], v[56:59], a[72:75], v[248:251]\n"
  "s_xor_b32 s72, s67, 0x340000\n"
  "buffer_load_dwordx4 v[96:99], v2, s[44:47], s72 offen\n"
  "v_mfma_f32_16x16x32_bf16 a[24:27], v[56:59], a[76:79], a[24:27]\n"
  "buffer_load_dwordx4 v[100:103], v3, s[44:47], s72 offen\n"
  "v_mfma_f32_16x16x32_bf16 a[56:59], v[56:59], a[80:83], a[56:59]\n"
  "s_waitcnt vmcnt(10)\n"
  "buffer_load_dwordx4 v[104:107], v4, s[44:47], s72 offen\n"
  "v_mfma_f32_16x16x32_bf16 v[188:191], v[60:63], a[64:67], v[188:191]\n"
  "v_perm_b32 v56, v132, v128, s69\n"
  "v_perm_b32 v57, v140, v136, s69\n"
  "buffer_load_dwordx4 v[108:111], v5, s[44:47], s72 offen\n"
  "v_mfma_f32_16x16x32_bf16 v[220:223], v[60:63], a[68:71], v[220:223]\n"
  "v_perm_b32 v58, v148, v144, s69\n"
  "v_perm_b32 v59, v156, v152, s69\n"
  "buffer_load_dwordx4 v[112:115], v6, s[44:47], s72 offen\n"
  "v_mfma_f32_16x16x32_bf16 v[252:255], v[60:63], a[72:75], v[252:255]\n"
  "buffer_load_dwordx4 v[116:119], v7, s[44:47], s72 offen\n"
  "v_mfma_f32_16x16x32_bf16 a[28:31], v[60:63], a[76:79], a[28:31]\n"
  "s_waitcnt lgkmcnt(0)\n"
  "buffer_load_dwordx4 v[120:123], v8, s[44:47], s72 offen\n"
  "v_mfma_f32_16x16x32_bf16 a[60:63], v[60:63], a[80:83], a[60:63]\n"
  "buffer_load_dwordx4 v[124:127], v9, s[44:47], s72 offen\n"
  "v_mfma_f32_16x16x32_bf16 v[160:163], v[56:59], a[84:87], v[160:163]\n"
  "v_perm_b32 v60, v132, v128, s70\n"
  "v_perm_b32 v61, v140, v136, s70\n"
  "v_mfma_f32_16x16x32_bf16 v[192:195], v[56:59], a[88:91], v[192:195]\n"
  "v_perm_b32 v62, v148, v144, s70\n"
  "v_perm_b32 v63, v156, v152, s70\n"
  "v_mfma_f32_16x16x32_bf16 v[224:227], v[56:59], a[92:95], v[224:227]\n"
  "buffer_load_dwordx4 v14, s[36:39], s94 offen offset:1024 sc1 nt lds\n"
  "v_mfma_f32_16x16x32_bf16 a[0:3], v[56:59], a[96:99], a[0:3]\n"
  "v_xor_b32 v12, 0xf000, v11\n"
  "v_mfma_f32_16x16x32_bf16 a[32:35], v[56:59], a[100:103], a[32:35]\n"
  "s_barrier\n"
  "buffer_load_dwordx4 v15, s[36:39], s94 offen offset:2048 sc1 nt lds\n"
  "v_mfma_f32_16x16x32_bf16 v[164:167], v[60:63], a[84:87], v[164:167]\n"
  "v_perm_b32 v56, v133, v129, s69\n"
  "v_perm_b32 v57, v141, v137, s69\n"
  "buffer_load_dwordx4 v16, s[36:39], s94 offen offset:3072 sc1 nt lds\n"
  "v_mfma_f32_16x16x32_bf16 v[196:199], v[60:63], a[88:91], v[196:199]\n"
  "v_perm_b32 v58, v149, v145, s69\n"
  "v_perm_b32 v59, v157, v153, s69\n"
  "v_mfma_f32_16x16x32_bf16 v[228:231], v[60:63], a[92:95], v[228:231]\n"
  "s_add_u32 m0, s77, 0x4000\n"
  "s_nop 0\n"
  "buffer_load_dwordx4 v13, s[36:39], s95 offen sc1 nt lds\n"
  "v_mfma_f32_16x16x32_bf16 a[4:7], v[60:63], a[96:99], a[4:7]\n"
  "ds_read_b128 a[64:67], v12\n"
  "v_mfma_f32_16x16x32_bf16 a[36:39], v[60:63], a[100:103], a[36:39]\n"
  "buffer_load_dwordx4 v14, s[36:39], s95 offen offset:1024 sc1 nt lds\n"
  "v_mfma_f32_16x16x32_bf16 v[168:171], v[56:59], a[84:87], v[168:171]\n"
  "v_perm_b32 v60, v133, v129, s70\n"
  "v_perm_b32 v61, v141, v137, s70\n"
  "buffer_load_dwordx4 v15, s[36:39], s95 offen offset:2048 sc1 nt lds\n"
  "v_mfma_f32_16x16x32_bf16 v[200:203], v[56:59], a[88:91], v[200:203]\n"
  "v_perm_b32 v62, v149, v145, s70\n"
  "v_perm_b32 v63, v157, v153, s70\n"
  "v_mfma_f32_16x16x32_bf16 v[232:235], v[56:59], a[92:95], v[232:235]\n"
  "buffer_load_dwordx4 v16, s[36:39], s95 offen offset:3072 sc1 nt lds\n"
  "v_mfma_f32_16x16x32_bf16 a[8:11], v[56:59], a[96:99], a[8:11]\n"
  "ds_read_b128 a[68:71], v12 offset:4096\n"
  "v_mfma_f32_16x16x32_bf16 a[40:43], v[56:59], a[100:103], a[40:43]\n"
  "s_add_u32 m0, s76, 0\n"
  "s_nop 0\n"
  "buffer_load_dwordx4 v17, s[40:43], 0 offen sc1 nt lds\n"
  "v_mfma_f32_16x16x32_bf16 v[172:175], v[60:63], a[84:87], v[172:175]\n"
  "v_perm_b32 v56, v134, v130, s69\n"
  "v_perm_b32 v57, v142, v138, s69\n"
  "v_mfma_f32_16x16x32_bf16 v[204:207], v[60:63], a[88:91], v[204:207]\n"
  "v_perm_b32 v58, v150, v146, s69\n"
  "v_perm_b32 v59, v158, v154, s69\n"
  "v_mfma_f32_16x16x32_bf16 v[236:239], v[60:63], a[92:95], v[236:239]\n"
  "v_mfma_f32_16x16x32_bf16 a[12:15], v[60:63], a[96:99], a[12:15]\n"
  "ds_read_b128 a[72:75], v12 offset:8192\n"
  "v_mfma_f32_16x16x32_bf16 a[44:47], v[60:63], a[100:103], a[44:47]\n"
  "v_mfma_f32_16x16x32_bf16 v[176:179], v[56:59], a[84:87], v[176:179]\n"
  "v_perm_b32 v60, v134, v130, s70\n"
  "v_perm_b32 v61, v142, v138, s70\n"
  "v_mfma_f32_16x16x32_bf16 v[208:211], v[56:59], a[88:91], v[208:211]\n"
  "v_perm_b32 v62, v150, v146, s70\n"
  "v_perm_b32 v63, v158, v154, s70\n"
  "v_mfma_f32_16x16x32_bf16 v[240:243], v[56:59], a[92:95], v[240:243]\n"
  "v_mfma_f32_16x16x32_bf16 a[16:19], v[56:59], a[96:99], a[16:19]\n"
  "ds_read_b128 a[76:79], v12 offset:12288\n"
  "v_mfma_f32_16x16x32_bf16 a[48:51], v[56:59], a[100:103], a[48:51]\n"
  "v_mfma_f32_16x16x32_bf16 v[180:183], v[60:63], a[84:87], v[180:183]\n"
  "v_perm_b32 v56, v135, v131, s69\n"
  "v_perm_b32 v57, v143, v139, s69\n"
  "v_mfma_f32_16x16x32_bf16 v[212:215], v[60:63], a[88:91], v[212:215]\n"
  "v_perm_b32 v58, v151, v147, s69\n"
  "v_perm_b32 v59, v159, v155, s69\n"
  "v_mfma_f32_16x16x32_bf16 v[244:247], v[60:63], a[92:95], v[244:247]\n"
  "v_mfma_f32_16x16x32_bf16 a[20:23], v[60:63], a[96:99], a[20:23]\n"
  "ds_read_b128 a[80:83], v12 offset:16384\n"
  "v_mfma_f32_16x16x32_bf16 a[52:55], v[60:63], a[100:103], a[52:55]\n"
  "v_mfma_f32_16x16x32_bf16 v[184:187], v[56:59], a[84:87], v[184:187]\n"
  "v_perm_b32 v60, v135, v131, s70\n"
  "v_perm_b32 v61, v143, v139, s70\n"
  "v_mfma_f32_16x16x32_bf16 v[216:219], v[56:59], a[88:91], v[216:219]\n"
  "v_perm_b32 v62, v151, v147, s70\n"
  "v_perm_b32 v63, v159, v155, s70\n"
  "v_mfma_f32_16x16x32_bf16 v[248:251], v[56:59], a[92:95], v[248:251]\n"
  "v_mfma_f32_16x16x32_bf16 a[24:27], v[56:59], a[96:99], a[24:27]\n"
  "v_mfma_f32_16x16x32_bf16 a[56:59], v[56:59], a[100:103], a[56:59]\n"
  "s_waitcnt vmcnt(16)\n"
  "v_mfma_f32_16x16x32_bf16 v[188:191], v[60:63], a[84:87], v[188:191]\n"
  "v_perm_b32 v56, v68, v64, s69\n"
  "v_perm_b32 v57, v76, v72, s69\n"
  "v_mfma_f32_16x16x32_bf16 v[220:223], v[60:63], a[88:91], v[220:223]\n"
  "v_perm_b32 v58, v84, v80, s69\n"
  "v_perm_b32 v59, v92, v88, s69\n"
  "v_mfma_f32_16x16x32_bf16 v[252:255], v[60:63], a[92:95], v[252:255]\n"
  "v_mfma_f32_16x16x32_bf16 a[28:31], v[60:63], a[96:99], a[28:31]\n"
  "s_waitcnt lgkmcnt(0)\n"
  "v_mfma_f32_16x16x32_bf16 a[60:63], v[60:63], a[100:103], a[60:63]\n"
  "v_mfma_f32_16x16x32_bf16 v[160:163], v[56:59], a[64:67], v[160:163]\n"
  "v_perm_b32 v60, v68, v64, s70\n"
  "v_perm_b32 v61, v76, v72, s70\n"
  "v_mfma_f32_16x16x32_bf16 v[192:195], v[56:59], a[68:71], v[192:195]\n"
  "v_perm_b32 v62, v84, v80, s70\n"
  "v_perm_b32 v63, v92, v88, s70\n"
  "v_mfma_f32_16x16x32_bf16 v[224:227], v[56:59], a[72:75], v[224:227]\n"
  "v_mfma_f32_16x16x32_bf16 a[0:3], v[56:59], a[76:79], a[0:3]\n"
  "v_xor_b32 v12, 0xf040, v11\n"
  "v_mfma_f32_16x16x32_bf16 a[32:35], v[56:59], a[80:83], a[32:35]\n"
  "v_mfma_f32_16x16x32_bf16 v[164:167], v[60:63], a[64:67], v[164:167]\n"
  "v_perm_b32 v56, v69, v65, s69\n"
  "v_perm_b32 v57, v77, v73, s69\n"
  "v_mfma_f32_16x16x32_bf16 v[196:199], v[60:63], a[68:71], v[196:199]\n"
  "v_perm_b32 v58, v85, v81, s69\n"
  "v_perm_b32 v59, v93, v89, s69\n"
  "v_mfma_f32_16x16x32_bf16 v[228:231], v[60:63], a[72:75], v[228:231]\n"
  "v_mfma_f32_16x16x32_bf16 a[4:7], v[60:63], a[76:79], a[4:7]\n"
  "ds_read_b128 a[84:87], v12\n"
  "v_mfma_f32_16x16x32_bf16 a[36:39], v[60:63], a[80:83], a[36:39]\n"
  "v_mfma_f32_16x16x32_bf16 v[168:171], v[56:59], a[64:67], v[168:171]\n"
  "v_perm_b32 v60, v69, v65, s70\n"
  "v_perm_b32 v61, v77, v73, s70\n"
  "v_mfma_f32_16x16x32_bf16 v[200:203], v[56:59], a[68:71], v[200:203]\n"
  "v_perm_b32 v62, v85, v81, s70\n"
  "v_perm_b32 v63, v93, v89, s70\n"
  "v_mfma_f32_16x16x32_bf16 v[232:235], v[56:59], a[72:75], v[232:235]\n"
  "v_mfma_f32_16x16x32_bf16 a[8:11], v[56:59], a[76:79], a[8:11]\n"
  "ds_read_b128 a[88:91], v12 offset:4096\n"
  "v_mfma_f32_16x16x32_bf16 a[40:43], v[56:59], a[80:83], a[40:43]\n"
  "v_mfma_f32_16x16x32_bf16 v[172:175], v[60:63], a[64:67], v[172:175]\n"
  "v_perm_b32 v56, v70, v66, s69\n"
  "v_perm_b32 v57, v78, v74, s69\n"
  "v_mfma_f32_16x16x32_bf16 v[204:207], v[60:63], a[68:71], v[204:207]\n"
  "v_perm_b32 v58, v86, v82, s69\n"
  "v_perm_b32 v59, v94, v90, s69\n"
  "v_mfma_f32_16x16x32_bf16 v[236:239], v[60:63], a[72:75], v[236:239]\n"
  "v_mfma_f32_16x16x32_bf16 a[12:15], v[60:63], a[76:79], a[12:15]\n"
  "ds_read_b128 a[92:95], v12 offset:8192\n"
  "v_mfma_f32_16x16x32_bf16 a[44:47], v[60:63], a[80:83], a[44:47]\n"
  "v_mfma_f32_16x16x32_bf16 v[176:179], v[56:59], a[64:67], v[176:179]\n"
  "v_perm_b32 v60, v70, v66, s70\n"
  "v_perm_b32 v61, v78, v74, s70\n"
  "v_mfma_f32_16x16x32_bf16 v[208:211], v[56:59], a[68:71], v[208:211]\n"
  "v_perm_b32 v62, v86, v82, s70\n"
  "v_perm_b32 v63, v94, v90, s70\n"
  "v_mfma_f32_16x16x32_bf16 v[240:243], v[56:59], a[72:75], v[240:243]\n"
  "s_xor_b32 s72, s67, 0x380000\n"
  "buffer_load_dwordx4 v[128:131], v2, s[44:47], s72 offen\n"
  "v_mfma_f32_16x16x32_bf16 a[16:19], v[56:59], a[76:79], a[16:19]\n"
  "ds_read_b128 a[96:99], v12 offset:12288\n"
  "v_mfma_f32_16x16x32_bf16 a[48:51], v[56:59], a[80:83], a[48:51]\n"
  "buffer_load_dwordx4 v[132:135], v3, s[44:47], s72 offen\n"
  "v_mfma_f32_16x16x32_bf16 v[180:183], v[60:63], a[64:67], v[180:183]\n"
  "v_perm_b32 v56, v71, v67, s69\n"
  "v_perm_b32 v57, v79, v75, s69\n"
  "v_mfma_f32_16x16x32_bf16 v[212:215], v[60:63], a[68:71], v[212:215]\n"
  "v_perm_b32 v58, v87, v83, s69\n"
  "v_perm_b32 v59, v95, v91, s69\n"
  "buffer_load_dwordx4 v[136:139], v4, s[44:47], s72 offen\n"
  "v_mfma_f32_16x16x32_bf16 v[244:247], v[60:63], a[72:75], v[244:247]\n"
  "v_mfma_f32_16x16x32_bf16 a[20:23], v[60:63], a[76:79], a[20:23]\n"
  "buffer_load_dwordx4 v[140:143], v5, s[44:47], s72 offen\n"
  "ds_read_b128 a[100:103], v12 offset:16384\n"
  "v_mfma_f32_16x16x32_bf16 a[52:55], v[60:63], a[80:83], a[52:55]\n"
  "v_mfma_f32_16x16x32_bf16 v[184:187], v[56:59], a[64:67], v[184:187]\n"
  "v_perm_b32 v60, v71, v67, s70\n"
  "v_perm_b32 v61, v79, v75, s70\n"
  "buffer_load_dwordx4 v[144:147], v6, s[44:47], s72 offen\n"
  "v_mfma_f32_16x16x32_bf16 v[216:219], v[56:59], a[68:71], v[216:219]\n"
  "v_perm_b32 v62, v87, v83, s70\n"
  "v_perm_b32 v63, v95, v91, s70\n"
  "v_mfma_f32_16x16x32_bf16 v[248:251], v[56:59], a[72:75], v[248:251]\n"
  "buffer_load_dwordx4 v[148:151], v7, s[44:47], s72 offen\n"
  "v_mfma_f32_16x16x32_bf16 a[24:27], v[56:59], a[76:79], a[24:27]\n"
  "v_mfma_f32_16x16x32_bf16 a[56:59], v[56:59], a[80:83], a[56:59]\n"
  "s_waitcnt vmcnt(14)\n"
  "buffer_load_dwordx4 v[152:155], v8, s[44:47], s72 offen\n"
  "v_mfma_f32_16x16x32_bf16 v[188:191], v[60:63], a[64:67], v[188:191]\n"
  "v_perm_b32 v56, v100, v96, s69\n"
  "v_perm_b32 v57, v108, v104, s69\n"
  "v_mfma_f32_16x16x32_bf16 v[220:223], v[60:63], a[68:71], v[220:223]\n"
  "v_perm_b32 v58, v116, v112, s69\n"
  "v_perm_b32 v59, v124, v120, s69\n"
  "buffer_load_dwordx4 v[156:159], v9, s[44:47], s72 offen\n"
  "v_mfma_f32_16x16x32_bf16 v[252:255], v[60:63], a[72:75], v[252:255]\n"
  "v_mfma_f32_16x16x32_bf16 a[28:31], v[60:63], a[76:79], a[28:31]\n"
  "s_waitcnt lgkmcnt(0)\n"
  "v_mfma_f32_16x16x32_bf16 a[60:63], v[60:63], a[80:83], a[60:63]\n"
  "v_mfma_f32_16x16x32_bf16 v[160:163], v[56:59], a[84:87], v[160:163]\n"
  "v_perm_b32 v60, v100, v96, s70\n"
  "v_perm_b32 v61, v108, v104, s70\n"
  "s_xor_b32 s72, s67, 0x3c0000\n"
  "buffer_load_dwordx4 v[64:67], v2, s[44:47], s72 offen\n"
  "v_mfma_f32_16x16x32_bf16 v[192:195], v[56:59], a[88:91], v[192:195]\n"
  "v_perm_b32 v62, v116, v112, s70\n"
  "v_perm_b32 v63, v124, v120, s70\n"
  "v_mfma_f32_16x16x32_bf16 v[224:227], v[56:59], a[92:95], v[224:227]\n"
  "buffer_load_dwordx4 v[68:71], v3, s[44:47], s72 offen\n"
  "v_mfma_f32_16x16x32_bf16 a[0:3], v[56:59], a[96:99], a[0:3]\n"
  "v_xor_b32 v12, 0xf080, v11\n"
  "v_mfma_f32_16x16x32_bf16 a[32:35], v[56:59], a[100:103], a[32:35]\n"
  "buffer_load_dwordx4 v[72:75], v4, s[44:47], s72 offen\n"
  "v_mfma_f32_16x16x32_bf16 v[164:167], v[60:63], a[84:87], v[164:167]\n"
  "v_perm_b32 v56, v101, v97, s69\n"
  "v_perm_b32 v57, v109, v105, s69\n"
  "v_mfma_f32_16x16x32_bf16 v[196:199], v[60:63], a[88:91], v[196:199]\n"
  "v_perm_b32 v58, v117, v113, s69\n"
  "v_perm_b32 v59, v125, v121, s69\n"
  "buffer_load_dwordx4 v[76:79], v5, s[44:47], s72 offen\n"
  "v_mfma_f32_16x16x32_bf16 v[228:231], v[60:63], a[92:95], v[228:231]\n"
  "v_mfma_f32_16x16x32_bf16 a[4:7], v[60:63], a[96:99], a[4:7]\n"
  "buffer_load_dwordx4 v[80:83], v6, s[44:47], s72 offen\n"
  "ds_read_b128 a[64:67], v12\n"
  "v_mfma_f32_16x16x32_bf16 a[36:39], v[60:63], a[100:103], a[36:39]\n"
  "v_mfma_f32_16x16x32_bf16 v[168:171], v[56:59], a[84:87], v[168:171]\n"
  "v_perm_b32 v60, v101, v97, s70\n"
  "v_perm_b32 v61, v109, v105, s70\n"
  "buffer_load_dwordx4 v[84:87], v7, s[44:47], s72 offen\n"
  "v_mfma_f32_16x16x32_bf16 v[200:203], v[56:59], a[88:91], v[200:203]\n"
  "v_perm_b32 v62, v117, v113, s70\n"
  "v_perm_b32 v63, v125, v121, s70\n"
  "v_mfma_f32_16x16x32_bf16 v[232:235], v[56:59], a[92:95], v[232:235]\n"
  "buffer_load_dwordx4 v[88:91], v8, s[44:47], s72 offen\n"
  "v_mfma_f32_16x16x32_bf16 a[8:11], v[56:59], a[96:99], a[8:11]\n"
  "ds_read_b128 a[68:71], v12 offset:4096\n"
  "v_mfma_f32_16x16x32_bf16 a[40:43], v[56:59], a[100:103], a[40:43]\n"
  "buffer_load_dwordx4 v[92:95], v9, s[44:47], s72 offen\n"
  "v_mfma_f32_16x16x32_bf16 v[172:175], v[60:63], a[84:87], v[172:175]\n"
  "v_perm_b32 v56, v102, v98, s69\n"
  "v_perm_b32 v57, v110, v106, s69\n"
  "v_mfma_f32_16x16x32_bf16 v[204:207], v[60:63], a[88:91], v[204:207]\n"
  "v_perm_b32 v58, v118, v114, s69\n"
  "v_perm_b32 v59, v126, v122, s69\n"
  "v_mfma_f32_16x16x32_bf16 v[236:239], v[60:63], a[92:95], v[236:239]\n"
  "v_mfma_f32_16x16x32_bf16 a[12:15], v[60:63], a[96:99], a[12:15]\n"
  "ds_read_b128 a[72:75], v12 offset:8192\n"
  "v_mfma_f32_16x16x32_bf16 a[44:47], v[60:63], a[100:103], a[44:47]\n"
  "v_mfma_f32_16x16x32_bf16 v[176:179], v[56:59], a[84:87], v[176:179]\n"
  "v_perm_b32 v60, v102, v98, s70\n"
  "v_perm_b32 v61, v110, v106, s70\n"
  "v_mfma_f32_16x16x32_bf16 v[208:211], v[56:59], a[88:91], v[208:211]\n"
  "v_perm_b32 v62, v118, v114, s70\n"
  "v_perm_b32 v63, v126, v122, s70\n"
  "v_mfma_f32_16x16x32_bf16 v[240:243], v[56:59], a[92:95], v[240:243]\n"
  "v_mfma_f32_16x16x32_bf16 a[16:19], v[56:59], a[96:99], a[16:19]\n"
  "ds_read_b128 a[76:79], v12 offset:12288\n"
  "v_mfma_f32_16x16x32_bf16 a[48:51], v[56:59], a[100:103], a[48:51]\n"
  "v_mfma_f32_16x16x32_bf16 v[180:183], v[60:63], a[84:87], v[180:183]\n"
  "v_perm_b32 v56, v103, v99, s69\n"
  "v_perm_b32 v57, v111, v107, s69\n"
  "v_mfma_f32_16x16x32_bf16 v[212:215], v[60:63], a[88:91], v[212:215]\n"
  "v_perm_b32 v58, v119, v115, s69\n"
  "v_perm_b32 v59, v127, v123, s69\n"
  "v_mfma_f32_16x16x32_bf16 v[244:247], v[60:63], a[92:95], v[244:247]\n"
  "v_mfma_f32_16x16x32_bf16 a[20:23], v[60:63], a[96:99], a[20:23]\n"
  "ds_read_b128 a[80:83], v12 offset:16384\n"
  "v_mfma_f32_16x16x32_bf16 a[52:55], v[60:63], a[100:103], a[52:55]\n"
  "v_mfma_f32_16x16x32_bf16 v[184:187], v[56:59], a[84:87], v[184:187]\n"
  "v_perm_b32 v60, v103, v99, s70\n"
  "v_perm_b32 v61, v111, v107, s70\n"
  "v_mfma_f32_16x16x32_bf16 v[216:219], v[56:59], a[88:91], v[216:219]\n"
  "v_perm_b32 v62, v119, v115, s70\n"
  "v_perm_b32 v63, v127, v123, s70\n"
  "v_mfma_f32_16x16x32_bf16 v[248:251], v[56:59], a[92:95], v[248:251]\n"
  "v_mfma_f32_16x16x32_bf16 a[24:27], v[56:59], a[96:99], a[24:27]\n"
  "v_xor_b32 v49, 16, v19\n"
  "v_xor_b32 v50, 32, v19\n"
  "v_mfma_f32_16x16x32_bf16 a[56:59], v[56:59], a[100:103], a[56:59]\n"
  "s_waitcnt vmcnt(8)\n"
  "v_mfma_f32_16x16x32_bf16 v[188:191], v[60:63], a[84:87], v[188:191]\n"
  "v_perm_b32 v56, v132, v128, s69\n"
  "v_perm_b32 v57, v140, v136, s69\n"
  "v_mfma_f32_16x16x32_bf16 v[220:223], v[60:63], a[88:91], v[220:223]\n"
  "v_perm_b32 v58, v148, v144, s69\n"
  "v_perm_b32 v59, v156, v152, s69\n"
  "v_mfma_f32_16x16x32_bf16 v[252:255], v[60:63], a[92:95], v[252:255]\n"
  "v_mfma_f32_16x16x32_bf16 a[28:31], v[60:63], a[96:99], a[28:31]\n"
  "s_waitcnt lgkmcnt(0)\n"
  "v_mfma_f32_16x16x32_bf16 a[60:63], v[60:63], a[100:103], a[60:63]\n"
  "v_xor_b32 v51, 48, v19\n"
  "v_mfma_f32_16x16x32_bf16 v[160:163], v[56:59], a[64:67], v[160:163]\n"
  "v_perm_b32 v60, v132, v128, s70\n"
  "v_perm_b32 v61, v140, v136, s70\n"
  "v_mfma_f32_16x16x32_bf16 v[192:195], v[56:59], a[68:71], v[192:195]\n"
  "v_perm_b32 v62, v148, v144, s70\n"
  "v_perm_b32 v63, v156, v152, s70\n"
  "v_mfma_f32_16x16x32_bf16 v[224:227], v[56:59], a[72:75], v[224:227]\n"
  "ds_read_b128 a[104:107], v19\n"
  "ds_read_b128 a[108:111], v49\n"
  "buffer_load_dwordx4 v18, s[40:43], 0 offen offset:1024 sc1 nt lds\n"
  "v_mfma_f32_16x16x32_bf16 a[0:3], v[56:59], a[76:79], a[0:3]\n"
  "v_xor_b32 v12, 0xf0c0, v11\n"
  "v_mfma_f32_16x16x32_bf16 a[32:35], v[56:59], a[80:83], a[32:35]\n"
  "buffer_load_dwordx4 v17, s[40:43], 0 offen offset:2048 sc1 nt lds\n"
  "v_mfma_f32_16x16x32_bf16 v[164:167], v[60:63], a[64:67], v[164:167]\n"
  "v_perm_b32 v56, v133, v129, s69\n"
  "v_perm_b32 v57, v141, v137, s69\n"
  "buffer_load_dwordx4 v18, s[40:43], 0 offen offset:3072 sc1 nt lds\n"
  "v_mfma_f32_16x16x32_bf16 v[196:199], v[60:63], a[68:71], v[196:199]\n"
  "v_perm_b32 v58, v149, v145, s69\n"
  "v_perm_b32 v59, v157, v153, s69\n"
  "v_mfma_f32_16x16x32_bf16 v[228:231], v[60:63], a[72:75], v[228:231]\n"
  "ds_read_b128 a[112:115], v50\n"
  "ds_read_b128 a[116:119], v51\n"
  "s_add_u32 m0, s76, 0x4000\n"
  "s_nop 0\n"
  "buffer_load_dwordx4 v17, s[40:43], s92 offen sc1 nt lds\n"
  "v_mfma_f32_16x16x32_bf16 a[4:7], v[60:63], a[76:79], a[4:7]\n"
  "ds_read_b128 a[84:87], v12\n"
  "v_mfma_f32_16x16x32_bf16 a[36:39], v[60:63], a[80:83], a[36:39]\n"
  "buffer_load_dwordx4 v18, s[40:43], s92 offen offset:1024 sc1 nt lds\n"
  "v_mfma_f32_16x16x32_bf16 v[168:171], v[56:59], a[64:67], v[168:171]\n"
  "v_perm_b32 v60, v133, v129, s70\n"
  "v_perm_b32 v61, v141, v137, s70\n"
  "buffer_load_dwordx4 v17, s[40:43], s92 offen offset:2048 sc1 nt lds\n"
  "v_mfma_f32_16x16x32_bf16 v[200:203], v[56:59], a[68:71], v[200:203]\n"
  "v_perm_b32 v62, v149, v145, s70\n"
  "v_perm_b32 v63, v157, v153, s70\n"
  "v_mfma_f32_16x16x32_bf16 v[232:235], v[56:59], a[72:75], v[232:235]\n"
  "ds_read_b128 a[120:123], v19 offset:4096\n"
  "ds_read_b128 a[124:127], v49 offset:4096\n"
  "buffer_load_dwordx4 v18, s[40:43], s92 offen offset:3072 sc1 nt lds\n"
  "v_mfma_f32_16x16x32_bf16 a[8:11], v[56:59], a[76:79], a[8:11]\n"
  "ds_read_b128 a[88:91], v12 offset:4096\n"
  "v_mfma_f32_16x16x32_bf16 a[40:43], v[56:59], a[80:83], a[40:43]\n"
  "v_mfma_f32_16x16x32_bf16 v[172:175], v[60:63], a[64:67], v[172:175]\n"
  "v_perm_b32 v56, v134, v130, s69\n"
  "v_perm_b32 v57, v142, v138, s69\n"
  "v_mfma_f32_16x16x32_bf16 v[204:207], v[60:63], a[68:71], v[204:207]\n"
  "v_perm_b32 v58, v150, v146, s69\n"
  "v_perm_b32 v59, v158, v154, s69\n"
  "v_mfma_f32_16x16x32_bf16 v[236:239], v[60:63], a[72:75], v[236:239]\n"
  "ds_read_b128 a[128:131], v50 offset:4096\n"
  "ds_read_b128 a[132:135], v51 offset:4096\n"
  "v_mfma_f32_16x16x32_bf16 a[12:15], v[60:63], a[76:79], a[12:15]\n"
  "ds_read_b128 a[92:95], v12 offset:8192\n"
  "v_mfma_f32_16x16x32_bf16 a[44:47], v[60:63], a[80:83], a[44:47]\n"
  "v_mfma_f32_16x16x32_bf16 v[176:179], v[56:59], a[64:67], v[176:179]\n"
  "v_perm_b32 v60, v134, v130, s70\n"
  "v_perm_b32 v61, v142, v138, s70\n"
  "v_mfma_f32_16x16x32_bf16 v[208:211], v[56:59], a[68:71], v[208:211]\n"
  "v_perm_b32 v62, v150, v146, s70\n"
  "v_perm_b32 v63, v158, v154, s70\n"
  "v_mfma_f32_16x16x32_bf16 v[240:243], v[56:59], a[72:75], v[240:243]\n"
  "ds_read_b128 a[136:139], v19 offset:8192\n"
  "ds_read_b128 a[140:143], v49 offset:8192\n"
  "v_mfma_f32_16x16x32_bf16 a[16:19], v[56:59], a[76:79], a[16:19]\n"
  "ds_read_b128 a[96:99], v12 offset:12288\n"
  "v_mfma_f32_16x16x32_bf16 a[48:51], v[56:59], a[80:83], a[48:51]\n"
  "v_mfma_f32_16x16x32_bf16 v[180:183], v[60:63], a[64:67], v[180:183]\n"
  "v_perm_b32 v56, v135, v131, s69\n"
  "v_perm_b32 v57, v143, v139, s69\n"
  "v_mfma_f32_16x16x32_bf16 v[212:215], v[60:63], a[68:71], v[212:215]\n"
  "v_perm_b32 v58, v151, v147, s69\n"
  "v_perm_b32 v59, v159, v155, s69\n"
  "v_mfma_f32_16x16x32_bf16 v[244:247], v[60:63], a[72:75], v[244:247]\n"
  "ds_read_b128 a[144:147], v50 offset:8192\n"
  "ds_read_b128 a[148:151], v51 offset:8192\n"
  "v_mfma_f32_16x16x32_bf16 a[20:23], v[60:63], a[76:79], a[20:23]\n"
  "ds_read_b128 a[100:103], v12 offset:16384\n"
  "v_mfma_f32_16x16x32_bf16 a[52:55], v[60:63], a[80:83], a[52:55]\n"
  "v_mfma_f32_16x16x32_bf16 v[184:187], v[56:59], a[64:67], v[184:187]\n"
  "v_perm_b32 v60, v135, v131, s70\n"
  "v_perm_b32 v61, v143, v139, s70\n"
  "v_mfma_f32_16x16x32_bf16 v[216:219], v[56:59], a[68:71], v[216:219]\n"
  "v_perm_b32 v62, v151, v147, s70\n"
  "v_perm_b32 v63, v159, v155, s70\n"
  "v_mfma_f32_16x16x32_bf16 v[248:251], v[56:59], a[72:75], v[248:251]\n"
  "ds_read_b128 a[152:155], v19 offset:12288\n"
  "ds_read_b128 a[156:159], v49 offset:12288\n"
  "v_mfma_f32_16x16x32_bf16 a[24:27], v[56:59], a[76:79], a[24:27]\n"
  "ds_read_b128 a[160:163], v50 offset:12288\n"
  "ds_read_b128 a[164:167], v51 offset:12288\n"
  "v_mfma_f32_16x16x32_bf16 a[56:59], v[56:59], a[80:83], a[56:59]\n"
  "s_waitcnt vmcnt(7)\n"
  "v_mfma_f32_16x16x32_bf16 v[188:191], v[60:63], a[64:67], v[188:191]\n"
  "v_perm_b32 v96, v68, v64, s69\n"
  "v_perm_b32 v97, v76, v72, s69\n"
  "v_mfma_f32_16x16x32_bf16 v[220:223], v[60:63], a[68:71], v[220:223]\n"
  "v_perm_b32 v98, v84, v80, s69\n"
  "v_perm_b32 v99, v92, v88, s69\n"
  "v_mfma_f32_16x16x32_bf16 v[252:255], v[60:63], a[72:75], v[252:255]\n"
  "ds_read_b128 a[168:171], v19 offset:16384\n"
  "ds_read_b128 a[172:175], v49 offset:16384\n"
  "v_mfma_f32_16x16x32_bf16 a[28:31], v[60:63], a[76:79], a[28:31]\n"
  "ds_read_b128 a[176:179], v50 offset:16384\n"
  "ds_read_b128 a[180:183], v51 offset:16384\n"
  "v_mfma_f32_16x16x32_bf16 a[60:63], v[60:63], a[80:83], a[60:63]\n"
  "Lj_%=:\n"
  "s_waitcnt lgkmcnt(0)\n"
  "s_barrier\n"
  "s_add_u32 m0, s76, 0x8000\n"
  "s_xor_b32 s74, s79, 0\n"
  "buffer_load_dwordx4 v17, s[40:43], s93 offen sc1 nt lds\n"
  "v_mov_b32 v5, -1\n"
  "v_mov_b32 v4, -1\n"
  "s_mul_i32 s98, s64, 0xa00\n"
  "s_add_u32 s98, s98, s22\n"
  "v_mov_b32 v7, -1\n"
  "v_mov_b32 v6, -1\n"
  "v_mfma_f32_16x16x32_bf16 v[160:163], v[96:99], a[84:87], v[160:163]\n"
  "v_perm_b32 v108, v69, v65, s70\n"
  "v_perm_b32 v118, v86, v82, s70\n"
  "v_mfma_f32_16x16x32_bf16 a[0:3], v[96:99], a[96:99], a[0:3]\n"
  "v_perm_b32 v111, v93, v89, s70\n"
  "v_perm_b32 v103, v92, v88, s70\n"
  "buffer_load_dwordx4 v18, s[40:43], s93 offen offset:1024 sc1 nt lds\n"
  "v_mfma_f32_16x16x32_bf16 v[192:195], v[96:99], a[88:91], v[192:195]\n"
  "v_perm_b32 v119, v94, v90, s70\n"
  "v_perm_b32 v102, v84, v80, s70\n"
  "v_mfma_f32_16x16x32_bf16 v[224:227], v[96:99], a[92:95], v[224:227]\n"
  "v_perm_b32 v116, v70, v66, s70\n"
  "v_perm_b32 v112, v70, v66, s69\n"
  "v_mfma_f32_16x16x32_bf16 a[32:35], v[96:99], a[100:103], a[32:35]\n"
  "v_perm_b32 v117, v78, v74, s70\n"
  "v_perm_b32 v106, v85, v81, s69\n"
  "v_perm_b32 v101, v76, v72, s70\n"
  "v_mfma_f32_16x16x32_bf16 v[180:183], v[116:119], a[84:87], v[180:183]\n"
  "v_perm_b32 v122, v87, v83, s69\n"
  "Lh0_%=:\n"
  "s_cbranch_execz Lh1_%=\n"
  "v_perm_b32 v105, v77, v73, s69\n"
  "v_mfma_f32_16x16x32_bf16 v[212:215], v[116:119], a[88:91], v[212:215]\n"
  "buffer_load_dwordx4 v17, s[40:43], s93 offen offset:2048 sc1 nt lds\n"
  "v_mfma_f32_16x16x32_bf16 a[52:55], v[116:119], a[100:103], a[52:55]\n"
  "v_perm_b32 v109, v77, v73, s70\n"
  "v_perm_b32 v104, v69, v65, s69\n"
  "v_mfma_f32_16x16x32_bf16 v[244:247], v[116:119], a[92:95], v[244:247]\n"
  "v_perm_b32 v115, v94, v90, s69\n"
  "v_perm_b32 v110, v85, v81, s70\n"
  "v_mfma_f32_16x16x32_bf16 a[20:23], v[116:119], a[96:99], a[20:23]\n"
  "v_perm_b32 v107, v93, v89, s69\n"
  "v_perm_b32 v126, v87, v83, s70\n"
  "v_mfma_f32_16x16x32_bf16 v[172:175], v[108:111], a[84:87], v[172:175]\n"
  "v_perm_b32 v113, v78, v74, s69\n"
  "v_perm_b32 v114, v86, v82, s69\n"
  "v_mfma_f32_16x16x32_bf16 v[168:171], v[104:107], a[84:87], v[168:171]\n"
  "buffer_load_dwordx4 v18, s[40:43], s93 offen offset:3072 sc1 nt lds\n"
  "s_add_u32 m0, s76, 0xc000\n"
  "v_mfma_f32_16x16x32_bf16 v[176:179], v[112:115], a[84:87], v[176:179]\n"
  "v_perm_b32 v121, v79, v75, s69\n"
  "v_perm_b32 v123, v95, v91, s69\n"
  "v_mfma_f32_16x16x32_bf16 v[236:239], v[108:111], a[92:95], v[236:239]\n"
  "v_perm_b32 v125, v79, v75, s70\n"
  "v_perm_b32 v124, v71, v67, s70\n"
  "v_mfma_f32_16x16x32_bf16 v[204:207], v[108:111], a[88:91], v[204:207]\n"
  "v_cvt_pk_bf16_f32 v133, v169, v173\n"
  "v_cvt_pk_bf16_f32 v129, v168, v172\n"
  "v_mfma_f32_16x16x32_bf16 v[208:211], v[112:115], a[88:91], v[208:211]\n"
  "v_cvt_pk_bf16_f32 v134, v177, v181\n"
  "v_cvt_pk_bf16_f32 v142, v179, v183\n"
  "buffer_load_dwordx4 v17, s[40:43], s94 offen sc1 nt lds\n"
  "v_mfma_f32_16x16x32_bf16 v[200:203], v[104:107], a[88:91], v[200:203]\n"
  "v_cvt_pk_bf16_f32 v138, v178, v182\n"
  "v_cvt_pk_bf16_f32 v130, v176, v180\n"
  "v_mfma_f32_16x16x32_bf16 a[40:43], v[104:107], a[100:103], a[40:43]\n"
  "v_perm_b32 v100, v68, v64, s70\n"
  "v_perm_b32 v127, v95, v91, s70\n"
  "v_mfma_f32_16x16x32_bf16 a[48:51], v[112:115], a[100:103], a[48:51]\n"
  "v_perm_b32 v120, v71, v67, s69\n"
  "v_cvt_pk_bf16_f32 v137, v170, v174\n"
  "v_mfma_f32_16x16x32_bf16 v[188:191], v[124:127], a[84:87], v[188:191]\n"
  "s_mov_b32 s63, 0x3e0293ee\n"
  "v_cvt_pk_bf16_f32 v141, v171, v175\n"
  "v_mfma_f32_16x16x32_bf16 v[184:187], v[120:123], a[84:87], v[184:187]\n"
  "buffer_load_dwordx4 v18, s[40:43], s94 offen offset:1024 sc1 nt lds\n"
  "v_mfma_f32_16x16x32_bf16 v[164:167], v[100:103], a[84:87], v[164:167]\n"
  "s_mov_b32 s62, 0x3e0293ee\n"
  "v_cvt_pk_bf16_f32 v146, v208, v212\n"
  "v_mfma_f32_16x16x32_bf16 v[248:251], v[120:123], a[92:95], v[248:251]\n"
  "v_cvt_pk_bf16_f32 v149, v201, v205\n"
  "v_cvt_pk_bf16_f32 v150, v209, v213\n"
  "v_mfma_f32_16x16x32_bf16 v[216:219], v[120:123], a[88:91], v[216:219]\n"
  "v_cvt_pk_bf16_f32 v131, v184, v188\n"
  "v_cvt_pk_bf16_f32 v143, v187, v191\n"
  "v_mfma_f32_16x16x32_bf16 v[228:231], v[100:103], a[92:95], v[228:231]\n"
  "v_cvt_pk_bf16_f32 v136, v162, v166\n"
  "v_cvt_pk_bf16_f32 v135, v185, v189\n"
  "v_mfma_f32_16x16x32_bf16 a[24:27], v[120:123], a[96:99], a[24:27]\n"
  "buffer_load_dwordx4 v17, s[40:43], s94 offen offset:2048 sc1 nt lds\n"
  "v_mfma_f32_16x16x32_bf16 v[252:255], v[124:127], a[92:95], v[252:255]\n"
  "v_cvt_pk_bf16_f32 v139, v186, v190\n"
  "v_cvt_pk_bf16_f32 v132, v161, v165\n"
  "v_mfma_f32_16x16x32_bf16 v[220:223], v[124:127], a[88:91], v[220:223]\n"
  "Lh1_%=:\n"
  "s_cbranch_execz Lh2_%=\n"
  "v_cvt_pk_bf16_f32 v128, v160, v164\n"
  "v_cvt_pk_bf16_f32 v140, v163, v167\n"
  "v_mfma_f32_16x16x32_bf16 v[240:243], v[112:115], a[92:95], v[240:243]\n"
  "v_cvt_pk_bf16_f32 v154, v210, v214\n"
  "v_cvt_pk_bf16_f32 v158, v211, v215\n"
  "v_mfma_f32_16x16x32_bf16 v[76:79], a[152:155], v[128:131], 0\n"
  "v_cvt_pk_bf16_f32 v157, v203, v207\n"
  "v_cvt_pk_bf16_f32 v145, v200, v204\n"
  "v_mfma_f32_16x16x32_bf16 v[64:67], a[104:107], v[128:131], 0\n"
  "buffer_load_dwordx4 v18, s[40:43], s94 offen offset:3072 sc1 nt lds\n"
  "s_add_u32 m0, s76, 0x10000\n"
  "v_mfma_f32_16x16x32_bf16 v[64:67], a[108:111], v[132:135], v[64:67]\n"
  "v_cvt_pk_bf16_f32 v147, v216, v220\n"
  "v_cvt_pk_bf16_f32 v159, v219, v223\n"
  "v_mfma_f32_16x16x32_bf16 v[80:83], a[168:171], v[128:131], 0\n"
  "v_cvt_pk_bf16_f32 v151, v217, v221\n"
  "v_cvt_pk_bf16_f32 v155, v218, v222\n"
  "v_mfma_f32_16x16x32_bf16 v[68:71], a[120:123], v[128:131], 0\n"
  "v_cvt_pk_bf16_f32 v153, v202, v206\n"
  "v_accvgpr_read_b32 v48, a0\n"
  "v_mfma_f32_16x16x32_bf16 v[76:79], a[156:159], v[132:135], v[76:79]\n"
  "v_xor_b32 v14, 0xe0, v20\n"
  "v_xor_b32 v10, 0x60, v20\n"
  "v_mfma_f32_16x16x32_bf16 v[76:79], a[160:163], v[136:139], v[76:79]\n"
  "buffer_load_dwordx4 v17, s[40:43], s95 offen sc1 nt lds\n"
  "v_mfma_f32_16x16x32_bf16 v[76:79], a[164:167], v[140:143], v[76:79]\n"
  "v_accvgpr_read_b32 v53, a20\n"
  "v_xor_b32 v12, 0xa0, v20\n"
  "v_mfma_f32_16x16x32_bf16 v[80:83], a[172:175], v[132:135], v[80:83]\n"
  "v_accvgpr_read_b32 v54, a24\n"
  "v_add_u32 v2, 0x10000, v20\n"
  "v_mfma_f32_16x16x32_bf16 v[80:83], a[176:179], v[136:139], v[80:83]\n"
  "v_xor_b32 v11, 0x80, v20\n"
  "v_xor_b32 v8, 32, v20\n"
  "v_mfma_f32_16x16x32_bf16 v[64:67], a[112:115], v[136:139], v[64:67]\n"
  "v_max3_f32 v59, v76, v77, v78\n"
  "v_add_u32 v15, 0x10000, v8\n"
  "v_mfma_f32_16x16x32_bf16 v[72:75], a[136:139], v[128:131], 0\n"
  "buffer_load_dwordx4 v18, s[40:43], s95 offen offset:1024 sc1 nt lds\n"
  "v_mfma_f32_16x16x32_bf16 v[72:75], a[140:143], v[132:135], v[72:75]\n"
  "v_cvt_pk_bf16_f32 v128, v224, v228\n"
  "v_cvt_pk_bf16_f32 v131, v248, v252\n"
  "v_mfma_f32_16x16x32_bf16 v[72:75], a[144:147], v[136:139], v[72:75]\n"
  "v_cvt_pk_bf16_f32 v130, v240, v244\n"
  "v_xor_b32 v9, 64, v20\n"
  "v_mfma_f32_16x16x32_bf16 v[72:75], a[148:151], v[140:143], v[72:75]\n"
  "v_add_u32 v16, 0x10000, v9\n"
  "v_add_u32 v19, 0x10000, v10\n"
  "v_mfma_f32_16x16x32_bf16 v[80:83], a[180:183], v[140:143], v[80:83]\n"
  "v_xor_b32 v13, 0xc0, v20\n"
  "s_lshr_b32 s13, s30, 2\n"
  "v_mfma_f32_16x16x32_bf16 v[68:71], a[124:127], v[132:135], v[68:71]\n"
  "buffer_load_dwordx4 v17, s[40:43], s95 offen offset:2048 sc1 nt lds\n"
  "v_mfma_f32_16x16x32_bf16 v[68:71], a[128:131], v[136:139], v[68:71]\n"
  "v_max3_f32 v58, v72, v73, v74\n"
  "v_cvt_pk_bf16_f32 v136, v226, v230\n"
  "v_mfma_f32_16x16x32_bf16 v[68:71], a[132:135], v[140:143], v[68:71]\n"
  "v_max3_f32 v59, v59, v82, v83\n"
  "v_max3_f32 v58, v58, v75, v79\n"
  "v_mfma_f32_16x16x32_bf16 v[64:67], a[116:119], v[140:143], v[64:67]\n"
  "v_cvt_pk_bf16_f32 v140, v227, v231\n"
  "v_cvt_pk_bf16_f32 v134, v241, v245\n"
  "v_mfma_f32_16x16x32_bf16 v[232:235], v[104:107], a[92:95], v[232:235]\n"
  "v_cvt_pk_bf16_f32 v143, v251, v255\n"
  "v_cvt_pk_bf16_f32 v138, v242, v246\n"
  "Lh2_%=:\n"
  "s_cbranch_execz Lh3_%=\n"
  "v_mfma_f32_16x16x32_bf16 a[4:7], v[100:103], a[96:99], a[4:7]\n"
  "buffer_load_dwordx4 v18, s[40:43], s95 offen offset:3072 sc1 nt lds\n"
  "s_add_u32 m0, s76, 0x14000\n"
  "v_mfma_f32_16x16x32_bf16 a[56:59], v[120:123], a[100:103], a[56:59]\n"
  "v_max3_f32 v56, v64, v65, v66\n"
  "v_max3_f32 v57, v68, v69, v70\n"
  "v_mfma_f32_16x16x32_bf16 a[28:31], v[124:127], a[96:99], a[28:31]\n"
  "v_max3_f32 v56, v56, v67, v71\n"
  "v_max3_f32 v57, v57, v80, v81\n"
  "v_mfma_f32_16x16x32_bf16 v[196:199], v[100:103], a[88:91], v[196:199]\n"
  "v_max3_f32 v56, v56, v57, v58\n"
  "v_cvt_pk_bf16_f32 v129, v232, v236\n"
  "v_mfma_f32_16x16x32_bf16 a[36:39], v[100:103], a[100:103], a[36:39]\n"
  "v_max_f32 v56, v56, v59\n"
  "v_cvt_pk_bf16_f32 v137, v234, v238\n"
  "v_mfma_f32_16x16x32_bf16 a[60:63], v[124:127], a[100:103], a[60:63]\n"
  "v_mov_b32 v57, v56\n"
  "v_cvt_pk_bf16_f32 v133, v233, v237\n"
  "v_mfma_f32_16x16x32_bf16 a[12:15], v[108:111], a[96:99], a[12:15]\n"
  "v_permlane16_swap_b32 v56, v57\n"
  "v_mfma_f32_16x16x32_bf16 a[44:47], v[108:111], a[100:103], a[44:47]\n"
  "v_max_f32 v56, v56, v57\n"
  "v_cvt_pk_bf16_f32 v144, v192, v196\n"
  "v_mfma_f32_16x16x32_bf16 a[8:11], v[104:107], a[96:99], a[8:11]\n"
  "v_mov_b32 v57, v56\n"
  "v_cvt_pk_bf16_f32 v152, v194, v198\n"
  "v_mfma_f32_16x16x32_bf16 v[96:99], a[104:107], v[144:147], 0\n"
  "v_permlane32_swap_b32 v56, v57\n"
  "v_mfma_f32_16x16x32_bf16 v[104:107], a[136:139], v[144:147], 0\n"
  "v_max_f32 v56, v56, v57\n"
  "v_cvt_pk_bf16_f32 v148, v193, v197\n"
  "v_mfma_f32_16x16x32_bf16 v[108:111], a[152:155], v[144:147], 0\n"
  "v_mul_f32 v57, s97, v56\n"
  "v_mul_f32 v56, s97, v56\n"
  "v_cvt_pk_bf16_f32 v132, v225, v229\n"
  "v_cvt_pk_bf16_f32 v139, v250, v254\n"
  "v_cvt_pk_bf16_f32 v141, v235, v239\n"
  "v_pk_fma_f32 v[74:75], v[74:75], s[62:63], v[56:57]\n"
  "v_pk_fma_f32 v[76:77], v[76:77], s[62:63], v[56:57]\n"
  "v_pk_fma_f32 v[64:65], v[64:65], s[62:63], v[56:57]\n"
  "v_pk_fma_f32 v[68:69], v[68:69], s[62:63], v[56:57]\n"
  "v_pk_fma_f32 v[80:81], v[80:81], s[62:63], v[56:57]\n"
  "v_pk_fma_f32 v[82:83], v[82:83], s[62:63], v[56:57]\n"
  "v_pk_fma_f32 v[66:67], v[66:67], s[62:63], v[56:57]\n"
  "v_pk_fma_f32 v[72:73], v[72:73], s[62:63], v[56:57]\n"
  "v_pk_fma_f32 v[78:79], v[78:79], s[62:63], v[56:57]\n"
  "v_pk_fma_f32 v[70:71], v[70:71], s[62:63], v[56:57]\n"
  "v_exp_f32 v66, v66\n"
  "v_mfma_f32_16x16x32_bf16 v[96:99], a[108:111], v[148:151], v[96:99]\n"
  "v_exp_f32 v64, v64\n"
  "v_mfma_f32_16x16x32_bf16 v[96:99], a[112:115], v[152:155], v[96:99]\n"
  "v_exp_f32 v77, v77\n"
  "v_mfma_f32_16x16x32_bf16 v[108:111], a[156:159], v[148:151], v[108:111]\n"
  "v_exp_f32 v69, v69\n"
  "v_mfma_f32_16x16x32_bf16 v[100:103], a[120:123], v[144:147], 0\n"
  "v_exp_f32 v82, v82\n"
  "v_mfma_f32_16x16x32_bf16 v[100:103], a[124:127], v[148:151], v[100:103]\n"
  "v_exp_f32 v81, v81\n"
  "v_mfma_f32_16x16x32_bf16 v[100:103], a[128:131], v[152:155], v[100:103]\n"
  "v_exp_f32 v70, v70\n"
  "v_mfma_f32_16x16x32_bf16 v[104:107], a[140:143], v[148:151], v[104:107]\n"
  "v_exp_f32 v65, v65\n"
  "v_mfma_f32_16x16x32_bf16 v[104:107], a[144:147], v[152:155], v[104:107]\n"
  "v_exp_f32 v71, v71\n"
  "v_mfma_f32_16x16x32_bf16 a[16:19], v[112:115], a[96:99], a[16:19]\n"
  "v_exp_f32 v80, v80\n"
  "v_mfma_f32_16x16x32_bf16 v[112:115], a[168:171], v[144:147], 0\n"
  "v_exp_f32 v75, v75\n"
  "v_mfma_f32_16x16x32_bf16 v[112:115], a[172:175], v[148:151], v[112:115]\n"
  "v_exp_f32 v73, v73\n"
  "v_mfma_f32_16x16x32_bf16 v[112:115], a[176:179], v[152:155], v[112:115]\n"
  "v_exp_f32 v83, v83\n"
  "v_mfma_f32_16x16x32_bf16 v[108:111], a[160:163], v[152:155], v[108:111]\n"
  "v_exp_f32 v68, v68\n"
  "v_exp_f32 v78, v78\n"
  "v_exp_f32 v76, v76\n"
  "Lh3_%=:\n"
  "s_cbranch_execz Lh4_%=\n"
  "v_pk_add_f32 v[56:57], v[68:69], v[70:71]\n"
  "v_exp_f32 v67, v67\n"
  "v_exp_f32 v74, v74\n"
  "v_pk_add_f32 v[58:59], v[64:65], v[66:67]\n"
  "v_pk_add_f32 v[56:57], v[56:57], v[74:75]\n"
  "v_exp_f32 v72, v72\n"
  "v_exp_f32 v79, v79\n"
  "v_pk_add_f32 v[58:59], v[58:59], v[72:73]\n"
  "v_pk_add_f32 v[56:57], v[56:57], v[78:79]\n"
  "v_pk_add_f32 v[58:59], v[58:59], v[76:77]\n"
  "v_pk_add_f32 v[56:57], v[56:57], v[82:83]\n"
  "v_pk_add_f32 v[58:59], v[58:59], v[80:81]\n"
  "v_cvt_pk_bf16_f32 v135, v249, v253\n"
  "v_pk_add_f32 v[58:59], v[58:59], v[56:57]\n"
  "v_cvt_pk_bf16_f32 v142, v243, v247\n"
  "v_add_f32 v57, v58, v59\n"
  "v_cvt_pk_bf16_f32 v156, v195, v199\n"
  "v_mov_b32 v58, v57\n"
  "v_accvgpr_read_b32 v50, a8\n"
  "v_mfma_f32_16x16x32_bf16 v[112:115], a[180:183], v[156:159], v[112:115]\n"
  "v_permlane16_swap_b32 v57, v58\n"
  "v_mfma_f32_16x16x32_bf16 v[96:99], a[116:119], v[156:159], v[96:99]\n"
  "v_add_f32 v57, v57, v58\n"
  "v_accvgpr_read_b32 v55, a28\n"
  "v_mfma_f32_16x16x32_bf16 v[104:107], a[148:151], v[156:159], v[104:107]\n"
  "v_mov_b32 v58, v57\n"
  "v_cvt_pk_bf16_f32 v147, v54, v55\n"
  "v_mfma_f32_16x16x32_bf16 v[100:103], a[132:135], v[156:159], v[100:103]\n"
  "v_permlane32_swap_b32 v57, v58\n"
  "v_mfma_f32_16x16x32_bf16 v[108:111], a[164:167], v[156:159], v[108:111]\n"
  "v_add_f32 v57, v57, v58\n"
  "v_max3_f32 v60, v96, v97, v98\n"
  "v_rcp_f32 v57, v57\n"
  "v_max3_f32 v62, v104, v105, v106\n"
  "v_mov_b32 v56, v57\n"
  "v_accvgpr_read_b32 v55, a29\n"
  "v_pk_mul_f32 v[82:83], v[82:83], v[56:57]\n"
  "v_pk_mul_f32 v[70:71], v[70:71], v[56:57]\n"
  "v_pk_mul_f32 v[72:73], v[72:73], v[56:57]\n"
  "v_pk_mul_f32 v[64:65], v[64:65], v[56:57]\n"
  "v_pk_mul_f32 v[74:75], v[74:75], v[56:57]\n"
  "v_pk_mul_f32 v[76:77], v[76:77], v[56:57]\n"
  "v_pk_mul_f32 v[80:81], v[80:81], v[56:57]\n"
  "v_pk_mul_f32 v[66:67], v[66:67], v[56:57]\n"
  "v_pk_mul_f32 v[68:69], v[68:69], v[56:57]\n"
  "v_pk_mul_f32 v[78:79], v[78:79], v[56:57]\n"
  "v_cvt_pk_bf16_f32 v120, v72, v73\n"
  "v_cvt_pk_bf16_f32 v123, v78, v79\n"
  "v_cvt_pk_bf16_f32 v125, v82, v83\n"
  "v_cvt_pk_bf16_f32 v121, v74, v75\n"
  "v_mfma_f32_16x16x32_bf16 v[72:75], a[136:139], v[128:131], 0\n"
  "v_max3_f32 v61, v100, v101, v102\n"
  "v_cvt_pk_bf16_f32 v124, v80, v81\n"
  "v_mfma_f32_16x16x32_bf16 v[80:83], a[168:171], v[128:131], 0\n"
  "v_cvt_pk_bf16_f32 v116, v64, v65\n"
  "v_max3_f32 v62, v62, v107, v111\n"
  "v_mfma_f32_16x16x32_bf16 v[80:83], a[172:175], v[132:135], v[80:83]\n"
  "v_cvt_pk_bf16_f32 v119, v70, v71\n"
  "v_cvt_pk_bf16_f32 v122, v76, v77\n"
  "v_mfma_f32_16x16x32_bf16 v[72:75], a[140:143], v[132:135], v[72:75]\n"
  "v_cvt_pk_bf16_f32 v118, v68, v69\n"
  "v_cvt_pk_bf16_f32 v117, v66, v67\n"
  "v_mfma_f32_16x16x32_bf16 v[68:71], a[120:123], v[128:131], 0\n"
  "v_max3_f32 v60, v60, v99, v103\n"
  "v_max3_f32 v63, v108, v109, v110\n"
  "v_mfma_f32_16x16x32_bf16 v[64:67], a[104:107], v[128:131], 0\n"
  "v_max3_f32 v63, v63, v114, v115\n"
  "v_max3_f32 v61, v61, v112, v113\n"
  "v_mfma_f32_16x16x32_bf16 v[76:79], a[152:155], v[128:131], 0\n"
  "v_max3_f32 v60, v60, v61, v62\n"
  "Lh4_%=:\n"
  "s_cbranch_execz Lh5_%=\n"
  "v_accvgpr_read_b32 v54, a25\n"
  "v_mfma_f32_16x16x32_bf16 v[76:79], a[156:159], v[132:135], v[76:79]\n"
  "v_max_f32 v60, v60, v63\n"
  "v_accvgpr_read_b32 v51, a12\n"
  "v_mfma_f32_16x16x32_bf16 v[76:79], a[160:163], v[136:139], v[76:79]\n"
  "v_mov_b32 v61, v60\n"
  "v_accvgpr_read_b32 v49, a4\n"
  "v_mfma_f32_16x16x32_bf16 v[72:75], a[144:147], v[136:139], v[72:75]\n"
  "v_permlane16_swap_b32 v60, v61\n"
  "v_mfma_f32_16x16x32_bf16 v[80:83], a[176:179], v[136:139], v[80:83]\n"
  "v_max_f32 v60, v60, v61\n"
  "v_cvt_pk_bf16_f32 v144, v48, v49\n"
  "v_mfma_f32_16x16x32_bf16 v[68:71], a[124:127], v[132:135], v[68:71]\n"
  "v_mov_b32 v61, v60\n"
  "v_accvgpr_read_b32 v49, a5\n"
  "v_mfma_f32_16x16x32_bf16 v[68:71], a[128:131], v[136:139], v[68:71]\n"
  "v_permlane32_swap_b32 v60, v61\n"
  "v_mfma_f32_16x16x32_bf16 v[68:71], a[132:135], v[140:143], v[68:71]\n"
  "v_max_f32 v60, v60, v61\n"
  "v_accvgpr_read_b32 v48, a1\n"
  "v_mfma_f32_16x16x32_bf16 v[76:79], a[164:167], v[140:143], v[76:79]\n"
  "v_mul_f32 v61, s97, v60\n"
  "v_mul_f32 v60, s97, v60\n"
  "v_mfma_f32_16x16x32_bf16 v[64:67], a[108:111], v[132:135], v[64:67]\n"
  "v_cvt_pk_bf16_f32 v148, v48, v49\n"
  "v_accvgpr_read_b32 v48, a2\n"
  "v_mfma_f32_16x16x32_bf16 v[64:67], a[112:115], v[136:139], v[64:67]\n"
  "v_max3_f32 v57, v68, v69, v70\n"
  "v_accvgpr_read_b32 v49, a6\n"
  "v_mfma_f32_16x16x32_bf16 v[64:67], a[116:119], v[140:143], v[64:67]\n"
  "v_max3_f32 v59, v76, v77, v78\n"
  "v_cvt_pk_bf16_f32 v152, v48, v49\n"
  "v_mfma_f32_16x16x32_bf16 v[80:83], a[180:183], v[140:143], v[80:83]\n"
  "v_accvgpr_read_b32 v48, a3\n"
  "v_accvgpr_read_b32 v52, a16\n"
  "v_accvgpr_read_b32 v49, a7\n"
  "v_cvt_pk_bf16_f32 v146, v52, v53\n"
  "v_accvgpr_read_b32 v52, a17\n"
  "v_pk_fma_f32 v[96:97], v[96:97], s[62:63], v[60:61]\n"
  "v_pk_fma_f32 v[98:99], v[98:99], s[62:63], v[60:61]\n"
  "v_pk_fma_f32 v[112:113], v[112:113], s[62:63], v[60:61]\n"
  "v_pk_fma_f32 v[114:115], v[114:115], s[62:63], v[60:61]\n"
  "v_pk_fma_f32 v[110:111], v[110:111], s[62:63], v[60:61]\n"
  "v_pk_fma_f32 v[102:103], v[102:103], s[62:63], v[60:61]\n"
  "v_pk_fma_f32 v[100:101], v[100:101], s[62:63], v[60:61]\n"
  "v_pk_fma_f32 v[108:109], v[108:109], s[62:63], v[60:61]\n"
  "v_pk_fma_f32 v[104:105], v[104:105], s[62:63], v[60:61]\n"
  "v_pk_fma_f32 v[106:107], v[106:107], s[62:63], v[60:61]\n"
  "v_max3_f32 v56, v64, v65, v66\n"
  "v_mfma_f32_16x16x32_bf16 v[72:75], a[148:151], v[140:143], v[72:75]\n"
  "v_exp_f32 v101, v101\n"
  "v_exp_f32 v98, v98\n"
  "v_exp_f32 v100, v100\n"
  "v_max3_f32 v57, v57, v80, v81\n"
  "v_exp_f32 v112, v112\n"
  "v_exp_f32 v105, v105\n"
  "v_exp_f32 v99, v99\n"
  "v_exp_f32 v107, v107\n"
  "v_exp_f32 v97, v97\n"
  "v_exp_f32 v106, v106\n"
  "v_max3_f32 v59, v59, v82, v83\n"
  "v_exp_f32 v110, v110\n"
  "v_max3_f32 v58, v72, v73, v74\n"
  "v_exp_f32 v114, v114\n"
  "v_max3_f32 v58, v58, v75, v79\n"
  "v_max3_f32 v56, v56, v67, v71\n"
  "v_exp_f32 v96, v96\n"
  "v_max3_f32 v56, v56, v57, v58\n"
  "v_pk_add_f32 v[62:63], v[96:97], v[98:99]\n"
  "v_max_f32 v56, v56, v59\n"
  "v_exp_f32 v108, v108\n"
  "v_mov_b32 v57, v56\n"
  "v_exp_f32 v104, v104\n"
  "v_exp_f32 v102, v102\n"
  "v_pk_add_f32 v[62:63], v[62:63], v[104:105]\n"
  "v_permlane16_swap_b32 v56, v57\n"
  "v_exp_f32 v115, v115\n"
  "v_max_f32 v56, v56, v57\n"
  "Lh5_%=:\n"
  "s_cbranch_execz Lh6_%=\n"
  "v_exp_f32 v111, v111\n"
  "v_mov_b32 v57, v56\n"
  "v_exp_f32 v109, v109\n"
  "v_exp_f32 v113, v113\n"
  "v_pk_add_f32 v[62:63], v[62:63], v[108:109]\n"
  "v_permlane32_swap_b32 v56, v57\n"
  "v_pk_add_f32 v[62:63], v[62:63], v[112:113]\n"
  "v_max_f32 v56, v56, v57\n"
  "v_exp_f32 v103, v103\n"
  "v_mul_f32 v57, s97, v56\n"
  "v_pk_add_f32 v[60:61], v[100:101], v[102:103]\n"
  "v_mul_f32 v56, s97, v56\n"
  "v_pk_add_f32 v[60:61], v[60:61], v[106:107]\n"
  "v_pk_fma_f32 v[66:67], v[66:67], s[62:63], v[56:57]\n"
  "v_pk_fma_f32 v[82:83], v[82:83], s[62:63], v[56:57]\n"
  "v_pk_add_f32 v[60:61], v[60:61], v[110:111]\n"
  "v_pk_fma_f32 v[72:73], v[72:73], s[62:63], v[56:57]\n"
  "v_pk_fma_f32 v[70:71], v[70:71], s[62:63], v[56:57]\n"
  "v_pk_fma_f32 v[78:79], v[78:79], s[62:63], v[56:57]\n"
  "v_pk_fma_f32 v[64:65], v[64:65], s[62:63], v[56:57]\n"
  "v_pk_add_f32 v[60:61], v[60:61], v[114:115]\n"
  "v_pk_fma_f32 v[74:75], v[74:75], s[62:63], v[56:57]\n"
  "v_pk_fma_f32 v[80:81], v[80:81], s[62:63], v[56:57]\n"
  "v_pk_fma_f32 v[68:69], v[68:69], s[62:63], v[56:57]\n"
  "v_pk_fma_f32 v[76:77], v[76:77], s[62:63], v[56:57]\n"
  "v_pk_add_f32 v[62:63], v[62:63], v[60:61]\n"
  "v_exp_f32 v83, v83\n"
  "v_exp_f32 v67, v67\n"
  "v_exp_f32 v68, v68\n"
  "v_exp_f32 v69, v69\n"
  "v_exp_f32 v82, v82\n"
  "v_exp_f32 v66, v66\n"
  "v_exp_f32 v71, v71\n"
  "v_exp_f32 v64, v64\n"
  "v_exp_f32 v65, v65\n"
  "v_exp_f32 v79, v79\n"
  "v_pk_add_f32 v[58:59], v[64:65], v[66:67]\n"
  "v_exp_f32 v78, v78\n"
  "v_exp_f32 v74, v74\n"
  "v_exp_f32 v72, v72\n"
  "v_exp_f32 v76, v76\n"
  "v_exp_f32 v81, v81\n"
  "v_exp_f32 v80, v80\n"
  "v_exp_f32 v75, v75\n"
  "v_exp_f32 v77, v77\n"
  "v_exp_f32 v73, v73\n"
  "v_exp_f32 v70, v70\n"
  "v_pk_add_f32 v[58:59], v[58:59], v[72:73]\n"
  "v_pk_add_f32 v[56:57], v[68:69], v[70:71]\n"
  "v_pk_add_f32 v[58:59], v[58:59], v[76:77]\n"
  "v_pk_add_f32 v[56:57], v[56:57], v[74:75]\n"
  "v_pk_add_f32 v[58:59], v[58:59], v[80:81]\n"
  "v_pk_add_f32 v[56:57], v[56:57], v[78:79]\n"
  "v_add_f32 v61, v62, v63\n"
  "v_pk_add_f32 v[56:57], v[56:57], v[82:83]\n"
  "v_mov_b32 v62, v61\n"
  "v_pk_add_f32 v[58:59], v[58:59], v[56:57]\n"
  "v_cvt_pk_bf16_f32 v145, v50, v51\n"
  "v_add_f32 v57, v58, v59\n"
  "v_accvgpr_read_b32 v51, a13\n"
  "v_accvgpr_read_b32 v50, a9\n"
  "v_mov_b32 v58, v57\n"
  "v_cvt_pk_bf16_f32 v149, v50, v51\n"
  "v_accvgpr_read_b32 v50, a10\n"
  "v_permlane16_swap_b32 v57, v58\n"
  "v_accvgpr_read_b32 v51, a14\n"
  "v_add_f32 v57, v57, v58\n"
  "v_cvt_pk_bf16_f32 v153, v50, v51\n"
  "v_mov_b32 v58, v57\n"
  "v_permlane16_swap_b32 v61, v62\n"
  "v_accvgpr_read_b32 v53, a21\n"
  "v_permlane32_swap_b32 v57, v58\n"
  "v_add_f32 v61, v61, v62\n"
  "v_add_f32 v57, v57, v58\n"
  "v_mov_b32 v62, v61\n"
  "v_rcp_f32 v57, v57\n"
  "v_cvt_pk_bf16_f32 v151, v54, v55\n"
  "v_permlane32_swap_b32 v61, v62\n"
  "v_mov_b32 v56, v57\n"
  "v_add_f32 v61, v61, v62\n"
  "v_pk_mul_f32 v[72:73], v[72:73], v[56:57]\n"
  "v_pk_mul_f32 v[82:83], v[82:83], v[56:57]\n"
  "v_pk_mul_f32 v[78:79], v[78:79], v[56:57]\n"
  "v_pk_mul_f32 v[76:77], v[76:77], v[56:57]\n"
  "v_pk_mul_f32 v[80:81], v[80:81], v[56:57]\n"
  "v_pk_mul_f32 v[74:75], v[74:75], v[56:57]\n"
  "Lh6_%=:\n"
  "s_cbranch_execz Lh7_%=\n"
  "v_pk_mul_f32 v[68:69], v[68:69], v[56:57]\n"
  "v_pk_mul_f32 v[66:67], v[66:67], v[56:57]\n"
  "v_pk_mul_f32 v[70:71], v[70:71], v[56:57]\n"
  "v_pk_mul_f32 v[64:65], v[64:65], v[56:57]\n"
  "v_rcp_f32 v61, v61\n"
  "v_accvgpr_read_b32 v55, a30\n"
  "v_mov_b32 v60, v61\n"
  "v_cvt_pk_bf16_f32 v162, v68, v69\n"
  "v_pk_mul_f32 v[104:105], v[104:105], v[60:61]\n"
  "v_pk_mul_f32 v[114:115], v[114:115], v[60:61]\n"
  "v_pk_mul_f32 v[98:99], v[98:99], v[60:61]\n"
  "v_pk_mul_f32 v[102:103], v[102:103], v[60:61]\n"
  "v_pk_mul_f32 v[100:101], v[100:101], v[60:61]\n"
  "v_pk_mul_f32 v[112:113], v[112:113], v[60:61]\n"
  "v_pk_mul_f32 v[106:107], v[106:107], v[60:61]\n"
  "v_pk_mul_f32 v[110:111], v[110:111], v[60:61]\n"
  "v_pk_mul_f32 v[96:97], v[96:97], v[60:61]\n"
  "v_pk_mul_f32 v[108:109], v[108:109], v[60:61]\n"
  "v_cvt_pk_bf16_f32 v84, v96, v97\n"
  "v_cvt_pk_bf16_f32 v93, v114, v115\n"
  "v_accvgpr_read_b32 v54, a26\n"
  "v_cvt_pk_bf16_f32 v86, v100, v101\n"
  "v_cvt_pk_bf16_f32 v91, v110, v111\n"
  "v_cvt_pk_bf16_f32 v163, v70, v71\n"
  "v_cvt_pk_bf16_f32 v161, v66, v67\n"
  "v_cvt_pk_bf16_f32 v89, v106, v107\n"
  "v_cvt_pk_bf16_f32 v90, v108, v109\n"
  "v_mfma_f32_16x16x32_bf16 v[108:111], a[152:155], v[144:147], 0\n"
  "v_cvt_pk_bf16_f32 v92, v112, v113\n"
  "v_cvt_pk_bf16_f32 v150, v52, v53\n"
  "v_mfma_f32_16x16x32_bf16 v[112:115], a[168:171], v[144:147], 0\n"
  "v_accvgpr_read_b32 v53, a22\n"
  "v_accvgpr_read_b32 v51, a15\n"
  "v_mfma_f32_16x16x32_bf16 v[112:115], a[172:175], v[148:151], v[112:115]\n"
  "v_cvt_pk_bf16_f32 v156, v48, v49\n"
  "v_accvgpr_read_b32 v48, a32\n"
  "v_mfma_f32_16x16x32_bf16 v[108:111], a[156:159], v[148:151], v[108:111]\n"
  "v_accvgpr_read_b32 v49, a36\n"
  "v_accvgpr_read_b32 v52, a18\n"
  "v_cvt_pk_bf16_f32 v128, v48, v49\n"
  "v_cvt_pk_bf16_f32 v154, v52, v53\n"
  "v_accvgpr_read_b32 v48, a33\n"
  "v_accvgpr_read_b32 v49, a37\n"
  "v_accvgpr_read_b32 v52, a19\n"
  "v_cvt_pk_bf16_f32 v132, v48, v49\n"
  "v_accvgpr_read_b32 v49, a38\n"
  "v_cvt_pk_bf16_f32 v169, v82, v83\n"
  "v_cvt_pk_bf16_f32 v87, v102, v103\n"
  "v_mfma_f32_16x16x32_bf16 v[100:103], a[120:123], v[144:147], 0\n"
  "v_cvt_pk_bf16_f32 v155, v54, v55\n"
  "v_accvgpr_read_b32 v54, a27\n"
  "v_mfma_f32_16x16x32_bf16 v[100:103], a[124:127], v[148:151], v[100:103]\n"
  "v_accvgpr_read_b32 v55, a31\n"
  "v_accvgpr_read_b32 v53, a23\n"
  "v_mfma_f32_16x16x32_bf16 v[112:115], a[176:179], v[152:155], v[112:115]\n"
  "v_cvt_pk_bf16_f32 v158, v52, v53\n"
  "v_accvgpr_read_b32 v52, a48\n"
  "v_mfma_f32_16x16x32_bf16 v[100:103], a[128:131], v[152:155], v[100:103]\n"
  "v_accvgpr_read_b32 v53, a52\n"
  "v_cvt_pk_bf16_f32 v88, v104, v105\n"
  "v_mfma_f32_16x16x32_bf16 v[104:107], a[136:139], v[144:147], 0\n"
  "v_cvt_pk_bf16_f32 v130, v52, v53\n"
  "v_accvgpr_read_b32 v52, a49\n"
  "v_mfma_f32_16x16x32_bf16 v[104:107], a[140:143], v[148:151], v[104:107]\n"
  "v_accvgpr_read_b32 v53, a53\n"
  "Lh7_%=:\n"
  "s_cbranch_execz Lh8_%=\n"
  "v_cvt_pk_bf16_f32 v85, v98, v99\n"
  "v_mfma_f32_16x16x32_bf16 v[96:99], a[104:107], v[144:147], 0\n"
  "v_cvt_pk_bf16_f32 v134, v52, v53\n"
  "v_accvgpr_read_b32 v52, a50\n"
  "v_mfma_f32_16x16x32_bf16 v[104:107], a[144:147], v[152:155], v[104:107]\n"
  "v_accvgpr_read_b32 v53, a54\n"
  "v_accvgpr_read_b32 v50, a11\n"
  "v_mfma_f32_16x16x32_bf16 v[96:99], a[108:111], v[148:151], v[96:99]\n"
  "v_cvt_pk_bf16_f32 v157, v50, v51\n"
  "v_accvgpr_read_b32 v51, a44\n"
  "v_mfma_f32_16x16x32_bf16 v[96:99], a[112:115], v[152:155], v[96:99]\n"
  "v_accvgpr_read_b32 v50, a40\n"
  "v_cvt_pk_bf16_f32 v138, v52, v53\n"
  "v_mfma_f32_16x16x32_bf16 v[108:111], a[160:163], v[152:155], v[108:111]\n"
  "v_accvgpr_read_b32 v52, a51\n"
  "buffer_load_dwordx4 v22, s[48:51], s74 offen lds\n"
  "v_cvt_pk_bf16_f32 v129, v50, v51\n"
  "v_accvgpr_read_b32 v50, a41\n"
  "v_accvgpr_read_b32 v48, a34\n"
  "v_cvt_pk_bf16_f32 v166, v76, v77\n"
  "v_cvt_pk_bf16_f32 v136, v48, v49\n"
  "v_accvgpr_read_b32 v49, a39\n"
  "v_accvgpr_read_b32 v48, a35\n"
  "v_cvt_pk_bf16_f32 v168, v80, v81\n"
  "v_cvt_pk_bf16_f32 v159, v54, v55\n"
  "v_accvgpr_read_b32 v55, a60\n"
  "v_accvgpr_read_b32 v54, a56\n"
  "v_mfma_f32_16x16x32_bf16 v[108:111], a[164:167], v[156:159], v[108:111]\n"
  "v_cvt_pk_bf16_f32 v131, v54, v55\n"
  "v_accvgpr_read_b32 v54, a57\n"
  "v_mfma_f32_16x16x32_bf16 v[96:99], a[116:119], v[156:159], v[96:99]\n"
  "v_accvgpr_read_b32 v55, a61\n"
  "v_cvt_pk_bf16_f32 v167, v78, v79\n"
  "v_mfma_f32_16x16x32_bf16 v[76:79], a[152:155], v[128:131], 0\n"
  "v_cvt_pk_bf16_f32 v135, v54, v55\n"
  "v_accvgpr_read_b32 v54, a58\n"
  "v_mfma_f32_16x16x32_bf16 v[68:71], a[120:123], v[128:131], 0\n"
  "v_accvgpr_read_b32 v51, a45\n"
  "v_accvgpr_read_b32 v55, a62\n"
  "v_mfma_f32_16x16x32_bf16 v[100:103], a[132:135], v[156:159], v[100:103]\n"
  "v_cvt_pk_bf16_f32 v133, v50, v51\n"
  "v_accvgpr_read_b32 v51, a46\n"
  "v_mfma_f32_16x16x32_bf16 v[112:115], a[180:183], v[156:159], v[112:115]\n"
  "v_max3_f32 v60, v96, v97, v98\n"
  "v_cvt_pk_bf16_f32 v139, v54, v55\n"
  "v_mfma_f32_16x16x32_bf16 v[76:79], a[156:159], v[132:135], v[76:79]\n"
  "buffer_load_dwordx4 v23, s[48:51], s74 offen offset:1024 lds\n"
  "v_mfma_f32_16x16x32_bf16 v[80:83], a[168:171], v[128:131], 0\n"
  "v_accvgpr_read_b32 v55, a63\n"
  "v_accvgpr_read_b32 v50, a42\n"
  "v_mfma_f32_16x16x32_bf16 v[80:83], a[172:175], v[132:135], v[80:83]\n"
  "v_cvt_pk_bf16_f32 v137, v50, v51\n"
  "v_accvgpr_read_b32 v50, a43\n"
  "v_mfma_f32_16x16x32_bf16 v[68:71], a[124:127], v[132:135], v[68:71]\n"
  "v_accvgpr_read_b32 v51, a47\n"
  "v_cvt_pk_bf16_f32 v160, v64, v65\n"
  "v_mfma_f32_16x16x32_bf16 v[64:67], a[104:107], v[128:131], 0\n"
  "v_cvt_pk_bf16_f32 v141, v50, v51\n"
  "v_cvt_pk_bf16_f32 v164, v72, v73\n"
  "v_mfma_f32_16x16x32_bf16 v[64:67], a[108:111], v[132:135], v[64:67]\n"
  "v_cvt_pk_bf16_f32 v165, v74, v75\n"
  "v_accvgpr_read_b32 v53, a55\n"
  "v_mfma_f32_16x16x32_bf16 v[68:71], a[128:131], v[136:139], v[68:71]\n"
  "Lh8_%=:\n"
  "s_cbranch_execz Lh9_%=\n"
  "v_cvt_pk_bf16_f32 v142, v52, v53\n"
  "v_accvgpr_read_b32 v54, a59\n"
  "v_mfma_f32_16x16x32_bf16 v[64:67], a[112:115], v[136:139], v[64:67]\n"
  "v_cvt_pk_bf16_f32 v143, v54, v55\n"
  "v_cvt_pk_bf16_f32 v140, v48, v49\n"
  "v_mfma_f32_16x16x32_bf16 v[72:75], a[136:139], v[128:131], 0\n"
  "s_xor_b32 s13, s13, s64\n"
  "s_mul_i32 s13, s13, 0x5000\n"
  "v_mfma_f32_16x16x32_bf16 v[68:71], a[132:135], v[140:143], v[68:71]\n"
  "buffer_load_dwordx4 v24, s[48:51], s74 offen offset:2048 lds\n"
  "v_mfma_f32_16x16x32_bf16 v[64:67], a[116:119], v[140:143], v[64:67]\n"
  "v_max3_f32 v63, v108, v109, v110\n"
  "v_max3_f32 v60, v60, v99, v103\n"
  "v_mfma_f32_16x16x32_bf16 v[72:75], a[140:143], v[132:135], v[72:75]\n"
  "v_max3_f32 v63, v63, v114, v115\n"
  "v_max3_f32 v61, v100, v101, v102\n"
  "v_mfma_f32_16x16x32_bf16 v[72:75], a[144:147], v[136:139], v[72:75]\n"
  "v_max3_f32 v61, v61, v112, v113\n"
  "v_max3_f32 v57, v68, v69, v70\n"
  "v_mfma_f32_16x16x32_bf16 v[72:75], a[148:151], v[140:143], v[72:75]\n"
  "v_max3_f32 v56, v64, v65, v66\n"
  "v_add_u32 v21, s13, v1\n"
  "v_mfma_f32_16x16x32_bf16 v[80:83], a[176:179], v[136:139], v[80:83]\n"
  "v_max3_f32 v56, v56, v67, v71\n"
  "v_lshlrev_b32 v3, 22, v1\n"
  "v_mfma_f32_16x16x32_bf16 v[80:83], a[180:183], v[140:143], v[80:83]\n"
  "v_and_b32 v3, 0x80000000, v3\n"
  "v_mfma_f32_16x16x32_bf16 v[104:107], a[148:151], v[156:159], v[104:107]\n"
  "v_max3_f32 v58, v72, v73, v74\n"
  "v_or_b32 v3, v3, v35\n"
  "v_mfma_f32_16x16x32_bf16 v[76:79], a[160:163], v[136:139], v[76:79]\n"
  "v_mfma_f32_16x16x32_bf16 v[76:79], a[164:167], v[140:143], v[76:79]\n"
  "buffer_load_dwordx4 v25, s[48:51], s74 offen offset:3072 lds\n"
  "s_add_u32 m0, s76, 0x18000\n"
  "s_xor_b32 s74, s79, 0x10000\n"
  "v_max3_f32 v57, v57, v80, v81\n"
  "v_max3_f32 v62, v104, v105, v106\n"
  "v_max3_f32 v62, v62, v107, v111\n"
  "v_max3_f32 v60, v60, v61, v62\n"
  "s_nop 0\n"
  "v_max3_f32 v59, v76, v77, v78\n"
  "v_max3_f32 v58, v58, v75, v79\n"
  "v_max3_f32 v59, v59, v82, v83\n"
  "v_max3_f32 v56, v56, v57, v58\n"
  "v_max_f32 v60, v60, v63\n"
  "v_max_f32 v56, v56, v59\n"
  "v_mov_b32 v61, v60\n"
  "v_mov_b32 v57, v56\n"
  "s_nop 0\n"
  "v_permlane16_swap_b32 v60, v61\n"
  "v_permlane16_swap_b32 v56, v57\n"
  "v_max_f32 v60, v60, v61\n"
  "v_max_f32 v56, v56, v57\n"
  "v_mov_b32 v61, v60\n"
  "v_mov_b32 v57, v56\n"
  "s_nop 0\n"
  "v_permlane32_swap_b32 v60, v61\n"
  "v_permlane32_swap_b32 v56, v57\n"
  "v_max_f32 v60, v60, v61\n"
  "v_max_f32 v56, v56, v57\n"
  "v_mul_f32 v61, s97, v60\n"
  "v_mul_f32 v60, s97, v60\n"
  "v_mul_f32 v57, s97, v56\n"
  "v_pk_fma_f32 v[102:103], v[102:103], s[62:63], v[60:61]\n"
  "v_pk_fma_f32 v[98:99], v[98:99], s[62:63], v[60:61]\n"
  "v_pk_fma_f32 v[108:109], v[108:109], s[62:63], v[60:61]\n"
  "v_pk_fma_f32 v[106:107], v[106:107], s[62:63], v[60:61]\n"
  "v_pk_fma_f32 v[110:111], v[110:111], s[62:63], v[60:61]\n"
  "v_pk_fma_f32 v[96:97], v[96:97], s[62:63], v[60:61]\n"
  "v_pk_fma_f32 v[100:101], v[100:101], s[62:63], v[60:61]\n"
  "v_pk_fma_f32 v[104:105], v[104:105], s[62:63], v[60:61]\n"
  "v_pk_fma_f32 v[112:113], v[112:113], s[62:63], v[60:61]\n"
  "v_pk_fma_f32 v[114:115], v[114:115], s[62:63], v[60:61]\n"
  "buffer_load_dwordx4 v22, s[48:51], s74 offen lds\n"
  "v_mul_f32 v56, s97, v56\n"
  "v_exp_f32 v97, v97\n"
  "v_pk_fma_f32 v[82:83], v[82:83], s[62:63], v[56:57]\n"
  "Lh9_%=:\n"
  "s_cbranch_execz Lret_%=\n"
  "v_pk_fma_f32 v[80:81], v[80:81], s[62:63], v[56:57]\n"
  "v_pk_fma_f32 v[70:71], v[70:71], s[62:63], v[56:57]\n"
  "v_pk_fma_f32 v[66:67], v[66:67], s[62:63], v[56:57]\n"
  "v_pk_fma_f32 v[74:75], v[74:75], s[62:63], v[56:57]\n"
  "v_pk_fma_f32 v[68:69], v[68:69], s[62:63], v[56:57]\n"
  "v_pk_fma_f32 v[78:79], v[78:79], s[62:63], v[56:57]\n"
  "v_pk_fma_f32 v[64:65], v[64:65], s[62:63], v[56:57]\n"
  "v_pk_fma_f32 v[72:73], v[72:73], s[62:63], v[56:57]\n"
  "v_pk_fma_f32 v[76:77], v[76:77], s[62:63], v[56:57]\n"
  "v_exp_f32 v64, v64\n"
  "v_exp_f32 v81, v81\n"
  "v_exp_f32 v67, v67\n"
  "v_exp_f32 v82, v82\n"
  "v_exp_f32 v112, v112\n"
  "v_exp_f32 v80, v80\n"
  "buffer_load_dwordx4 v23, s[48:51], s74 offen offset:1024 lds\n"
  "v_exp_f32 v101, v101\n"
  "v_exp_f32 v98, v98\n"
  "v_exp_f32 v74, v74\n"
  "v_exp_f32 v71, v71\n"
  "v_exp_f32 v109, v109\n"
  "v_exp_f32 v83, v83\n"
  "s_waitcnt vmcnt(6)\n"
  "ds_read_b64_tr_b16 a[246:247], v13 offset:16384\n"
  "ds_read_b64_tr_b16 a[234:235], v12\n"
  "ds_read_b64_tr_b16 a[70:71], v14 offset:49152\n"
  "ds_read_b64_tr_b16 a[202:203], v15\n"
  "ds_read_b64_tr_b16 a[194:195], v8\n"
  "ds_read_b64_tr_b16 a[192:193], v2\n"
  "ds_read_b64_tr_b16 a[248:249], v13 offset:32768\n"
  "ds_read_b64_tr_b16 a[224:225], v11\n"
  "ds_read_b64_tr_b16 a[190:191], v20 offset:49152\n"
  "ds_read_b64_tr_b16 a[186:187], v20 offset:16384\n"
  "ds_read_b64_tr_b16 a[240:241], v12 offset:49152\n"
  "ds_read_b64_tr_b16 a[212:213], v16\n"
  "buffer_load_dwordx4 v24, s[48:51], s74 offen offset:2048 lds\n"
  "ds_read_b64_tr_b16 a[198:199], v8 offset:32768\n"
  "ds_read_b64_tr_b16 a[66:67], v14 offset:16384\n"
  "ds_read_b64_tr_b16 a[184:185], v20\n"
  "ds_read_b64_tr_b16 a[206:207], v9 offset:16384\n"
  "ds_read_b64_tr_b16 a[64:65], v14\n"
  "ds_read_b64_tr_b16 a[204:205], v9\n"
  "s_waitcnt lgkmcnt(14)\n"
  "v_mfma_f32_16x16x16_bf16 v[196:199], a[202:203], v[124:125], 0\n"
  "ds_read_b64_tr_b16 a[222:223], v19\n"
  "s_waitcnt lgkmcnt(13)\n"
  "v_mfma_f32_16x16x16_bf16 v[192:195], a[192:193], v[124:125], 0\n"
  "ds_read_b64_tr_b16 a[196:197], v8 offset:16384\n"
  "v_mfma_f32_16x16x16_bf16 v[224:227], a[192:193], v[92:93], 0\n"
  "ds_read_b64_tr_b16 a[236:237], v12 offset:16384\n"
  "v_mfma_f32_16x16x16_bf16 v[228:231], a[202:203], v[92:93], 0\n"
  "ds_read_b64_tr_b16 a[226:227], v11 offset:16384\n"
  "s_waitcnt lgkmcnt(10)\n"
  "v_mfma_f32_16x16x16_bf16 v[200:203], a[212:213], v[124:125], 0\n"
  "ds_read_b64_tr_b16 a[230:231], v11 offset:49152\n"
  "v_mfma_f32_16x16x16_bf16 v[232:235], a[212:213], v[92:93], 0\n"
  "buffer_load_dwordx4 v25, s[48:51], s74 offen offset:3072 lds\n"
  "s_add_u32 m0, s76, 0x1c000\n"
  "s_xor_b32 s74, s79, 0x20000\n"
  "s_waitcnt lgkmcnt(8)\n"
  "v_mfma_f32_16x16x32_bf16 v[192:195], a[184:187], v[116:119], v[192:195]\n"
  "ds_read_b64_tr_b16 a[208:209], v9 offset:32768\n"
  "s_waitcnt lgkmcnt(6)\n"
  "v_mfma_f32_16x16x32_bf16 v[200:203], a[204:207], v[116:119], v[200:203]\n"
  "ds_read_b64_tr_b16 a[216:217], v10 offset:16384\n"
  "s_waitcnt lgkmcnt(6)\n"
  "v_mfma_f32_16x16x16_bf16 v[204:207], a[222:223], v[124:125], 0\n"
  "ds_read_b64_tr_b16 a[68:69], v14 offset:32768\n"
  "v_mfma_f32_16x16x16_bf16 v[236:239], a[222:223], v[92:93], 0\n"
  "v_add_u32 v14, 0x10000, v14\n"
  "ds_read_b64_tr_b16 a[200:201], v8 offset:49152\n"
  "ds_read_b64_tr_b16 a[72:73], v14\n"
  "s_waitcnt lgkmcnt(8)\n"
  "Lh10_%=:\n"
  "s_cbranch_execz Lh11_%=\n"
  "v_mfma_f32_16x16x32_bf16 v[196:199], a[194:197], v[116:119], v[196:199]\n"
  "ds_read_b64_tr_b16 a[228:229], v11 offset:32768\n"
  "v_mfma_f32_16x16x32_bf16 v[232:235], a[204:207], v[84:87], v[232:235]\n"
  "v_add_u32 v11, 0x10000, v11\n"
  "ds_read_b64_tr_b16 a[218:219], v10 offset:32768\n"
  "v_mfma_f32_16x16x32_bf16 v[224:227], a[184:187], v[84:87], v[224:227]\n"
  "ds_read_b64_tr_b16 a[232:233], v11\n"
  "v_mfma_f32_16x16x32_bf16 v[228:231], a[194:197], v[84:87], v[228:231]\n"
  "buffer_load_dwordx4 v22, s[48:51], s74 offen lds\n"
  "ds_read_b64_tr_b16 a[244:245], v13\n"
  "ds_read_b64_tr_b16 a[188:189], v20 offset:32768\n"
  "ds_read_b64_tr_b16 a[250:251], v13 offset:49152\n"
  "v_add_u32 v13, 0x10000, v13\n"
  "ds_read_b64_tr_b16 a[238:239], v12 offset:32768\n"
  "ds_read_b64_tr_b16 a[252:253], v13\n"
  "s_waitcnt lgkmcnt(9)\n"
  "v_mfma_f32_16x16x32_bf16 v[196:199], a[198:201], v[120:123], v[196:199]\n"
  "v_add_u32 v12, 0x10000, v12\n"
  "ds_read_b64_tr_b16 a[214:215], v10\n"
  "s_waitcnt lgkmcnt(9)\n"
  "v_mfma_f32_16x16x16_bf16 v[220:223], a[72:73], v[124:125], 0\n"
  "ds_read_b64_tr_b16 a[242:243], v12\n"
  "v_mfma_f32_16x16x16_bf16 v[252:255], a[72:73], v[92:93], 0\n"
  "ds_read_b64_tr_b16 a[210:211], v9 offset:49152\n"
  "ds_read_b64_tr_b16 a[220:221], v10 offset:49152\n"
  "s_waitcnt lgkmcnt(9)\n"
  "v_mfma_f32_16x16x16_bf16 v[208:211], a[232:233], v[124:125], 0\n"
  "v_exp_f32 v72, v72\n"
  "v_mfma_f32_16x16x16_bf16 v[240:243], a[232:233], v[92:93], 0\n"
  "v_exp_f32 v73, v73\n"
  "v_exp_f32 v107, v107\n"
  "buffer_load_dwordx4 v23, s[48:51], s74 offen offset:1024 lds\n"
  "v_mfma_f32_16x16x32_bf16 v[228:231], a[198:201], v[88:91], v[228:231]\n"
  "v_exp_f32 v79, v79\n"
  "s_waitcnt lgkmcnt(4)\n"
  "v_mfma_f32_16x16x16_bf16 v[216:219], a[252:253], v[124:125], 0\n"
  "v_exp_f32 v65, v65\n"
  "v_mfma_f32_16x16x16_bf16 v[248:251], a[252:253], v[92:93], 0\n"
  "v_exp_f32 v103, v103\n"
  "s_waitcnt lgkmcnt(2)\n"
  "v_mfma_f32_16x16x16_bf16 v[212:215], a[242:243], v[124:125], 0\n"
  "v_exp_f32 v114, v114\n"
  "v_mfma_f32_16x16x16_bf16 v[244:247], a[242:243], v[92:93], 0\n"
  "v_exp_f32 v111, v111\n"
  "v_exp_f32 v75, v75\n"
  "v_mfma_f32_16x16x32_bf16 v[204:207], a[214:217], v[116:119], v[204:207]\n"
  "v_exp_f32 v66, v66\n"
  "s_waitcnt lgkmcnt(1)\n"
  "v_mfma_f32_16x16x32_bf16 v[200:203], a[208:211], v[120:123], v[200:203]\n"
  "v_exp_f32 v76, v76\n"
  "v_mfma_f32_16x16x32_bf16 v[216:219], a[244:247], v[116:119], v[216:219]\n"
  "v_exp_f32 v102, v102\n"
  "v_mfma_f32_16x16x32_bf16 v[216:219], a[248:251], v[120:123], v[216:219]\n"
  "buffer_load_dwordx4 v24, s[48:51], s74 offen offset:2048 lds\n"
  "v_mfma_f32_16x16x32_bf16 v[208:211], a[224:227], v[116:119], v[208:211]\n"
  "v_exp_f32 v96, v96\n"
  "v_mfma_f32_16x16x32_bf16 v[212:215], a[234:237], v[116:119], v[212:215]\n"
  "v_cvt_pk_bf16_f32 v200, v200, v201\n"
  "v_exp_f32 v99, v99\n"
  "v_mfma_f32_16x16x32_bf16 v[212:215], a[238:241], v[120:123], v[212:215]\n"
  "v_exp_f32 v110, v110\n"
  "v_mfma_f32_16x16x32_bf16 v[192:195], a[188:191], v[120:123], v[192:195]\n"
  "v_cvt_pk_bf16_f32 v216, v216, v217\n"
  "v_cvt_pk_bf16_f32 v217, v218, v219\n"
  "v_exp_f32 v113, v113\n"
  "v_exp_f32 v104, v104\n"
  "v_pk_add_f32 v[58:59], v[64:65], v[66:67]\n"
  "v_pk_add_f32 v[62:63], v[96:97], v[98:99]\n"
  "v_pk_add_f32 v[58:59], v[58:59], v[72:73]\n"
  "s_waitcnt lgkmcnt(0)\n"
  "s_barrier\n"
  "v_mfma_f32_16x16x32_bf16 v[220:223], a[64:67], v[116:119], v[220:223]\n"
  "v_cvt_pk_bf16_f32 v192, v192, v193\n"
  "v_cvt_pk_bf16_f32 v193, v194, v195\n"
  "v_mfma_f32_16x16x32_bf16 v[208:211], a[228:231], v[120:123], v[208:211]\n"
  "v_cvt_pk_bf16_f32 v195, v198, v199\n"
  "Lh11_%=:\n"
  "s_cbranch_execz Lh12_%=\n"
  "v_exp_f32 v108, v108\n"
  "buffer_load_dwordx4 v25, s[48:51], s74 offen offset:3072 lds\n"
  "s_add_u32 m0, s76, 0x20000\n"
  "s_xor_b32 s74, s79, 0x30000\n"
  "v_mfma_f32_16x16x32_bf16 v[236:239], a[214:217], v[84:87], v[236:239]\n"
  "v_exp_f32 v69, v69\n"
  "v_mfma_f32_16x16x32_bf16 v[236:239], a[218:221], v[88:91], v[236:239]\n"
  "v_cvt_pk_bf16_f32 v208, v208, v209\n"
  "v_exp_f32 v100, v100\n"
  "v_exp_f32 v115, v115\n"
  "v_pk_add_f32 v[60:61], v[100:101], v[102:103]\n"
  "v_cvt_pk_bf16_f32 v194, v196, v197\n"
  "v_mfma_f32_16x16x32_bf16 v[220:223], a[68:71], v[120:123], v[220:223]\n"
  "ds_write_b128 v21, v[192:195]\n"
  "v_mfma_f32_16x16x16_bf16 v[192:195], a[192:193], v[168:169], 0\n"
  "v_exp_f32 v68, v68\n"
  "v_mfma_f32_16x16x16_bf16 v[196:199], a[202:203], v[168:169], 0\n"
  "v_cvt_pk_bf16_f32 v201, v202, v203\n"
  "v_exp_f32 v106, v106\n"
  "v_exp_f32 v105, v105\n"
  "v_pk_add_f32 v[60:61], v[60:61], v[106:107]\n"
  "v_pk_add_f32 v[62:63], v[62:63], v[104:105]\n"
  "v_pk_add_f32 v[60:61], v[60:61], v[110:111]\n"
  "v_pk_add_f32 v[62:63], v[62:63], v[108:109]\n"
  "v_pk_add_f32 v[60:61], v[60:61], v[114:115]\n"
  "v_pk_add_f32 v[62:63], v[62:63], v[112:113]\n"
  "buffer_load_dwordx4 v22, s[48:51], s74 offen lds\n"
  "v_pk_add_f32 v[62:63], v[62:63], v[60:61]\n"
  "v_cvt_pk_bf16_f32 v218, v220, v221\n"
  "v_mfma_f32_16x16x32_bf16 v[196:199], a[194:197], v[160:163], v[196:199]\n"
  "v_cvt_pk_bf16_f32 v219, v222, v223\n"
  "v_exp_f32 v70, v70\n"
  "v_cvt_pk_bf16_f32 v209, v210, v211\n"
  "v_cvt_pk_bf16_f32 v210, v212, v213\n"
  "v_pk_add_f32 v[56:57], v[68:69], v[70:71]\n"
  "v_cvt_pk_bf16_f32 v211, v214, v215\n"
  "v_pk_add_f32 v[56:57], v[56:57], v[74:75]\n"
  "ds_write_b128 v21, v[208:211] offset:10240\n"
  "v_mfma_f32_16x16x16_bf16 v[220:223], a[72:73], v[168:169], 0\n"
  "v_exp_f32 v77, v77\n"
  "v_mfma_f32_16x16x16_bf16 v[208:211], a[232:233], v[168:169], 0\n"
  "v_exp_f32 v78, v78\n"
  "ds_write_b128 v21, v[216:219] offset:15360\n"
  "v_pk_add_f32 v[58:59], v[58:59], v[76:77]\n"
  "v_pk_add_f32 v[56:57], v[56:57], v[78:79]\n"
  "v_pk_add_f32 v[58:59], v[58:59], v[80:81]\n"
  "v_pk_add_f32 v[56:57], v[56:57], v[82:83]\n"
  "buffer_load_dwordx4 v23, s[48:51], s74 offen offset:1024 lds\n"
  "v_pk_add_f32 v[58:59], v[58:59], v[56:57]\n"
  "v_add_f32 v61, v62, v63\n"
  "v_mfma_f32_16x16x32_bf16 v[208:211], a[224:227], v[160:163], v[208:211]\n"
  "v_mov_b32 v62, v61\n"
  "v_add_f32 v57, v58, v59\n"
  "v_mfma_f32_16x16x32_bf16 v[208:211], a[228:231], v[164:167], v[208:211]\n"
  "v_mov_b32 v58, v57\n"
  "v_permlane16_swap_b32 v61, v62\n"
  "v_mfma_f32_16x16x32_bf16 v[192:195], a[184:187], v[160:163], v[192:195]\n"
  "v_add_f32 v61, v61, v62\n"
  "v_permlane16_swap_b32 v57, v58\n"
  "v_mfma_f32_16x16x16_bf16 v[216:219], a[252:253], v[168:169], 0\n"
  "v_mov_b32 v62, v61\n"
  "v_add_f32 v57, v57, v58\n"
  "v_mfma_f32_16x16x16_bf16 v[212:215], a[242:243], v[168:169], 0\n"
  "v_cvt_pk_bf16_f32 v208, v208, v209\n"
  "v_mov_b32 v58, v57\n"
  "v_cvt_pk_bf16_f32 v209, v210, v211\n"
  "v_permlane32_swap_b32 v61, v62\n"
  "v_mfma_f32_16x16x32_bf16 v[216:219], a[244:247], v[160:163], v[216:219]\n"
  "v_permlane32_swap_b32 v57, v58\n"
  "buffer_load_dwordx4 v24, s[48:51], s74 offen offset:2048 lds\n"
  "v_mfma_f32_16x16x32_bf16 v[212:215], a[234:237], v[160:163], v[212:215]\n"
  "v_add_f32 v61, v61, v62\n"
  "v_add_f32 v57, v57, v58\n"
  "v_mfma_f32_16x16x32_bf16 v[204:207], a[218:221], v[120:123], v[204:207]\n"
  "v_rcp_f32 v57, v57\n"
  "v_mfma_f32_16x16x32_bf16 v[248:251], a[244:247], v[84:87], v[248:251]\n"
  "Lh12_%=:\n"
  "s_cbranch_execz Lh13_%=\n"
  "v_mov_b32 v56, v57\n"
  "v_rcp_f32 v61, v61\n"
  "s_nop 0\n"
  "v_mov_b32 v60, v61\n"
  "v_pk_mul_f32 v[82:83], v[82:83], v[56:57]\n"
  "v_pk_mul_f32 v[112:113], v[112:113], v[60:61]\n"
  "v_pk_mul_f32 v[70:71], v[70:71], v[56:57]\n"
  "v_pk_mul_f32 v[104:105], v[104:105], v[60:61]\n"
  "v_pk_mul_f32 v[64:65], v[64:65], v[56:57]\n"
  "v_pk_mul_f32 v[80:81], v[80:81], v[56:57]\n"
  "v_pk_mul_f32 v[78:79], v[78:79], v[56:57]\n"
  "v_pk_mul_f32 v[96:97], v[96:97], v[60:61]\n"
  "v_pk_mul_f32 v[72:73], v[72:73], v[56:57]\n"
  "v_pk_mul_f32 v[110:111], v[110:111], v[60:61]\n"
  "v_pk_mul_f32 v[76:77], v[76:77], v[56:57]\n"
  "v_pk_mul_f32 v[106:107], v[106:107], v[60:61]\n"
  "v_pk_mul_f32 v[100:101], v[100:101], v[60:61]\n"
  "v_pk_mul_f32 v[114:115], v[114:115], v[60:61]\n"
  "v_pk_mul_f32 v[68:69], v[68:69], v[56:57]\n"
  "v_pk_mul_f32 v[102:103], v[102:103], v[60:61]\n"
  "v_pk_mul_f32 v[74:75], v[74:75], v[56:57]\n"
  "v_pk_mul_f32 v[98:99], v[98:99], v[60:61]\n"
  "v_pk_mul_f32 v[108:109], v[108:109], v[60:61]\n"
  "v_pk_mul_f32 v[66:67], v[66:67], v[56:57]\n"
  "buffer_load_dwordx4 v25, s[48:51], s74 offen offset:3072 lds\n"
  "s_xor_b32 s74, s79, 0x40000\n"
  "s_add_u32 m0, s76, 0x24000\n"
  "v_mfma_f32_16x16x32_bf16 v[240:243], a[224:227], v[84:87], v[240:243]\n"
  "v_cvt_pk_bf16_f32 v202, v204, v205\n"
  "v_cvt_pk_bf16_f32 v203, v206, v207\n"
  "v_mfma_f32_16x16x32_bf16 v[240:243], a[228:231], v[88:91], v[240:243]\n"
  "ds_write_b128 v21, v[200:203] offset:5120\n"
  "v_mfma_f32_16x16x16_bf16 v[200:203], a[212:213], v[168:169], 0\n"
  "v_cvt_pk_bf16_f32 v181, v66, v67\n"
  "v_cvt_pk_bf16_f32 v184, v72, v73\n"
  "v_mfma_f32_16x16x16_bf16 v[204:207], a[222:223], v[168:169], 0\n"
  "v_cvt_pk_bf16_f32 v174, v104, v105\n"
  "v_cvt_pk_bf16_f32 v182, v68, v69\n"
  "v_cvt_pk_bf16_f32 v170, v96, v97\n"
  "v_cvt_pk_bf16_f32 v240, v240, v241\n"
  "v_mfma_f32_16x16x32_bf16 v[200:203], a[204:207], v[160:163], v[200:203]\n"
  "v_cvt_pk_bf16_f32 v241, v242, v243\n"
  "v_cvt_pk_bf16_f32 v187, v78, v79\n"
  "v_mfma_f32_16x16x32_bf16 v[204:207], a[214:217], v[160:163], v[204:207]\n"
  "v_cvt_pk_bf16_f32 v189, v82, v83\n"
  "v_cvt_pk_bf16_f32 v176, v108, v109\n"
  "v_mfma_f32_16x16x32_bf16 v[204:207], a[218:221], v[164:167], v[204:207]\n"
  "v_cvt_pk_bf16_f32 v183, v70, v71\n"
  "buffer_load_dwordx4 v22, s[48:51], s74 offen lds\n"
  "v_mfma_f32_16x16x32_bf16 v[200:203], a[208:211], v[164:167], v[200:203]\n"
  "v_cvt_pk_bf16_f32 v179, v114, v115\n"
  "v_cvt_pk_bf16_f32 v177, v110, v111\n"
  "v_mfma_f32_16x16x32_bf16 v[192:195], a[188:191], v[164:167], v[192:195]\n"
  "v_cvt_pk_bf16_f32 v180, v64, v65\n"
  "v_cvt_pk_bf16_f32 v178, v112, v113\n"
  "v_mfma_f32_16x16x32_bf16 v[220:223], a[64:67], v[160:163], v[220:223]\n"
  "v_cvt_pk_bf16_f32 v172, v100, v101\n"
  "v_cvt_pk_bf16_f32 v185, v74, v75\n"
  "v_mfma_f32_16x16x32_bf16 v[252:255], a[64:67], v[84:87], v[252:255]\n"
  "v_cvt_pk_bf16_f32 v200, v200, v201\n"
  "v_cvt_pk_bf16_f32 v201, v202, v203\n"
  "v_mfma_f32_16x16x32_bf16 v[252:255], a[68:71], v[88:91], v[252:255]\n"
  "v_cvt_pk_bf16_f32 v192, v192, v193\n"
  "v_cvt_pk_bf16_f32 v202, v204, v205\n"
  "v_mfma_f32_16x16x32_bf16 v[248:251], a[248:251], v[88:91], v[248:251]\n"
  "v_cvt_pk_bf16_f32 v203, v206, v207\n"
  "Lh13_%=:\n"
  "s_cbranch_execz Lh14_%=\n"
  "v_cvt_pk_bf16_f32 v193, v194, v195\n"
  "v_mfma_f32_16x16x32_bf16 v[224:227], a[188:191], v[88:91], v[224:227]\n"
  "ds_write_b128 v21, v[200:203] offset:7168\n"
  "v_mfma_f32_16x16x32_bf16 v[196:199], a[198:201], v[164:167], v[196:199]\n"
  "v_cvt_pk_bf16_f32 v186, v76, v77\n"
  "buffer_load_dwordx4 v23, s[48:51], s74 offen offset:1024 lds\n"
  "v_mfma_f32_16x16x32_bf16 v[220:223], a[68:71], v[164:167], v[220:223]\n"
  "v_cvt_pk_bf16_f32 v248, v248, v249\n"
  "v_cvt_pk_bf16_f32 v249, v250, v251\n"
  "v_mfma_f32_16x16x32_bf16 v[216:219], a[248:251], v[164:167], v[216:219]\n"
  "v_cvt_pk_bf16_f32 v250, v252, v253\n"
  "v_cvt_pk_bf16_f32 v224, v224, v225\n"
  "v_mfma_f32_16x16x32_bf16 v[244:247], a[234:237], v[84:87], v[244:247]\n"
  "v_cvt_pk_bf16_f32 v195, v198, v199\n"
  "v_cvt_pk_bf16_f32 v194, v196, v197\n"
  "v_mfma_f32_16x16x32_bf16 v[244:247], a[238:241], v[88:91], v[244:247]\n"
  "ds_write_b128 v21, v[192:195] offset:2048\n"
  "v_mfma_f32_16x16x32_bf16 v[212:215], a[238:241], v[164:167], v[212:215]\n"
  "v_cvt_pk_bf16_f32 v216, v216, v217\n"
  "v_cvt_pk_bf16_f32 v217, v218, v219\n"
  "v_mfma_f32_16x16x32_bf16 v[232:235], a[208:211], v[88:91], v[232:235]\n"
  "v_cvt_pk_bf16_f32 v218, v220, v221\n"
  "v_cvt_pk_bf16_f32 v251, v254, v255\n"
  "v_mfma_f32_16x16x16_bf16 v[252:255], a[72:73], v[178:179], 0\n"
  "ds_write_b128 v21, v[248:251] offset:16384\n"
  "buffer_load_dwordx4 v24, s[48:51], s74 offen offset:2048 lds\n"
  "v_mfma_f32_16x16x16_bf16 v[248:251], a[252:253], v[178:179], 0\n"
  "v_cvt_pk_bf16_f32 v210, v212, v213\n"
  "v_cvt_pk_bf16_f32 v211, v214, v215\n"
  "v_cvt_pk_bf16_f32 v232, v232, v233\n"
  "v_cvt_pk_bf16_f32 v242, v244, v245\n"
  "v_cvt_pk_bf16_f32 v233, v234, v235\n"
  "ds_write_b128 v21, v[208:211] offset:12288\n"
  "v_cvt_pk_bf16_f32 v235, v238, v239\n"
  "v_cvt_pk_bf16_f32 v243, v246, v247\n"
  "v_mfma_f32_16x16x16_bf16 v[244:247], a[242:243], v[178:179], 0\n"
  "v_cvt_pk_bf16_f32 v225, v226, v227\n"
  "v_cvt_pk_bf16_f32 v227, v230, v231\n"
  "v_cvt_pk_bf16_f32 v226, v228, v229\n"
  "v_mfma_f32_16x16x16_bf16 v[228:231], a[202:203], v[178:179], 0\n"
  "ds_write_b128 v21, v[240:243] offset:11264\n"
  "v_mfma_f32_16x16x16_bf16 v[240:243], a[232:233], v[178:179], 0\n"
  "v_cvt_pk_bf16_f32 v234, v236, v237\n"
  "v_cvt_pk_bf16_f32 v219, v222, v223\n"
  "v_mfma_f32_16x16x16_bf16 v[236:239], a[222:223], v[178:179], 0\n"
  "v_cvt_pk_bf16_f32 v188, v80, v81\n"
  "buffer_load_dwordx4 v25, s[48:51], s74 offen offset:3072 lds\n"
  "ds_write_b128 v21, v[232:235] offset:6144\n"
  "v_mfma_f32_16x16x16_bf16 v[204:207], a[222:223], v[188:189], 0\n"
  "v_cvt_pk_bf16_f32 v175, v106, v107\n"
  "v_cvt_pk_bf16_f32 v173, v102, v103\n"
  "v_mfma_f32_16x16x16_bf16 v[208:211], a[232:233], v[188:189], 0\n"
  "v_cvt_pk_bf16_f32 v171, v98, v99\n"
  "ds_write_b128 v21, v[216:219] offset:17408\n"
  "v_mfma_f32_16x16x16_bf16 v[216:219], a[252:253], v[188:189], 0\n"
  "v_mfma_f32_16x16x16_bf16 v[220:223], a[72:73], v[188:189], 0\n"
  "ds_write_b128 v21, v[224:227] offset:1024\n"
  "v_mfma_f32_16x16x16_bf16 v[224:227], a[192:193], v[178:179], 0\n"
  "v_mfma_f32_16x16x16_bf16 v[212:215], a[242:243], v[188:189], 0\n"
  "v_mfma_f32_16x16x16_bf16 v[192:195], a[192:193], v[188:189], 0\n"
  "v_mfma_f32_16x16x16_bf16 v[200:203], a[212:213], v[188:189], 0\n"
  "v_mfma_f32_16x16x16_bf16 v[196:199], a[202:203], v[188:189], 0\n"
  "v_mfma_f32_16x16x16_bf16 v[232:235], a[212:213], v[178:179], 0\n"
  "Lh14_%=:\n"
  "s_cbranch_execz Lh15_%=\n"
  "v_mfma_f32_16x16x32_bf16 v[216:219], a[244:247], v[180:183], v[216:219]\n"
  "v_mfma_f32_16x16x32_bf16 v[240:243], a[224:227], v[170:173], v[240:243]\n"
  "v_mfma_f32_16x16x32_bf16 v[236:239], a[214:217], v[170:173], v[236:239]\n"
  "s_nop 0\n"
  "v_mfma_f32_16x16x32_bf16 v[200:203], a[204:207], v[180:183], v[200:203]\n"
  "v_mfma_f32_16x16x32_bf16 v[224:227], a[184:187], v[170:173], v[224:227]\n"
  "v_mfma_f32_16x16x32_bf16 v[200:203], a[208:211], v[184:187], v[200:203]\n"
  "v_mfma_f32_16x16x32_bf16 v[236:239], a[218:221], v[174:177], v[236:239]\n"
  "v_mfma_f32_16x16x32_bf16 v[240:243], a[228:231], v[174:177], v[240:243]\n"
  "v_mfma_f32_16x16x32_bf16 v[196:199], a[194:197], v[180:183], v[196:199]\n"
  "v_mfma_f32_16x16x32_bf16 v[224:227], a[188:191], v[174:177], v[224:227]\n"
  "v_mfma_f32_16x16x32_bf16 v[232:235], a[204:207], v[170:173], v[232:235]\n"
  "v_mfma_f32_16x16x32_bf16 v[232:235], a[208:211], v[174:177], v[232:235]\n"
  "v_mfma_f32_16x16x32_bf16 v[216:219], a[248:251], v[184:187], v[216:219]\n"
  "s_nop 0\n"
  "v_cvt_pk_bf16_f32 v200, v200, v201\n"
  "v_mfma_f32_16x16x32_bf16 v[208:211], a[224:227], v[180:183], v[208:211]\n"
  "v_cvt_pk_bf16_f32 v240, v240, v241\n"
  "v_cvt_pk_bf16_f32 v241, v242, v243\n"
  "v_mfma_f32_16x16x32_bf16 v[208:211], a[228:231], v[184:187], v[208:211]\n"
  "v_cvt_pk_bf16_f32 v224, v224, v225\n"
  "v_cvt_pk_bf16_f32 v225, v226, v227\n"
  "v_mfma_f32_16x16x32_bf16 v[252:255], a[64:67], v[170:173], v[252:255]\n"
  "v_cvt_pk_bf16_f32 v216, v216, v217\n"
  "v_cvt_pk_bf16_f32 v232, v232, v233\n"
  "v_mfma_f32_16x16x32_bf16 v[252:255], a[68:71], v[174:177], v[252:255]\n"
  "v_cvt_pk_bf16_f32 v233, v234, v235\n"
  "v_cvt_pk_bf16_f32 v235, v238, v239\n"
  "v_mfma_f32_16x16x32_bf16 v[220:223], a[64:67], v[180:183], v[220:223]\n"
  "v_cvt_pk_bf16_f32 v217, v218, v219\n"
  "v_cvt_pk_bf16_f32 v208, v208, v209\n"
  "v_mfma_f32_16x16x32_bf16 v[220:223], a[68:71], v[184:187], v[220:223]\n"
  "v_cvt_pk_bf16_f32 v209, v210, v211\n"
  "v_cvt_pk_bf16_f32 v201, v202, v203\n"
  "v_mfma_f32_16x16x32_bf16 v[204:207], a[214:217], v[180:183], v[204:207]\n"
  "buffer_store_dwordx4 v[4:7], v32, s[52:55], s98 offen sc1\n"
  "v_mfma_f32_16x16x32_bf16 v[204:207], a[218:221], v[184:187], v[204:207]\n"
  "v_cvt_pk_bf16_f32 v234, v236, v237\n"
  "v_mfma_f32_16x16x32_bf16 v[196:199], a[198:201], v[184:187], v[196:199]\n"
  "ds_write_b128 v21, v[232:235] offset:8192\n"
  "v_mfma_f32_16x16x32_bf16 v[228:231], a[194:197], v[170:173], v[228:231]\n"
  "buffer_store_dwordx4 v[4:7], v32, s[52:55], s98 offen offset:1024 sc1\n"
  "v_mfma_f32_16x16x32_bf16 v[228:231], a[198:201], v[174:177], v[228:231]\n"
  "v_cvt_pk_bf16_f32 v219, v222, v223\n"
  "v_cvt_pk_bf16_f32 v218, v220, v221\n"
  "v_mfma_f32_16x16x32_bf16 v[192:195], a[184:187], v[180:183], v[192:195]\n"
  "v_cvt_pk_bf16_f32 v203, v206, v207\n"
  "ds_write_b128 v21, v[216:219] offset:19456\n"
  "buffer_store_dwordx4 v[4:7], v32, s[52:55], s98 offen offset:2048 sc1\n"
  "v_mfma_f32_16x16x32_bf16 v[248:251], a[244:247], v[170:173], v[248:251]\n"
  "v_cvt_pk_bf16_f32 v202, v204, v205\n"
  "v_cvt_pk_bf16_f32 v227, v230, v231\n"
  "v_mfma_f32_16x16x32_bf16 v[248:251], a[248:251], v[174:177], v[248:251]\n"
  "ds_write_b128 v21, v[200:203] offset:9216\n"
  "v_mfma_f32_16x16x32_bf16 v[212:215], a[234:237], v[180:183], v[212:215]\n"
  "v_cvt_pk_bf16_f32 v226, v228, v229\n"
  "v_mfma_f32_16x16x32_bf16 v[212:215], a[238:241], v[184:187], v[212:215]\n"
  "ds_write_b128 v21, v[224:227] offset:3072\n"
  "v_mfma_f32_16x16x32_bf16 v[244:247], a[234:237], v[170:173], v[244:247]\n"
  "v_mfma_f32_16x16x32_bf16 v[244:247], a[238:241], v[174:177], v[244:247]\n"
  "s_nop 0\n"
  "v_cvt_pk_bf16_f32 v248, v248, v249\n"
  "v_mfma_f32_16x16x32_bf16 v[192:195], a[188:191], v[184:187], v[192:195]\n"
  "v_cvt_pk_bf16_f32 v249, v250, v251\n"
  "v_cvt_pk_bf16_f32 v251, v254, v255\n"
  "Lh15_%=:\n"
  "s_cbranch_execz Lh16_%=\n"
  "v_cvt_pk_bf16_f32 v211, v214, v215\n"
  "v_cvt_pk_bf16_f32 v250, v252, v253\n"
  "v_cvt_pk_bf16_f32 v210, v212, v213\n"
  "ds_write_b128 v21, v[248:251] offset:18432\n"
  "v_cvt_pk_bf16_f32 v243, v246, v247\n"
  "v_cvt_pk_bf16_f32 v242, v244, v245\n"
  "v_cvt_pk_bf16_f32 v192, v192, v193\n"
  "v_cvt_pk_bf16_f32 v193, v194, v195\n"
  "v_cvt_pk_bf16_f32 v194, v196, v197\n"
  "v_cvt_pk_bf16_f32 v195, v198, v199\n"
  "ds_write_b128 v21, v[240:243] offset:13312\n"
  "ds_write_b128 v21, v[208:211] offset:14336\n"
  "ds_write_b128 v21, v[192:195] offset:4096\n"
  "s_waitcnt lgkmcnt(0)\n"
  "s_waitcnt vmcnt(19)\n"
  "s_barrier\n"
  "v_add_u32 v21, 0, v1\n"
  "ds_read_b64_tr_b16 v[208:209], v28 offset:16384\n"
  "ds_read_b64_tr_b16 v[210:211], v28 offset:20480\n"
  "ds_read_b128 a[0:3], v21\n"
  "ds_read_b128 a[4:7], v21 offset:1024\n"
  "ds_read_b128 a[8:11], v21 offset:2048\n"
  "ds_read_b128 a[12:15], v21 offset:3072\n"
  "ds_read_b128 a[16:19], v21 offset:4096\n"
  "ds_read_b64_tr_b16 v[212:213], v29 offset:16384\n"
  "ds_read_b64_tr_b16 v[214:215], v29 offset:20480\n"
  "s_waitcnt lgkmcnt(6)\n"
  "v_mfma_f32_16x16x32_bf16 v[128:131], v[208:211], a[0:3], 0\n"
  "ds_read_b64_tr_b16 v[216:217], v28 offset:24576\n"
  "ds_read_b64_tr_b16 v[218:219], v28 offset:28672\n"
  "buffer_store_dwordx4 v[4:7], v32, s[52:55], s98 offen offset:3072 sc1\n"
  "s_waitcnt lgkmcnt(7)\n"
  "v_mfma_f32_16x16x32_bf16 v[132:135], v[208:211], a[4:7], 0\n"
  "ds_read_b128 a[20:23], v21 offset:5120\n"
  "ds_read_b128 a[24:27], v21 offset:6144\n"
  "s_waitcnt lgkmcnt(8)\n"
  "v_mfma_f32_16x16x32_bf16 v[136:139], v[208:211], a[8:11], 0\n"
  "ds_read_b128 a[28:31], v21 offset:7168\n"
  "ds_read_b128 a[32:35], v21 offset:8192\n"
  "s_add_u32 s98, s98, 0x1000\n"
  "buffer_store_dwordx4 v[4:7], v32, s[52:55], s98 offen sc1\n"
  "s_waitcnt lgkmcnt(9)\n"
  "v_mfma_f32_16x16x32_bf16 v[140:143], v[208:211], a[12:15], 0\n"
  "ds_read_b128 a[36:39], v21 offset:9216\n"
  "ds_read_b64_tr_b16 v[220:221], v29 offset:24576\n"
  "s_waitcnt lgkmcnt(10)\n"
  "v_mfma_f32_16x16x32_bf16 v[144:147], v[208:211], a[16:19], 0\n"
  "ds_read_b64_tr_b16 v[222:223], v29 offset:28672\n"
  "buffer_store_dwordx4 v[4:7], v32, s[52:55], s23 offen sc1\n"
  "s_waitcnt lgkmcnt(9)\n"
  "v_mfma_f32_16x16x32_bf16 v[148:151], v[212:215], a[0:3], 0\n"
  "v_mfma_f32_16x16x32_bf16 v[152:155], v[212:215], a[4:7], 0\n"
  "buffer_store_dwordx4 v[4:7], v32, s[52:55], s23 offen offset:1024 sc1\n"
  "v_mfma_f32_16x16x32_bf16 v[156:159], v[212:215], a[8:11], 0\n"
  "v_mfma_f32_16x16x32_bf16 v[160:163], v[212:215], a[12:15], 0\n"
  "buffer_store_dwordx4 v[4:7], v3, s[52:55], s23 offen sc1\n"
  "v_mfma_f32_16x16x32_bf16 v[164:167], v[212:215], a[16:19], 0\n"
  "s_waitcnt lgkmcnt(6)\n"
  "v_mfma_f32_16x16x32_bf16 v[128:131], v[216:219], a[20:23], v[128:131]\n"
  "s_waitcnt vmcnt(20)\n"
  "s_waitcnt lgkmcnt(0)\n"
  "s_barrier\n"
  "v_mfma_f32_16x16x32_bf16 v[132:135], v[216:219], a[24:27], v[132:135]\n"
  "ds_read_b64_tr_b16 v[208:209], v28 offset:32768\n"
  "ds_read_b64_tr_b16 v[210:211], v28 offset:36864\n"
  "v_mfma_f32_16x16x32_bf16 v[136:139], v[216:219], a[28:31], v[136:139]\n"
  "ds_read_b128 a[40:43], v21 offset:10240\n"
  "ds_read_b128 a[44:47], v21 offset:11264\n"
  "v_mfma_f32_16x16x32_bf16 v[140:143], v[216:219], a[32:35], v[140:143]\n"
  "ds_read_b128 a[48:51], v21 offset:12288\n"
  "ds_read_b128 a[52:55], v21 offset:13312\n"
  "Lh16_%=:\n"
  "s_cbranch_execz Lh17_%=\n"
  "s_add_u32 m0, s76, 0x14000\n"
  "s_xor_b32 s74, s79, 0x50000\n"
  "v_mfma_f32_16x16x32_bf16 v[144:147], v[216:219], a[36:39], v[144:147]\n"
  "ds_read_b128 a[56:59], v21 offset:14336\n"
  "ds_read_b64_tr_b16 v[212:213], v29 offset:32768\n"
  "buffer_load_dwordx4 v22, s[48:51], s74 offen lds\n"
  "v_mfma_f32_16x16x32_bf16 v[148:151], v[220:223], a[20:23], v[148:151]\n"
  "ds_read_b64_tr_b16 v[214:215], v29 offset:36864\n"
  "v_mfma_f32_16x16x32_bf16 v[152:155], v[220:223], a[24:27], v[152:155]\n"
  "buffer_load_dwordx4 v23, s[48:51], s74 offen offset:1024 lds\n"
  "v_mfma_f32_16x16x32_bf16 v[156:159], v[220:223], a[28:31], v[156:159]\n"
  "v_mfma_f32_16x16x32_bf16 v[160:163], v[220:223], a[32:35], v[160:163]\n"
  "buffer_load_dwordx4 v24, s[48:51], s74 offen offset:2048 lds\n"
  "v_mfma_f32_16x16x32_bf16 v[164:167], v[220:223], a[36:39], v[164:167]\n"
  "s_waitcnt lgkmcnt(6)\n"
  "v_mfma_f32_16x16x32_bf16 v[128:131], v[208:211], a[40:43], v[128:131]\n"
  "ds_read_b64_tr_b16 v[216:217], v28 offset:40960\n"
  "ds_read_b64_tr_b16 v[218:219], v28 offset:45056\n"
  "s_waitcnt lgkmcnt(7)\n"
  "v_mfma_f32_16x16x32_bf16 v[132:135], v[208:211], a[44:47], v[132:135]\n"
  "ds_read_b128 a[60:63], v21 offset:15360\n"
  "ds_read_b128 a[64:67], v21 offset:16384\n"
  "s_waitcnt lgkmcnt(8)\n"
  "v_mfma_f32_16x16x32_bf16 v[136:139], v[208:211], a[48:51], v[136:139]\n"
  "ds_read_b128 a[68:71], v21 offset:17408\n"
  "ds_read_b128 a[72:75], v21 offset:18432\n"
  "s_waitcnt lgkmcnt(9)\n"
  "v_mfma_f32_16x16x32_bf16 v[140:143], v[208:211], a[52:55], v[140:143]\n"
  "ds_read_b128 a[76:79], v21 offset:19456\n"
  "ds_read_b64_tr_b16 v[220:221], v29 offset:40960\n"
  "s_waitcnt lgkmcnt(10)\n"
  "v_mfma_f32_16x16x32_bf16 v[144:147], v[208:211], a[56:59], v[144:147]\n"
  "ds_read_b64_tr_b16 v[222:223], v29 offset:45056\n"
  "buffer_load_dwordx4 v25, s[48:51], s74 offen offset:3072 lds\n"
  "s_waitcnt lgkmcnt(9)\n"
  "v_mfma_f32_16x16x32_bf16 v[148:151], v[212:215], a[40:43], v[148:151]\n"
  "v_mfma_f32_16x16x32_bf16 v[152:155], v[212:215], a[44:47], v[152:155]\n"
  "v_mfma_f32_16x16x32_bf16 v[156:159], v[212:215], a[48:51], v[156:159]\n"
  "v_mfma_f32_16x16x32_bf16 v[160:163], v[212:215], a[52:55], v[160:163]\n"
  "v_mfma_f32_16x16x32_bf16 v[164:167], v[212:215], a[56:59], v[164:167]\n"
  "s_waitcnt lgkmcnt(6)\n"
  "v_mfma_f32_16x16x32_bf16 v[128:131], v[216:219], a[60:63], v[128:131]\n"
  "s_waitcnt vmcnt(20)\n"
  "s_waitcnt lgkmcnt(0)\n"
  "s_barrier\n"
  "v_mfma_f32_16x16x32_bf16 v[132:135], v[216:219], a[64:67], v[132:135]\n"
  "v_add_u32 v21, 0x5000, v1\n"
  "ds_read_b64_tr_b16 v[208:209], v28 offset:49152\n"
  "v_mfma_f32_16x16x32_bf16 v[136:139], v[216:219], a[68:71], v[136:139]\n"
  "ds_read_b64_tr_b16 v[210:211], v28 offset:53248\n"
  "ds_read_b128 a[80:83], v21\n"
  "v_mfma_f32_16x16x32_bf16 v[140:143], v[216:219], a[72:75], v[140:143]\n"
  "ds_read_b128 a[84:87], v21 offset:1024\n"
  "ds_read_b128 a[88:91], v21 offset:2048\n"
  "s_add_u32 m0, s76, 0x18000\n"
  "s_xor_b32 s74, s79, 0x60000\n"
  "v_mfma_f32_16x16x32_bf16 v[144:147], v[216:219], a[76:79], v[144:147]\n"
  "ds_read_b128 a[92:95], v21 offset:3072\n"
  "ds_read_b128 a[96:99], v21 offset:4096\n"
  "buffer_load_dwordx4 v22, s[48:51], s74 offen lds\n"
  "v_mfma_f32_16x16x32_bf16 v[148:151], v[220:223], a[60:63], v[148:151]\n"
  "ds_read_b64_tr_b16 v[212:213], v29 offset:49152\n"
  "ds_read_b64_tr_b16 v[214:215], v29 offset:53248\n"
  "v_mfma_f32_16x16x32_bf16 v[152:155], v[220:223], a[64:67], v[152:155]\n"
  "buffer_load_dwordx4 v23, s[48:51], s74 offen offset:1024 lds\n"
  "v_mfma_f32_16x16x32_bf16 v[156:159], v[220:223], a[68:71], v[156:159]\n"
  "v_mfma_f32_16x16x32_bf16 v[160:163], v[220:223], a[72:75], v[160:163]\n"
  "buffer_load_dwordx4 v24, s[48:51], s74 offen offset:2048 lds\n"
  "Lh17_%=:\n"
  "s_cbranch_execz Lh18_%=\n"
  "v_mfma_f32_16x16x32_bf16 v[164:167], v[220:223], a[76:79], v[164:167]\n"
  "s_waitcnt lgkmcnt(6)\n"
  "v_mfma_f32_16x16x32_bf16 v[128:131], v[208:211], a[80:83], v[128:131]\n"
  "ds_read_b64_tr_b16 v[216:217], v28 offset:57344\n"
  "ds_read_b64_tr_b16 v[218:219], v28 offset:61440\n"
  "s_waitcnt lgkmcnt(7)\n"
  "v_mfma_f32_16x16x32_bf16 v[132:135], v[208:211], a[84:87], v[132:135]\n"
  "ds_read_b128 a[100:103], v21 offset:5120\n"
  "ds_read_b128 a[104:107], v21 offset:6144\n"
  "s_waitcnt lgkmcnt(8)\n"
  "v_mfma_f32_16x16x32_bf16 v[136:139], v[208:211], a[88:91], v[136:139]\n"
  "ds_read_b128 a[108:111], v21 offset:7168\n"
  "ds_read_b128 a[112:115], v21 offset:8192\n"
  "s_waitcnt lgkmcnt(9)\n"
  "v_mfma_f32_16x16x32_bf16 v[140:143], v[208:211], a[92:95], v[140:143]\n"
  "ds_read_b128 a[116:119], v21 offset:9216\n"
  "ds_read_b64_tr_b16 v[220:221], v29 offset:57344\n"
  "s_waitcnt lgkmcnt(10)\n"
  "v_mfma_f32_16x16x32_bf16 v[144:147], v[208:211], a[96:99], v[144:147]\n"
  "ds_read_b64_tr_b16 v[222:223], v29 offset:61440\n"
  "s_waitcnt lgkmcnt(9)\n"
  "v_mfma_f32_16x16x32_bf16 v[148:151], v[212:215], a[80:83], v[148:151]\n"
  "v_mfma_f32_16x16x32_bf16 v[152:155], v[212:215], a[84:87], v[152:155]\n"
  "buffer_load_dwordx4 v25, s[48:51], s74 offen offset:3072 lds\n"
  "v_mfma_f32_16x16x32_bf16 v[156:159], v[212:215], a[88:91], v[156:159]\n"
  "s_add_u32 m0, s76, 0\n"
  "s_xor_b32 s74, s79, 0x70000\n"
  "v_mfma_f32_16x16x32_bf16 v[160:163], v[212:215], a[92:95], v[160:163]\n"
  "buffer_load_dwordx4 v22, s[48:51], s74 offen lds\n"
  "v_mfma_f32_16x16x32_bf16 v[164:167], v[212:215], a[96:99], v[164:167]\n"
  "s_waitcnt lgkmcnt(6)\n"
  "v_mfma_f32_16x16x32_bf16 v[128:131], v[216:219], a[100:103], v[128:131]\n"
  "s_waitcnt vmcnt(21)\n"
  "s_waitcnt lgkmcnt(0)\n"
  "s_barrier\n"
  "v_mfma_f32_16x16x32_bf16 v[132:135], v[216:219], a[104:107], v[132:135]\n"
  "ds_read_b64_tr_b16 v[208:209], v30\n"
  "ds_read_b64_tr_b16 v[210:211], v30 offset:4096\n"
  "v_mfma_f32_16x16x32_bf16 v[136:139], v[216:219], a[108:111], v[136:139]\n"
  "ds_read_b128 a[120:123], v21 offset:10240\n"
  "ds_read_b128 a[124:127], v21 offset:11264\n"
  "v_mfma_f32_16x16x32_bf16 v[140:143], v[216:219], a[112:115], v[140:143]\n"
  "ds_read_b128 a[128:131], v21 offset:12288\n"
  "ds_read_b128 a[132:135], v21 offset:13312\n"
  "v_mfma_f32_16x16x32_bf16 v[144:147], v[216:219], a[116:119], v[144:147]\n"
  "ds_read_b128 a[136:139], v21 offset:14336\n"
  "ds_read_b64_tr_b16 v[212:213], v31\n"
  "buffer_load_dwordx4 v23, s[48:51], s74 offen offset:1024 lds\n"
  "v_mfma_f32_16x16x32_bf16 v[148:151], v[220:223], a[100:103], v[148:151]\n"
  "ds_read_b64_tr_b16 v[214:215], v31 offset:4096\n"
  "v_mfma_f32_16x16x32_bf16 v[152:155], v[220:223], a[104:107], v[152:155]\n"
  "buffer_load_dwordx4 v24, s[48:51], s74 offen offset:2048 lds\n"
  "v_mfma_f32_16x16x32_bf16 v[156:159], v[220:223], a[108:111], v[156:159]\n"
  "v_mfma_f32_16x16x32_bf16 v[160:163], v[220:223], a[112:115], v[160:163]\n"
  "buffer_load_dwordx4 v25, s[48:51], s74 offen offset:3072 lds\n"
  "v_mfma_f32_16x16x32_bf16 v[164:167], v[220:223], a[116:119], v[164:167]\n"
  "s_waitcnt lgkmcnt(6)\n"
  "v_mfma_f32_16x16x32_bf16 v[128:131], v[208:211], a[120:123], v[128:131]\n"
  "ds_read_b64_tr_b16 v[216:217], v30 offset:8192\n"
  "ds_read_b64_tr_b16 v[218:219], v30 offset:12288\n"
  "s_waitcnt lgkmcnt(7)\n"
  "v_mfma_f32_16x16x32_bf16 v[132:135], v[208:211], a[124:127], v[132:135]\n"
  "ds_read_b128 a[140:143], v21 offset:15360\n"
  "ds_read_b128 a[144:147], v21 offset:16384\n"
  "s_waitcnt lgkmcnt(8)\n"
  "v_mfma_f32_16x16x32_bf16 v[136:139], v[208:211], a[128:131], v[136:139]\n"
  "ds_read_b128 a[148:151], v21 offset:17408\n"
  "ds_read_b128 a[152:155], v21 offset:18432\n"
  "s_waitcnt lgkmcnt(9)\n"
  "v_mfma_f32_16x16x32_bf16 v[140:143], v[208:211], a[132:135], v[140:143]\n"
  "ds_read_b128 a[156:159], v21 offset:19456\n"
  "Lh18_%=:\n"
  "s_cbranch_execz Lh19_%=\n"
  "ds_read_b64_tr_b16 v[220:221], v31 offset:8192\n"
  "s_add_u32 m0, s76, 0x1c000\n"
  "s_xor_b32 s74, s80, 0\n"
  "s_waitcnt lgkmcnt(10)\n"
  "v_mfma_f32_16x16x32_bf16 v[144:147], v[208:211], a[136:139], v[144:147]\n"
  "ds_read_b64_tr_b16 v[222:223], v31 offset:12288\n"
  "buffer_load_dwordx4 v22, s[48:51], s74 offen lds\n"
  "s_waitcnt lgkmcnt(9)\n"
  "v_mfma_f32_16x16x32_bf16 v[148:151], v[212:215], a[120:123], v[148:151]\n"
  "v_mfma_f32_16x16x32_bf16 v[152:155], v[212:215], a[124:127], v[152:155]\n"
  "buffer_load_dwordx4 v23, s[48:51], s74 offen offset:1024 lds\n"
  "v_mfma_f32_16x16x32_bf16 v[156:159], v[212:215], a[128:131], v[156:159]\n"
  "v_mfma_f32_16x16x32_bf16 v[160:163], v[212:215], a[132:135], v[160:163]\n"
  "buffer_load_dwordx4 v24, s[48:51], s74 offen offset:2048 lds\n"
  "v_mfma_f32_16x16x32_bf16 v[164:167], v[212:215], a[136:139], v[164:167]\n"
  "s_waitcnt lgkmcnt(6)\n"
  "v_mfma_f32_16x16x32_bf16 v[128:131], v[216:219], a[140:143], v[128:131]\n"
  "s_waitcnt vmcnt(23)\n"
  "s_waitcnt lgkmcnt(0)\n"
  "s_barrier\n"
  "v_mfma_f32_16x16x32_bf16 v[132:135], v[216:219], a[144:147], v[132:135]\n"
  "v_add_u32 v21, 0xa000, v1\n"
  "ds_read_b64_tr_b16 v[208:209], v30 offset:16384\n"
  "v_mfma_f32_16x16x32_bf16 v[136:139], v[216:219], a[148:151], v[136:139]\n"
  "ds_read_b64_tr_b16 v[210:211], v30 offset:20480\n"
  "ds_read_b128 a[160:163], v21\n"
  "v_mfma_f32_16x16x32_bf16 v[140:143], v[216:219], a[152:155], v[140:143]\n"
  "ds_read_b128 a[164:167], v21 offset:1024\n"
  "ds_read_b128 a[168:171], v21 offset:2048\n"
  "v_mfma_f32_16x16x32_bf16 v[144:147], v[216:219], a[156:159], v[144:147]\n"
  "ds_read_b128 a[172:175], v21 offset:3072\n"
  "ds_read_b128 a[176:179], v21 offset:4096\n"
  "buffer_load_dwordx4 v25, s[48:51], s74 offen offset:3072 lds\n"
  "v_mfma_f32_16x16x32_bf16 v[148:151], v[220:223], a[140:143], v[148:151]\n"
  "ds_read_b64_tr_b16 v[212:213], v31 offset:16384\n"
  "ds_read_b64_tr_b16 v[214:215], v31 offset:20480\n"
  "s_add_u32 m0, s76, 0x20000\n"
  "s_xor_b32 s74, s80, 0x10000\n"
  "v_mfma_f32_16x16x32_bf16 v[152:155], v[220:223], a[144:147], v[152:155]\n"
  "buffer_load_dwordx4 v22, s[48:51], s74 offen lds\n"
  "v_mfma_f32_16x16x32_bf16 v[156:159], v[220:223], a[148:151], v[156:159]\n"
  "v_mfma_f32_16x16x32_bf16 v[160:163], v[220:223], a[152:155], v[160:163]\n"
  "buffer_load_dwordx4 v23, s[48:51], s74 offen offset:1024 lds\n"
  "v_mfma_f32_16x16x32_bf16 v[164:167], v[220:223], a[156:159], v[164:167]\n"
  "s_waitcnt lgkmcnt(6)\n"
  "v_mfma_f32_16x16x32_bf16 v[128:131], v[208:211], a[160:163], v[128:131]\n"
  "ds_read_b64_tr_b16 v[216:217], v30 offset:24576\n"
  "ds_read_b64_tr_b16 v[218:219], v30 offset:28672\n"
  "s_waitcnt lgkmcnt(7)\n"
  "v_mfma_f32_16x16x32_bf16 v[132:135], v[208:211], a[164:167], v[132:135]\n"
  "ds_read_b128 a[180:183], v21 offset:5120\n"
  "ds_read_b128 a[184:187], v21 offset:6144\n"
  "s_waitcnt lgkmcnt(8)\n"
  "v_mfma_f32_16x16x32_bf16 v[136:139], v[208:211], a[168:171], v[136:139]\n"
  "ds_read_b128 a[188:191], v21 offset:7168\n"
  "ds_read_b128 a[192:195], v21 offset:8192\n"
  "s_waitcnt lgkmcnt(9)\n"
  "v_mfma_f32_16x16x32_bf16 v[140:143], v[208:211], a[172:175], v[140:143]\n"
  "ds_read_b128 a[196:199], v21 offset:9216\n"
  "ds_read_b64_tr_b16 v[220:221], v31 offset:24576\n"
  "s_waitcnt lgkmcnt(10)\n"
  "v_mfma_f32_16x16x32_bf16 v[144:147], v[208:211], a[176:179], v[144:147]\n"
  "ds_read_b64_tr_b16 v[222:223], v31 offset:28672\n"
  "s_waitcnt lgkmcnt(9)\n"
  "v_mfma_f32_16x16x32_bf16 v[148:151], v[212:215], a[160:163], v[148:151]\n"
  "v_mfma_f32_16x16x32_bf16 v[152:155], v[212:215], a[164:167], v[152:155]\n"
  "buffer_load_dwordx4 v24, s[48:51], s74 offen offset:2048 lds\n"
  "v_mfma_f32_16x16x32_bf16 v[156:159], v[212:215], a[168:171], v[156:159]\n"
  "v_mfma_f32_16x16x32_bf16 v[160:163], v[212:215], a[172:175], v[160:163]\n"
  "buffer_load_dwordx4 v25, s[48:51], s74 offen offset:3072 lds\n"
  "Lh19_%=:\n"
  "s_cbranch_execz Lret_%=\n"
  "v_mfma_f32_16x16x32_bf16 v[164:167], v[212:215], a[176:179], v[164:167]\n"
  "s_waitcnt lgkmcnt(6)\n"
  "v_mfma_f32_16x16x32_bf16 v[128:131], v[216:219], a[180:183], v[128:131]\n"
  "s_waitcnt vmcnt(16)\n"
  "s_waitcnt lgkmcnt(0)\n"
  "s_barrier\n"
  "v_mfma_f32_16x16x32_bf16 v[132:135], v[216:219], a[184:187], v[132:135]\n"
  "ds_read_b64_tr_b16 v[208:209], v28 offset:16384\n"
  "ds_read_b64_tr_b16 v[210:211], v28 offset:20480\n"
  "v_mfma_f32_16x16x32_bf16 v[136:139], v[216:219], a[188:191], v[136:139]\n"
  "ds_read_b128 a[200:203], v21 offset:10240\n"
  "ds_read_b128 a[204:207], v21 offset:11264\n"
  "v_mfma_f32_16x16x32_bf16 v[140:143], v[216:219], a[192:195], v[140:143]\n"
  "ds_read_b128 a[208:211], v21 offset:12288\n"
  "ds_read_b128 a[212:215], v21 offset:13312\n"
  "s_add_u32 m0, s76, 0x4000\n"
  "s_xor_b32 s74, s80, 0x20000\n"
  "v_mfma_f32_16x16x32_bf16 v[144:147], v[216:219], a[196:199], v[144:147]\n"
  "ds_read_b128 a[216:219], v21 offset:14336\n"
  "ds_read_b64_tr_b16 v[212:213], v29 offset:16384\n"
  "buffer_load_dwordx4 v22, s[48:51], s74 offen lds\n"
  "v_mfma_f32_16x16x32_bf16 v[148:151], v[220:223], a[180:183], v[148:151]\n"
  "ds_read_b64_tr_b16 v[214:215], v29 offset:20480\n"
  "v_mfma_f32_16x16x32_bf16 v[152:155], v[220:223], a[184:187], v[152:155]\n"
  "buffer_load_dwordx4 v23, s[48:51], s74 offen offset:1024 lds\n"
  "v_mfma_f32_16x16x32_bf16 v[156:159], v[220:223], a[188:191], v[156:159]\n"
  "v_mfma_f32_16x16x32_bf16 v[160:163], v[220:223], a[192:195], v[160:163]\n"
  "buffer_load_dwordx4 v24, s[48:51], s74 offen offset:2048 lds\n"
  "v_mfma_f32_16x16x32_bf16 v[164:167], v[220:223], a[196:199], v[164:167]\n"
  "s_waitcnt lgkmcnt(6)\n"
  "v_mfma_f32_16x16x32_bf16 v[128:131], v[208:211], a[200:203], v[128:131]\n"
  "ds_read_b64_tr_b16 v[216:217], v28 offset:24576\n"
  "ds_read_b64_tr_b16 v[218:219], v28 offset:28672\n"
  "s_waitcnt lgkmcnt(7)\n"
  "v_mfma_f32_16x16x32_bf16 v[132:135], v[208:211], a[204:207], v[132:135]\n"
  "ds_read_b128 a[220:223], v21 offset:15360\n"
  "ds_read_b128 a[224:227], v21 offset:16384\n"
  "s_waitcnt lgkmcnt(8)\n"
  "v_mfma_f32_16x16x32_bf16 v[136:139], v[208:211], a[208:211], v[136:139]\n"
  "ds_read_b128 a[228:231], v21 offset:17408\n"
  "ds_read_b128 a[232:235], v21 offset:18432\n"
  "s_waitcnt lgkmcnt(9)\n"
  "v_mfma_f32_16x16x32_bf16 v[140:143], v[208:211], a[212:215], v[140:143]\n"
  "ds_read_b128 a[236:239], v21 offset:19456\n"
  "ds_read_b64_tr_b16 v[220:221], v29 offset:24576\n"
  "s_waitcnt lgkmcnt(10)\n"
  "v_mfma_f32_16x16x32_bf16 v[144:147], v[208:211], a[216:219], v[144:147]\n"
  "ds_read_b64_tr_b16 v[222:223], v29 offset:28672\n"
  "buffer_load_dwordx4 v25, s[48:51], s74 offen offset:3072 lds\n"
  "s_waitcnt lgkmcnt(9)\n"
  "v_mfma_f32_16x16x32_bf16 v[148:151], v[212:215], a[200:203], v[148:151]\n"
  "s_add_u32 m0, s76, 0x24000\n"
  "s_xor_b32 s74, s80, 0x30000\n"
  "v_mfma_f32_16x16x32_bf16 v[152:155], v[212:215], a[204:207], v[152:155]\n"
  "buffer_load_dwordx4 v22, s[48:51], s74 offen lds\n"
  "v_mfma_f32_16x16x32_bf16 v[156:159], v[212:215], a[208:211], v[156:159]\n"
  "v_mfma_f32_16x16x32_bf16 v[160:163], v[212:215], a[212:215], v[160:163]\n"
  "buffer_load_dwordx4 v23, s[48:51], s74 offen offset:1024 lds\n"
  "v_mfma_f32_16x16x32_bf16 v[164:167], v[212:215], a[216:219], v[164:167]\n"
  "s_waitcnt lgkmcnt(6)\n"
  "v_mfma_f32_16x16x32_bf16 v[128:131], v[216:219], a[220:223], v[128:131]\n"
  "s_waitcnt vmcnt(18)\n"
  "s_waitcnt lgkmcnt(0)\n"
  "s_barrier\n"
  "v_mfma_f32_16x16x32_bf16 v[132:135], v[216:219], a[224:227], v[132:135]\n"
  "v_add_u32 v21, 0xf000, v1\n"
  "ds_read_b64_tr_b16 v[208:209], v28 offset:32768\n"
  "v_mfma_f32_16x16x32_bf16 v[136:139], v[216:219], a[228:231], v[136:139]\n"
  "ds_read_b64_tr_b16 v[210:211], v28 offset:36864\n"
  "ds_read_b128 a[240:243], v21\n"
  "v_mfma_f32_16x16x32_bf16 v[140:143], v[216:219], a[232:235], v[140:143]\n"
  "Lh20_%=:\n"
  "s_cbranch_execz Lh21_%=\n"
  "ds_read_b128 a[244:247], v21 offset:1024\n"
  "ds_read_b128 a[248:251], v21 offset:2048\n"
  "v_mfma_f32_16x16x32_bf16 v[144:147], v[216:219], a[236:239], v[144:147]\n"
  "ds_read_b128 a[252:255], v21 offset:3072\n"
  "ds_read_b128 v[64:67], v21 offset:4096\n"
  "buffer_load_dwordx4 v24, s[48:51], s74 offen offset:2048 lds\n"
  "v_mfma_f32_16x16x32_bf16 v[148:151], v[220:223], a[220:223], v[148:151]\n"
  "ds_read_b64_tr_b16 v[212:213], v29 offset:32768\n"
  "ds_read_b64_tr_b16 v[214:215], v29 offset:36864\n"
  "v_mfma_f32_16x16x32_bf16 v[152:155], v[220:223], a[224:227], v[152:155]\n"
  "buffer_load_dwordx4 v25, s[48:51], s74 offen offset:3072 lds\n"
  "v_mfma_f32_16x16x32_bf16 v[156:159], v[220:223], a[228:231], v[156:159]\n"
  "s_add_u32 m0, s76, 0x14000\n"
  "s_xor_b32 s74, s80, 0x40000\n"
  "v_mfma_f32_16x16x32_bf16 v[160:163], v[220:223], a[232:235], v[160:163]\n"
  "buffer_load_dwordx4 v22, s[48:51], s74 offen lds\n"
  "v_mfma_f32_16x16x32_bf16 v[164:167], v[220:223], a[236:239], v[164:167]\n"
  "s_waitcnt lgkmcnt(6)\n"
  "v_mfma_f32_16x16x32_bf16 v[128:131], v[208:211], a[240:243], v[128:131]\n"
  "ds_read_b64_tr_b16 v[216:217], v28 offset:40960\n"
  "ds_read_b64_tr_b16 v[218:219], v28 offset:45056\n"
  "s_waitcnt lgkmcnt(7)\n"
  "v_mfma_f32_16x16x32_bf16 v[132:135], v[208:211], a[244:247], v[132:135]\n"
  "ds_read_b128 v[68:71], v21 offset:5120\n"
  "ds_read_b128 v[72:75], v21 offset:6144\n"
  "s_waitcnt lgkmcnt(8)\n"
  "v_mfma_f32_16x16x32_bf16 v[136:139], v[208:211], a[248:251], v[136:139]\n"
  "ds_read_b128 v[76:79], v21 offset:7168\n"
  "ds_read_b128 v[80:83], v21 offset:8192\n"
  "s_waitcnt lgkmcnt(9)\n"
  "v_mfma_f32_16x16x32_bf16 v[140:143], v[208:211], a[252:255], v[140:143]\n"
  "ds_read_b128 v[84:87], v21 offset:9216\n"
  "ds_read_b64_tr_b16 v[220:221], v29 offset:40960\n"
  "s_waitcnt lgkmcnt(10)\n"
  "v_mfma_f32_16x16x32_bf16 v[144:147], v[208:211], v[64:67], v[144:147]\n"
  "ds_read_b64_tr_b16 v[222:223], v29 offset:45056\n"
  "s_waitcnt lgkmcnt(9)\n"
  "v_mfma_f32_16x16x32_bf16 v[148:151], v[212:215], a[240:243], v[148:151]\n"
  "v_mfma_f32_16x16x32_bf16 v[152:155], v[212:215], a[244:247], v[152:155]\n"
  "buffer_load_dwordx4 v23, s[48:51], s74 offen offset:1024 lds\n"
  "v_mfma_f32_16x16x32_bf16 v[156:159], v[212:215], a[248:251], v[156:159]\n"
  "v_mfma_f32_16x16x32_bf16 v[160:163], v[212:215], a[252:255], v[160:163]\n"
  "buffer_load_dwordx4 v24, s[48:51], s74 offen offset:2048 lds\n"
  "v_mfma_f32_16x16x32_bf16 v[164:167], v[212:215], v[64:67], v[164:167]\n"
  "s_waitcnt lgkmcnt(6)\n"
  "v_mfma_f32_16x16x32_bf16 v[128:131], v[216:219], v[68:71], v[128:131]\n"
  "s_waitcnt vmcnt(19)\n"
  "s_waitcnt lgkmcnt(0)\n"
  "s_barrier\n"
  "v_mfma_f32_16x16x32_bf16 v[132:135], v[216:219], v[72:75], v[132:135]\n"
  "ds_read_b64_tr_b16 v[208:209], v26\n"
  "ds_read_b64_tr_b16 v[210:211], v26 offset:4096\n"
  "v_mfma_f32_16x16x32_bf16 v[136:139], v[216:219], v[76:79], v[136:139]\n"
  "ds_read_b128 v[88:91], v21 offset:10240\n"
  "ds_read_b128 v[92:95], v21 offset:11264\n"
  "v_mfma_f32_16x16x32_bf16 v[140:143], v[216:219], v[80:83], v[140:143]\n"
  "ds_read_b128 v[96:99], v21 offset:12288\n"
  "ds_read_b128 v[100:103], v21 offset:13312\n"
  "v_mfma_f32_16x16x32_bf16 v[144:147], v[216:219], v[84:87], v[144:147]\n"
  "ds_read_b128 v[104:107], v21 offset:14336\n"
  "ds_read_b64_tr_b16 v[212:213], v27\n"
  "buffer_load_dwordx4 v25, s[48:51], s74 offen offset:3072 lds\n"
  "v_mfma_f32_16x16x32_bf16 v[148:151], v[220:223], v[68:71], v[148:151]\n"
  "ds_read_b64_tr_b16 v[214:215], v27 offset:4096\n"
  "s_add_u32 m0, s76, 0x8000\n"
  "s_xor_b32 s74, s80, 0x50000\n"
  "v_mfma_f32_16x16x32_bf16 v[152:155], v[220:223], v[72:75], v[152:155]\n"
  "buffer_load_dwordx4 v22, s[48:51], s74 offen lds\n"
  "Lh21_%=:\n"
  "s_cbranch_execz Lh22_%=\n"
  "v_mfma_f32_16x16x32_bf16 v[156:159], v[220:223], v[76:79], v[156:159]\n"
  "v_mfma_f32_16x16x32_bf16 v[160:163], v[220:223], v[80:83], v[160:163]\n"
  "buffer_load_dwordx4 v23, s[48:51], s74 offen offset:1024 lds\n"
  "v_mfma_f32_16x16x32_bf16 v[164:167], v[220:223], v[84:87], v[164:167]\n"
  "s_waitcnt lgkmcnt(6)\n"
  "v_mfma_f32_16x16x32_bf16 v[128:131], v[208:211], v[88:91], v[128:131]\n"
  "ds_read_b64_tr_b16 v[216:217], v26 offset:8192\n"
  "ds_read_b64_tr_b16 v[218:219], v26 offset:12288\n"
  "s_waitcnt lgkmcnt(7)\n"
  "v_mfma_f32_16x16x32_bf16 v[132:135], v[208:211], v[92:95], v[132:135]\n"
  "ds_read_b128 v[108:111], v21 offset:15360\n"
  "ds_read_b128 v[112:115], v21 offset:16384\n"
  "s_waitcnt lgkmcnt(8)\n"
  "v_mfma_f32_16x16x32_bf16 v[136:139], v[208:211], v[96:99], v[136:139]\n"
  "ds_read_b128 v[116:119], v21 offset:17408\n"
  "ds_read_b128 v[120:123], v21 offset:18432\n"
  "s_waitcnt lgkmcnt(9)\n"
  "v_mfma_f32_16x16x32_bf16 v[140:143], v[208:211], v[100:103], v[140:143]\n"
  "ds_read_b128 v[124:127], v21 offset:19456\n"
  "ds_read_b64_tr_b16 v[220:221], v27 offset:8192\n"
  "s_waitcnt lgkmcnt(10)\n"
  "v_mfma_f32_16x16x32_bf16 v[144:147], v[208:211], v[104:107], v[144:147]\n"
  "ds_read_b64_tr_b16 v[222:223], v27 offset:12288\n"
  "buffer_load_dwordx4 v24, s[48:51], s74 offen offset:2048 lds\n"
  "s_waitcnt lgkmcnt(9)\n"
  "v_mfma_f32_16x16x32_bf16 v[148:151], v[212:215], v[88:91], v[148:151]\n"
  "v_mfma_f32_16x16x32_bf16 v[152:155], v[212:215], v[92:95], v[152:155]\n"
  "buffer_load_dwordx4 v25, s[48:51], s74 offen offset:3072 lds\n"
  "v_mfma_f32_16x16x32_bf16 v[156:159], v[212:215], v[96:99], v[156:159]\n"
  "s_add_u32 m0, s76, 0x18000\n"
  "s_xor_b32 s74, s80, 0x60000\n"
  "v_mfma_f32_16x16x32_bf16 v[160:163], v[212:215], v[100:103], v[160:163]\n"
  "buffer_load_dwordx4 v22, s[48:51], s74 offen lds\n"
  "v_mfma_f32_16x16x32_bf16 v[164:167], v[212:215], v[104:107], v[164:167]\n"
  "s_waitcnt lgkmcnt(6)\n"
  "v_mfma_f32_16x16x32_bf16 v[128:131], v[216:219], v[108:111], v[128:131]\n"
  "s_waitcnt vmcnt(17)\n"
  "s_waitcnt lgkmcnt(0)\n"
  "s_barrier\n"
  "v_mfma_f32_16x16x32_bf16 v[132:135], v[216:219], v[112:115], v[132:135]\n"
  "ds_read_b64_tr_b16 v[208:209], v28 offset:49152\n"
  "ds_read_b64_tr_b16 v[210:211], v28 offset:53248\n"
  "v_mfma_f32_16x16x32_bf16 v[136:139], v[216:219], v[116:119], v[136:139]\n"
  "ds_read_b64_tr_b16 v[212:213], v29 offset:49152\n"
  "ds_read_b64_tr_b16 v[214:215], v29 offset:53248\n"
  "v_mfma_f32_16x16x32_bf16 v[140:143], v[216:219], v[120:123], v[140:143]\n"
  "buffer_load_dwordx4 v23, s[48:51], s74 offen offset:1024 lds\n"
  "v_mfma_f32_16x16x32_bf16 v[144:147], v[216:219], v[124:127], v[144:147]\n"
  "v_mfma_f32_16x16x32_bf16 v[148:151], v[220:223], v[108:111], v[148:151]\n"
  "v_mfma_f32_16x16x32_bf16 v[152:155], v[220:223], v[112:115], v[152:155]\n"
  "buffer_load_dwordx4 v24, s[48:51], s74 offen offset:2048 lds\n"
  "v_mfma_f32_16x16x32_bf16 v[156:159], v[220:223], v[116:119], v[156:159]\n"
  "v_mfma_f32_16x16x32_bf16 v[160:163], v[220:223], v[120:123], v[160:163]\n"
  "v_mfma_f32_16x16x32_bf16 v[164:167], v[220:223], v[124:127], v[164:167]\n"
  "s_waitcnt lgkmcnt(2)\n"
  "v_mfma_f32_16x16x32_bf16 v[168:171], v[208:211], a[0:3], 0\n"
  "ds_read_b64_tr_b16 v[216:217], v28 offset:57344\n"
  "ds_read_b64_tr_b16 v[218:219], v28 offset:61440\n"
  "v_mfma_f32_16x16x32_bf16 v[172:175], v[208:211], a[4:7], 0\n"
  "ds_read_b64_tr_b16 v[220:221], v29 offset:57344\n"
  "ds_read_b64_tr_b16 v[222:223], v29 offset:61440\n"
  "v_mfma_f32_16x16x32_bf16 v[176:179], v[208:211], a[8:11], 0\n"
  "v_cvt_pk_bf16_f32 v128, v128, v129\n"
  "v_cvt_pk_bf16_f32 v129, v130, v131\n"
  "v_mfma_f32_16x16x32_bf16 v[180:183], v[208:211], a[12:15], 0\n"
  "buffer_load_dwordx4 v25, s[48:51], s74 offen offset:3072 lds\n"
  "v_cvt_pk_bf16_f32 v130, v132, v133\n"
  "v_cvt_pk_bf16_f32 v131, v134, v135\n"
  "v_mfma_f32_16x16x32_bf16 v[184:187], v[208:211], a[16:19], 0\n"
  "Lh22_%=:\n"
  "s_cbranch_execz Lh23_%=\n"
  "buffer_store_dwordx4 v[128:131], v32, s[52:55], s16 offen sc1\n"
  "s_waitcnt lgkmcnt(4)\n"
  "v_mfma_f32_16x16x32_bf16 v[188:191], v[212:215], a[0:3], 0\n"
  "v_cvt_pk_bf16_f32 v136, v136, v137\n"
  "v_cvt_pk_bf16_f32 v137, v138, v139\n"
  "v_mfma_f32_16x16x32_bf16 v[192:195], v[212:215], a[4:7], 0\n"
  "v_cvt_pk_bf16_f32 v138, v140, v141\n"
  "v_cvt_pk_bf16_f32 v139, v142, v143\n"
  "v_mfma_f32_16x16x32_bf16 v[196:199], v[212:215], a[8:11], 0\n"
  "buffer_store_dwordx4 v[136:139], v32, s[52:55], s16 offen offset:1024 sc1\n"
  "v_mfma_f32_16x16x32_bf16 v[200:203], v[212:215], a[12:15], 0\n"
  "v_mfma_f32_16x16x32_bf16 v[204:207], v[212:215], a[16:19], 0\n"
  "s_waitcnt lgkmcnt(2)\n"
  "v_mfma_f32_16x16x32_bf16 v[168:171], v[216:219], a[20:23], v[168:171]\n"
  "ds_read_b64_tr_b16 v[208:209], v30\n"
  "ds_read_b64_tr_b16 v[210:211], v30 offset:4096\n"
  "v_mfma_f32_16x16x32_bf16 v[172:175], v[216:219], a[24:27], v[172:175]\n"
  "ds_read_b64_tr_b16 v[212:213], v31\n"
  "ds_read_b64_tr_b16 v[214:215], v31 offset:4096\n"
  "v_mfma_f32_16x16x32_bf16 v[176:179], v[216:219], a[28:31], v[176:179]\n"
  "s_add_u32 m0, s76, 0\n"
  "s_xor_b32 s74, s80, 0x70000\n"
  "v_cvt_pk_bf16_f32 v148, v148, v149\n"
  "v_cvt_pk_bf16_f32 v149, v150, v151\n"
  "v_mfma_f32_16x16x32_bf16 v[180:183], v[216:219], a[32:35], v[180:183]\n"
  "buffer_load_dwordx4 v22, s[48:51], s74 offen lds\n"
  "v_cvt_pk_bf16_f32 v150, v152, v153\n"
  "v_cvt_pk_bf16_f32 v151, v154, v155\n"
  "v_mfma_f32_16x16x32_bf16 v[184:187], v[216:219], a[36:39], v[184:187]\n"
  "s_add_u32 s78, s16, 0x14000\n"
  "buffer_store_dwordx4 v[148:151], v32, s[52:55], s78 offen sc1\n"
  "s_waitcnt lgkmcnt(4)\n"
  "v_mfma_f32_16x16x32_bf16 v[188:191], v[220:223], a[20:23], v[188:191]\n"
  "v_cvt_pk_bf16_f32 v156, v156, v157\n"
  "v_cvt_pk_bf16_f32 v157, v158, v159\n"
  "v_mfma_f32_16x16x32_bf16 v[192:195], v[220:223], a[24:27], v[192:195]\n"
  "v_cvt_pk_bf16_f32 v158, v160, v161\n"
  "v_cvt_pk_bf16_f32 v159, v162, v163\n"
  "v_mfma_f32_16x16x32_bf16 v[196:199], v[220:223], a[28:31], v[196:199]\n"
  "buffer_store_dwordx4 v[156:159], v32, s[52:55], s78 offen offset:1024 sc1\n"
  "v_cvt_pk_bf16_f32 v144, v144, v145\n"
  "v_cvt_pk_bf16_f32 v145, v146, v147\n"
  "v_cvt_pk_bf16_f32 v146, v164, v165\n"
  "v_cvt_pk_bf16_f32 v147, v166, v167\n"
  "v_mfma_f32_16x16x32_bf16 v[200:203], v[220:223], a[32:35], v[200:203]\n"
  "v_permlane32_swap_b32 v144, v146\n"
  "v_permlane32_swap_b32 v145, v147\n"
  "v_mfma_f32_16x16x32_bf16 v[204:207], v[220:223], a[36:39], v[204:207]\n"
  "buffer_store_dwordx4 v[144:147], v38, s[52:55], s16 offen sc1\n"
  "s_waitcnt lgkmcnt(2)\n"
  "v_mfma_f32_16x16x32_bf16 v[168:171], v[208:211], a[40:43], v[168:171]\n"
  "ds_read_b64_tr_b16 v[216:217], v30 offset:8192\n"
  "ds_read_b64_tr_b16 v[218:219], v30 offset:12288\n"
  "v_mfma_f32_16x16x32_bf16 v[172:175], v[208:211], a[44:47], v[172:175]\n"
  "ds_read_b64_tr_b16 v[220:221], v31 offset:8192\n"
  "ds_read_b64_tr_b16 v[222:223], v31 offset:12288\n"
  "v_mfma_f32_16x16x32_bf16 v[176:179], v[208:211], a[48:51], v[176:179]\n"
  "v_mfma_f32_16x16x32_bf16 v[180:183], v[208:211], a[52:55], v[180:183]\n"
  "buffer_load_dwordx4 v23, s[48:51], s74 offen offset:1024 lds\n"
  "v_mfma_f32_16x16x32_bf16 v[184:187], v[208:211], a[56:59], v[184:187]\n"
  "s_waitcnt lgkmcnt(4)\n"
  "v_mfma_f32_16x16x32_bf16 v[188:191], v[212:215], a[40:43], v[188:191]\n"
  "v_mfma_f32_16x16x32_bf16 v[192:195], v[212:215], a[44:47], v[192:195]\n"
  "buffer_load_dwordx4 v24, s[48:51], s74 offen offset:2048 lds\n"
  "v_mfma_f32_16x16x32_bf16 v[196:199], v[212:215], a[48:51], v[196:199]\n"
  "v_mfma_f32_16x16x32_bf16 v[200:203], v[212:215], a[52:55], v[200:203]\n"
  "v_mfma_f32_16x16x32_bf16 v[204:207], v[212:215], a[56:59], v[204:207]\n"
  "s_waitcnt lgkmcnt(2)\n"
  "Lh23_%=:\n"
  "s_cbranch_execz Lh24_%=\n"
  "v_mfma_f32_16x16x32_bf16 v[168:171], v[216:219], a[60:63], v[168:171]\n"
  "s_waitcnt vmcnt(20)\n"
  "s_waitcnt lgkmcnt(0)\n"
  "s_barrier\n"
  "v_mfma_f32_16x16x32_bf16 v[172:175], v[216:219], a[64:67], v[172:175]\n"
  "ds_read_b64_tr_b16 v[208:209], v26 offset:16384\n"
  "ds_read_b64_tr_b16 v[210:211], v26 offset:20480\n"
  "v_mfma_f32_16x16x32_bf16 v[176:179], v[216:219], a[68:71], v[176:179]\n"
  "ds_read_b64_tr_b16 v[212:213], v27 offset:16384\n"
  "ds_read_b64_tr_b16 v[214:215], v27 offset:20480\n"
  "v_mfma_f32_16x16x32_bf16 v[180:183], v[216:219], a[72:75], v[180:183]\n"
  "buffer_load_dwordx4 v25, s[48:51], s74 offen offset:3072 lds\n"
  "v_mfma_f32_16x16x32_bf16 v[184:187], v[216:219], a[76:79], v[184:187]\n"
  "v_mfma_f32_16x16x32_bf16 v[188:191], v[220:223], a[60:63], v[188:191]\n"
  "v_mfma_f32_16x16x32_bf16 v[192:195], v[220:223], a[64:67], v[192:195]\n"
  "v_mfma_f32_16x16x32_bf16 v[196:199], v[220:223], a[68:71], v[196:199]\n"
  "v_mfma_f32_16x16x32_bf16 v[200:203], v[220:223], a[72:75], v[200:203]\n"
  "v_mfma_f32_16x16x32_bf16 v[204:207], v[220:223], a[76:79], v[204:207]\n"
  "s_waitcnt lgkmcnt(2)\n"
  "v_mfma_f32_16x16x32_bf16 v[168:171], v[208:211], a[80:83], v[168:171]\n"
  "ds_read_b64_tr_b16 v[216:217], v26 offset:24576\n"
  "ds_read_b64_tr_b16 v[218:219], v26 offset:28672\n"
  "v_mfma_f32_16x16x32_bf16 v[172:175], v[208:211], a[84:87], v[172:175]\n"
  "ds_read_b64_tr_b16 v[220:221], v27 offset:24576\n"
  "ds_read_b64_tr_b16 v[222:223], v27 offset:28672\n"
  "v_mfma_f32_16x16x32_bf16 v[176:179], v[208:211], a[88:91], v[176:179]\n"
  "s_add_u32 m0, s76, 0xc000\n"
  "s_xor_b32 s74, s81, 0\n"
  "v_mfma_f32_16x16x32_bf16 v[180:183], v[208:211], a[92:95], v[180:183]\n"
  "buffer_load_dwordx4 v22, s[48:51], s74 offen lds\n"
  "v_mfma_f32_16x16x32_bf16 v[184:187], v[208:211], a[96:99], v[184:187]\n"
  "s_waitcnt lgkmcnt(4)\n"
  "v_mfma_f32_16x16x32_bf16 v[188:191], v[212:215], a[80:83], v[188:191]\n"
  "v_mfma_f32_16x16x32_bf16 v[192:195], v[212:215], a[84:87], v[192:195]\n"
  "v_mfma_f32_16x16x32_bf16 v[196:199], v[212:215], a[88:91], v[196:199]\n"
  "v_mfma_f32_16x16x32_bf16 v[200:203], v[212:215], a[92:95], v[200:203]\n"
  "v_mfma_f32_16x16x32_bf16 v[204:207], v[212:215], a[96:99], v[204:207]\n"
  "s_waitcnt lgkmcnt(2)\n"
  "v_mfma_f32_16x16x32_bf16 v[168:171], v[216:219], a[100:103], v[168:171]\n"
  "ds_read_b64_tr_b16 v[208:209], v30 offset:16384\n"
  "ds_read_b64_tr_b16 v[210:211], v30 offset:20480\n"
  "v_mfma_f32_16x16x32_bf16 v[172:175], v[216:219], a[104:107], v[172:175]\n"
  "ds_read_b64_tr_b16 v[212:213], v31 offset:16384\n"
  "ds_read_b64_tr_b16 v[214:215], v31 offset:20480\n"
  "v_mfma_f32_16x16x32_bf16 v[176:179], v[216:219], a[108:111], v[176:179]\n"
  "v_mfma_f32_16x16x32_bf16 v[180:183], v[216:219], a[112:115], v[180:183]\n"
  "buffer_load_dwordx4 v23, s[48:51], s74 offen offset:1024 lds\n"
  "v_mfma_f32_16x16x32_bf16 v[184:187], v[216:219], a[116:119], v[184:187]\n"
  "s_waitcnt lgkmcnt(4)\n"
  "v_mfma_f32_16x16x32_bf16 v[188:191], v[220:223], a[100:103], v[188:191]\n"
  "v_mfma_f32_16x16x32_bf16 v[192:195], v[220:223], a[104:107], v[192:195]\n"
  "v_mfma_f32_16x16x32_bf16 v[196:199], v[220:223], a[108:111], v[196:199]\n"
  "v_mfma_f32_16x16x32_bf16 v[200:203], v[220:223], a[112:115], v[200:203]\n"
  "v_mfma_f32_16x16x32_bf16 v[204:207], v[220:223], a[116:119], v[204:207]\n"
  "s_waitcnt lgkmcnt(2)\n"
  "v_mfma_f32_16x16x32_bf16 v[168:171], v[208:211], a[120:123], v[168:171]\n"
  "ds_read_b64_tr_b16 v[216:217], v30 offset:24576\n"
  "ds_read_b64_tr_b16 v[218:219], v30 offset:28672\n"
  "v_mfma_f32_16x16x32_bf16 v[172:175], v[208:211], a[124:127], v[172:175]\n"
  "ds_read_b64_tr_b16 v[220:221], v31 offset:24576\n"
  "ds_read_b64_tr_b16 v[222:223], v31 offset:28672\n"
  "v_mfma_f32_16x16x32_bf16 v[176:179], v[208:211], a[128:131], v[176:179]\n"
  "v_mfma_f32_16x16x32_bf16 v[180:183], v[208:211], a[132:135], v[180:183]\n"
  "buffer_load_dwordx4 v24, s[48:51], s74 offen offset:2048 lds\n"
  "v_mfma_f32_16x16x32_bf16 v[184:187], v[208:211], a[136:139], v[184:187]\n"
  "s_waitcnt lgkmcnt(4)\n"
  "v_mfma_f32_16x16x32_bf16 v[188:191], v[212:215], a[120:123], v[188:191]\n"
  "v_mfma_f32_16x16x32_bf16 v[192:195], v[212:215], a[124:127], v[192:195]\n"
  "buffer_load_dwordx4 v25, s[48:51], s74 offen offset:3072 lds\n"
  "Lh24_%=:\n"
  "s_cbranch_execz Lh25_%=\n"
  "v_mfma_f32_16x16x32_bf16 v[196:199], v[212:215], a[128:131], v[196:199]\n"
  "v_mfma_f32_16x16x32_bf16 v[200:203], v[212:215], a[132:135], v[200:203]\n"
  "v_mfma_f32_16x16x32_bf16 v[204:207], v[212:215], a[136:139], v[204:207]\n"
  "s_waitcnt lgkmcnt(2)\n"
  "v_mfma_f32_16x16x32_bf16 v[168:171], v[216:219], a[140:143], v[168:171]\n"
  "s_waitcnt vmcnt(17)\n"
  "s_waitcnt lgkmcnt(0)\n"
  "s_barrier\n"
  "v_mfma_f32_16x16x32_bf16 v[172:175], v[216:219], a[144:147], v[172:175]\n"
  "ds_read_b64_tr_b16 v[208:209], v28 offset:16384\n"
  "ds_read_b64_tr_b16 v[210:211], v28 offset:20480\n"
  "v_mfma_f32_16x16x32_bf16 v[176:179], v[216:219], a[148:151], v[176:179]\n"
  "ds_read_b64_tr_b16 v[212:213], v29 offset:16384\n"
  "ds_read_b64_tr_b16 v[214:215], v29 offset:20480\n"
  "s_add_u32 m0, s76, 0x10000\n"
  "s_xor_b32 s74, s81, 0x10000\n"
  "v_mfma_f32_16x16x32_bf16 v[180:183], v[216:219], a[152:155], v[180:183]\n"
  "buffer_load_dwordx4 v22, s[48:51], s74 offen lds\n"
  "v_mfma_f32_16x16x32_bf16 v[184:187], v[216:219], a[156:159], v[184:187]\n"
  "v_mfma_f32_16x16x32_bf16 v[188:191], v[220:223], a[140:143], v[188:191]\n"
  "v_mfma_f32_16x16x32_bf16 v[192:195], v[220:223], a[144:147], v[192:195]\n"
  "buffer_load_dwordx4 v23, s[48:51], s74 offen offset:1024 lds\n"
  "v_mfma_f32_16x16x32_bf16 v[196:199], v[220:223], a[148:151], v[196:199]\n"
  "v_mfma_f32_16x16x32_bf16 v[200:203], v[220:223], a[152:155], v[200:203]\n"
  "buffer_load_dwordx4 v24, s[48:51], s74 offen offset:2048 lds\n"
  "v_mfma_f32_16x16x32_bf16 v[204:207], v[220:223], a[156:159], v[204:207]\n"
  "s_waitcnt lgkmcnt(2)\n"
  "v_mfma_f32_16x16x32_bf16 v[168:171], v[208:211], a[160:163], v[168:171]\n"
  "ds_read_b64_tr_b16 v[216:217], v28 offset:24576\n"
  "ds_read_b64_tr_b16 v[218:219], v28 offset:28672\n"
  "v_mfma_f32_16x16x32_bf16 v[172:175], v[208:211], a[164:167], v[172:175]\n"
  "ds_read_b64_tr_b16 v[220:221], v29 offset:24576\n"
  "ds_read_b64_tr_b16 v[222:223], v29 offset:28672\n"
  "v_mfma_f32_16x16x32_bf16 v[176:179], v[208:211], a[168:171], v[176:179]\n"
  "v_mfma_f32_16x16x32_bf16 v[180:183], v[208:211], a[172:175], v[180:183]\n"
  "buffer_load_dwordx4 v25, s[48:51], s74 offen offset:3072 lds\n"
  "v_mfma_f32_16x16x32_bf16 v[184:187], v[208:211], a[176:179], v[184:187]\n"
  "s_waitcnt lgkmcnt(4)\n"
  "v_mfma_f32_16x16x32_bf16 v[188:191], v[212:215], a[160:163], v[188:191]\n"
  "s_add_u32 m0, s76, 0x1c000\n"
  "s_xor_b32 s74, s81, 0x20000\n"
  "v_mfma_f32_16x16x32_bf16 v[192:195], v[212:215], a[164:167], v[192:195]\n"
  "buffer_load_dwordx4 v22, s[48:51], s74 offen lds\n"
  "v_mfma_f32_16x16x32_bf16 v[196:199], v[212:215], a[168:171], v[196:199]\n"
  "v_mfma_f32_16x16x32_bf16 v[200:203], v[212:215], a[172:175], v[200:203]\n"
  "v_mfma_f32_16x16x32_bf16 v[204:207], v[212:215], a[176:179], v[204:207]\n"
  "s_waitcnt lgkmcnt(2)\n"
  "v_mfma_f32_16x16x32_bf16 v[168:171], v[216:219], a[180:183], v[168:171]\n"
  "ds_read_b64_tr_b16 v[208:209], v26 offset:32768\n"
  "ds_read_b64_tr_b16 v[210:211], v26 offset:36864\n"
  "v_mfma_f32_16x16x32_bf16 v[172:175], v[216:219], a[184:187], v[172:175]\n"
  "ds_read_b64_tr_b16 v[212:213], v27 offset:32768\n"
  "ds_read_b64_tr_b16 v[214:215], v27 offset:36864\n"
  "v_mfma_f32_16x16x32_bf16 v[176:179], v[216:219], a[188:191], v[176:179]\n"
  "v_mfma_f32_16x16x32_bf16 v[180:183], v[216:219], a[192:195], v[180:183]\n"
  "buffer_load_dwordx4 v23, s[48:51], s74 offen offset:1024 lds\n"
  "v_mfma_f32_16x16x32_bf16 v[184:187], v[216:219], a[196:199], v[184:187]\n"
  "s_waitcnt lgkmcnt(4)\n"
  "v_mfma_f32_16x16x32_bf16 v[188:191], v[220:223], a[180:183], v[188:191]\n"
  "v_mfma_f32_16x16x32_bf16 v[192:195], v[220:223], a[184:187], v[192:195]\n"
  "buffer_load_dwordx4 v24, s[48:51], s74 offen offset:2048 lds\n"
  "v_mfma_f32_16x16x32_bf16 v[196:199], v[220:223], a[188:191], v[196:199]\n"
  "v_mfma_f32_16x16x32_bf16 v[200:203], v[220:223], a[192:195], v[200:203]\n"
  "buffer_load_dwordx4 v25, s[48:51], s74 offen offset:3072 lds\n"
  "v_mfma_f32_16x16x32_bf16 v[204:207], v[220:223], a[196:199], v[204:207]\n"
  "s_waitcnt lgkmcnt(2)\n"
  "v_mfma_f32_16x16x32_bf16 v[168:171], v[208:211], a[200:203], v[168:171]\n"
  "ds_read_b64_tr_b16 v[216:217], v26 offset:40960\n"
  "Lh25_%=:\n"
  "s_cbranch_execz Lh26_%=\n"
  "ds_read_b64_tr_b16 v[218:219], v26 offset:45056\n"
  "v_mfma_f32_16x16x32_bf16 v[172:175], v[208:211], a[204:207], v[172:175]\n"
  "ds_read_b64_tr_b16 v[220:221], v27 offset:40960\n"
  "ds_read_b64_tr_b16 v[222:223], v27 offset:45056\n"
  "v_mfma_f32_16x16x32_bf16 v[176:179], v[208:211], a[208:211], v[176:179]\n"
  "s_add_u32 m0, s76, 0x20000\n"
  "s_xor_b32 s74, s81, 0x30000\n"
  "v_mfma_f32_16x16x32_bf16 v[180:183], v[208:211], a[212:215], v[180:183]\n"
  "buffer_load_dwordx4 v22, s[48:51], s74 offen lds\n"
  "v_mfma_f32_16x16x32_bf16 v[184:187], v[208:211], a[216:219], v[184:187]\n"
  "s_waitcnt lgkmcnt(4)\n"
  "v_mfma_f32_16x16x32_bf16 v[188:191], v[212:215], a[200:203], v[188:191]\n"
  "v_mfma_f32_16x16x32_bf16 v[192:195], v[212:215], a[204:207], v[192:195]\n"
  "buffer_load_dwordx4 v23, s[48:51], s74 offen offset:1024 lds\n"
  "v_mfma_f32_16x16x32_bf16 v[196:199], v[212:215], a[208:211], v[196:199]\n"
  "v_mfma_f32_16x16x32_bf16 v[200:203], v[212:215], a[212:215], v[200:203]\n"
  "v_mfma_f32_16x16x32_bf16 v[204:207], v[212:215], a[216:219], v[204:207]\n"
  "s_waitcnt lgkmcnt(2)\n"
  "v_mfma_f32_16x16x32_bf16 v[168:171], v[216:219], a[220:223], v[168:171]\n"
  "s_waitcnt vmcnt(14)\n"
  "s_waitcnt lgkmcnt(0)\n"
  "s_barrier\n"
  "v_mfma_f32_16x16x32_bf16 v[172:175], v[216:219], a[224:227], v[172:175]\n"
  "ds_read_b64_tr_b16 v[208:209], v28 offset:32768\n"
  "ds_read_b64_tr_b16 v[210:211], v28 offset:36864\n"
  "v_mfma_f32_16x16x32_bf16 v[176:179], v[216:219], a[228:231], v[176:179]\n"
  "ds_read_b64_tr_b16 v[212:213], v29 offset:32768\n"
  "ds_read_b64_tr_b16 v[214:215], v29 offset:36864\n"
  "v_mfma_f32_16x16x32_bf16 v[180:183], v[216:219], a[232:235], v[180:183]\n"
  "buffer_load_dwordx4 v24, s[48:51], s74 offen offset:2048 lds\n"
  "v_mfma_f32_16x16x32_bf16 v[184:187], v[216:219], a[236:239], v[184:187]\n"
  "v_mfma_f32_16x16x32_bf16 v[188:191], v[220:223], a[220:223], v[188:191]\n"
  "v_mfma_f32_16x16x32_bf16 v[192:195], v[220:223], a[224:227], v[192:195]\n"
  "buffer_load_dwordx4 v25, s[48:51], s74 offen offset:3072 lds\n"
  "v_mfma_f32_16x16x32_bf16 v[196:199], v[220:223], a[228:231], v[196:199]\n"
  "v_mfma_f32_16x16x32_bf16 v[200:203], v[220:223], a[232:235], v[200:203]\n"
  "v_mfma_f32_16x16x32_bf16 v[204:207], v[220:223], a[236:239], v[204:207]\n"
  "s_waitcnt lgkmcnt(2)\n"
  "v_mfma_f32_16x16x32_bf16 v[168:171], v[208:211], a[240:243], v[168:171]\n"
  "ds_read_b64_tr_b16 v[216:217], v28 offset:40960\n"
  "ds_read_b64_tr_b16 v[218:219], v28 offset:45056\n"
  "v_mfma_f32_16x16x32_bf16 v[172:175], v[208:211], a[244:247], v[172:175]\n"
  "ds_read_b64_tr_b16 v[220:221], v29 offset:40960\n"
  "ds_read_b64_tr_b16 v[222:223], v29 offset:45056\n"
  "v_mfma_f32_16x16x32_bf16 v[176:179], v[208:211], a[248:251], v[176:179]\n"
  "s_add_u32 m0, s76, 0x4000\n"
  "s_xor_b32 s74, s81, 0x40000\n"
  "v_mfma_f32_16x16x32_bf16 v[180:183], v[208:211], a[252:255], v[180:183]\n"
  "buffer_load_dwordx4 v22, s[48:51], s74 offen lds\n"
  "v_mfma_f32_16x16x32_bf16 v[184:187], v[208:211], v[64:67], v[184:187]\n"
  "s_waitcnt lgkmcnt(4)\n"
  "v_mfma_f32_16x16x32_bf16 v[188:191], v[212:215], a[240:243], v[188:191]\n"
  "v_mfma_f32_16x16x32_bf16 v[192:195], v[212:215], a[244:247], v[192:195]\n"
  "buffer_load_dwordx4 v23, s[48:51], s74 offen offset:1024 lds\n"
  "v_mfma_f32_16x16x32_bf16 v[196:199], v[212:215], a[248:251], v[196:199]\n"
  "v_mfma_f32_16x16x32_bf16 v[200:203], v[212:215], a[252:255], v[200:203]\n"
  "buffer_load_dwordx4 v24, s[48:51], s74 offen offset:2048 lds\n"
  "v_mfma_f32_16x16x32_bf16 v[204:207], v[212:215], v[64:67], v[204:207]\n"
  "s_waitcnt lgkmcnt(2)\n"
  "v_mfma_f32_16x16x32_bf16 v[168:171], v[216:219], v[68:71], v[168:171]\n"
  "ds_read_b64_tr_b16 v[208:209], v26\n"
  "ds_read_b64_tr_b16 v[210:211], v26 offset:4096\n"
  "v_mfma_f32_16x16x32_bf16 v[172:175], v[216:219], v[72:75], v[172:175]\n"
  "ds_read_b64_tr_b16 v[212:213], v27\n"
  "ds_read_b64_tr_b16 v[214:215], v27 offset:4096\n"
  "v_mfma_f32_16x16x32_bf16 v[176:179], v[216:219], v[76:79], v[176:179]\n"
  "v_mfma_f32_16x16x32_bf16 v[180:183], v[216:219], v[80:83], v[180:183]\n"
  "Lh26_%=:\n"
  "s_cbranch_execz Lh27_%=\n"
  "buffer_load_dwordx4 v25, s[48:51], s74 offen offset:3072 lds\n"
  "v_mfma_f32_16x16x32_bf16 v[184:187], v[216:219], v[84:87], v[184:187]\n"
  "s_waitcnt lgkmcnt(4)\n"
  "v_mfma_f32_16x16x32_bf16 v[188:191], v[220:223], v[68:71], v[188:191]\n"
  "s_add_u32 m0, s76, 0x24000\n"
  "s_xor_b32 s74, s81, 0x50000\n"
  "v_mfma_f32_16x16x32_bf16 v[192:195], v[220:223], v[72:75], v[192:195]\n"
  "buffer_load_dwordx4 v22, s[48:51], s74 offen lds\n"
  "v_mfma_f32_16x16x32_bf16 v[196:199], v[220:223], v[76:79], v[196:199]\n"
  "v_mfma_f32_16x16x32_bf16 v[200:203], v[220:223], v[80:83], v[200:203]\n"
  "v_mfma_f32_16x16x32_bf16 v[204:207], v[220:223], v[84:87], v[204:207]\n"
  "s_waitcnt lgkmcnt(2)\n"
  "v_mfma_f32_16x16x32_bf16 v[168:171], v[208:211], v[88:91], v[168:171]\n"
  "ds_read_b64_tr_b16 v[216:217], v26 offset:8192\n"
  "ds_read_b64_tr_b16 v[218:219], v26 offset:12288\n"
  "v_mfma_f32_16x16x32_bf16 v[172:175], v[208:211], v[92:95], v[172:175]\n"
  "ds_read_b64_tr_b16 v[220:221], v27 offset:8192\n"
  "ds_read_b64_tr_b16 v[222:223], v27 offset:12288\n"
  "v_mfma_f32_16x16x32_bf16 v[176:179], v[208:211], v[96:99], v[176:179]\n"
  "v_mfma_f32_16x16x32_bf16 v[180:183], v[208:211], v[100:103], v[180:183]\n"
  "buffer_load_dwordx4 v23, s[48:51], s74 offen offset:1024 lds\n"
  "v_mfma_f32_16x16x32_bf16 v[184:187], v[208:211], v[104:107], v[184:187]\n"
  "s_waitcnt lgkmcnt(4)\n"
  "v_mfma_f32_16x16x32_bf16 v[188:191], v[212:215], v[88:91], v[188:191]\n"
  "v_mfma_f32_16x16x32_bf16 v[192:195], v[212:215], v[92:95], v[192:195]\n"
  "buffer_load_dwordx4 v24, s[48:51], s74 offen offset:2048 lds\n"
  "v_mfma_f32_16x16x32_bf16 v[196:199], v[212:215], v[96:99], v[196:199]\n"
  "v_mfma_f32_16x16x32_bf16 v[200:203], v[212:215], v[100:103], v[200:203]\n"
  "buffer_load_dwordx4 v25, s[48:51], s74 offen offset:3072 lds\n"
  "v_mfma_f32_16x16x32_bf16 v[204:207], v[212:215], v[104:107], v[204:207]\n"
  "s_waitcnt lgkmcnt(2)\n"
  "v_mfma_f32_16x16x32_bf16 v[168:171], v[216:219], v[108:111], v[168:171]\n"
  "s_waitcnt vmcnt(16)\n"
  "s_waitcnt lgkmcnt(0)\n"
  "s_barrier\n"
  "v_mov_b32 v48, s90\n"
  "v_mov_b32 v49, s91\n"
  "buffer_store_dwordx2 v[48:49], v33, s[56:59], 0 offen sc1\n"
  "v_mfma_f32_16x16x32_bf16 v[172:175], v[216:219], v[112:115], v[172:175]\n"
  "ds_read_b64_tr_b16 v[208:209], v26 offset:49152\n"
  "ds_read_b64_tr_b16 v[210:211], v26 offset:53248\n"
  "v_mfma_f32_16x16x32_bf16 v[176:179], v[216:219], v[116:119], v[176:179]\n"
  "ds_read_b64_tr_b16 v[212:213], v27 offset:49152\n"
  "ds_read_b64_tr_b16 v[214:215], v27 offset:53248\n"
  "s_add_u32 m0, s76, 0x14000\n"
  "s_xor_b32 s74, s81, 0x60000\n"
  "v_mfma_f32_16x16x32_bf16 v[180:183], v[216:219], v[120:123], v[180:183]\n"
  "buffer_load_dwordx4 v22, s[48:51], s74 offen lds\n"
  "v_mfma_f32_16x16x32_bf16 v[184:187], v[216:219], v[124:127], v[184:187]\n"
  "v_mfma_f32_16x16x32_bf16 v[188:191], v[220:223], v[108:111], v[188:191]\n"
  "v_mfma_f32_16x16x32_bf16 v[192:195], v[220:223], v[112:115], v[192:195]\n"
  "v_mfma_f32_16x16x32_bf16 v[196:199], v[220:223], v[116:119], v[196:199]\n"
  "v_mfma_f32_16x16x32_bf16 v[200:203], v[220:223], v[120:123], v[200:203]\n"
  "v_mfma_f32_16x16x32_bf16 v[204:207], v[220:223], v[124:127], v[204:207]\n"
  "s_waitcnt lgkmcnt(2)\n"
  "v_mfma_f32_16x16x32_bf16 v[128:131], v[208:211], a[0:3], 0\n"
  "ds_read_b64_tr_b16 v[216:217], v26 offset:57344\n"
  "ds_read_b64_tr_b16 v[218:219], v26 offset:61440\n"
  "v_mfma_f32_16x16x32_bf16 v[132:135], v[208:211], a[4:7], 0\n"
  "ds_read_b64_tr_b16 v[220:221], v27 offset:57344\n"
  "ds_read_b64_tr_b16 v[222:223], v27 offset:61440\n"
  "v_mfma_f32_16x16x32_bf16 v[136:139], v[208:211], a[8:11], 0\n"
  "v_cvt_pk_bf16_f32 v168, v168, v169\n"
  "v_cvt_pk_bf16_f32 v169, v170, v171\n"
  "v_mfma_f32_16x16x32_bf16 v[140:143], v[208:211], a[12:15], 0\n"
  "buffer_load_dwordx4 v23, s[48:51], s74 offen offset:1024 lds\n"
  "v_cvt_pk_bf16_f32 v170, v172, v173\n"
  "v_cvt_pk_bf16_f32 v171, v174, v175\n"
  "v_mfma_f32_16x16x32_bf16 v[144:147], v[208:211], a[16:19], 0\n"
  "Lh27_%=:\n"
  "s_cbranch_execz Lh28_%=\n"
  "buffer_store_dwordx4 v[168:171], v32, s[52:55], s17 offen sc1\n"
  "s_waitcnt lgkmcnt(4)\n"
  "v_mfma_f32_16x16x32_bf16 v[148:151], v[212:215], a[0:3], 0\n"
  "v_cvt_pk_bf16_f32 v176, v176, v177\n"
  "v_cvt_pk_bf16_f32 v177, v178, v179\n"
  "v_mfma_f32_16x16x32_bf16 v[152:155], v[212:215], a[4:7], 0\n"
  "v_cvt_pk_bf16_f32 v178, v180, v181\n"
  "v_cvt_pk_bf16_f32 v179, v182, v183\n"
  "v_mfma_f32_16x16x32_bf16 v[156:159], v[212:215], a[8:11], 0\n"
  "buffer_store_dwordx4 v[176:179], v32, s[52:55], s17 offen offset:1024 sc1\n"
  "v_mfma_f32_16x16x32_bf16 v[160:163], v[212:215], a[12:15], 0\n"
  "v_mfma_f32_16x16x32_bf16 v[164:167], v[212:215], a[16:19], 0\n"
  "s_waitcnt lgkmcnt(2)\n"
  "v_mfma_f32_16x16x32_bf16 v[128:131], v[216:219], a[20:23], v[128:131]\n"
  "ds_read_b64_tr_b16 v[208:209], v28\n"
  "ds_read_b64_tr_b16 v[210:211], v28 offset:4096\n"
  "v_mfma_f32_16x16x32_bf16 v[132:135], v[216:219], a[24:27], v[132:135]\n"
  "ds_read_b64_tr_b16 v[212:213], v29\n"
  "ds_read_b64_tr_b16 v[214:215], v29 offset:4096\n"
  "v_mfma_f32_16x16x32_bf16 v[136:139], v[216:219], a[28:31], v[136:139]\n"
  "v_cvt_pk_bf16_f32 v188, v188, v189\n"
  "v_cvt_pk_bf16_f32 v189, v190, v191\n"
  "v_mfma_f32_16x16x32_bf16 v[140:143], v[216:219], a[32:35], v[140:143]\n"
  "buffer_load_dwordx4 v24, s[48:51], s74 offen offset:2048 lds\n"
  "v_cvt_pk_bf16_f32 v190, v192, v193\n"
  "v_cvt_pk_bf16_f32 v191, v194, v195\n"
  "v_mfma_f32_16x16x32_bf16 v[144:147], v[216:219], a[36:39], v[144:147]\n"
  "s_add_u32 s78, s17, 0x14000\n"
  "buffer_store_dwordx4 v[188:191], v32, s[52:55], s78 offen sc1\n"
  "s_waitcnt lgkmcnt(4)\n"
  "v_mfma_f32_16x16x32_bf16 v[148:151], v[220:223], a[20:23], v[148:151]\n"
  "v_cvt_pk_bf16_f32 v196, v196, v197\n"
  "v_cvt_pk_bf16_f32 v197, v198, v199\n"
  "v_mfma_f32_16x16x32_bf16 v[152:155], v[220:223], a[24:27], v[152:155]\n"
  "v_cvt_pk_bf16_f32 v198, v200, v201\n"
  "v_cvt_pk_bf16_f32 v199, v202, v203\n"
  "v_mfma_f32_16x16x32_bf16 v[156:159], v[220:223], a[28:31], v[156:159]\n"
  "buffer_store_dwordx4 v[196:199], v32, s[52:55], s78 offen offset:1024 sc1\n"
  "v_cvt_pk_bf16_f32 v184, v184, v185\n"
  "v_cvt_pk_bf16_f32 v185, v186, v187\n"
  "v_cvt_pk_bf16_f32 v186, v204, v205\n"
  "v_cvt_pk_bf16_f32 v187, v206, v207\n"
  "v_mfma_f32_16x16x32_bf16 v[160:163], v[220:223], a[32:35], v[160:163]\n"
  "v_permlane32_swap_b32 v184, v186\n"
  "v_permlane32_swap_b32 v185, v187\n"
  "v_mfma_f32_16x16x32_bf16 v[164:167], v[220:223], a[36:39], v[164:167]\n"
  "buffer_store_dwordx4 v[184:187], v38, s[52:55], s17 offen sc1\n"
  "s_waitcnt lgkmcnt(2)\n"
  "v_mfma_f32_16x16x32_bf16 v[128:131], v[208:211], a[40:43], v[128:131]\n"
  "ds_read_b64_tr_b16 v[216:217], v28 offset:8192\n"
  "ds_read_b64_tr_b16 v[218:219], v28 offset:12288\n"
  "v_mfma_f32_16x16x32_bf16 v[132:135], v[208:211], a[44:47], v[132:135]\n"
  "ds_read_b64_tr_b16 v[220:221], v29 offset:8192\n"
  "ds_read_b64_tr_b16 v[222:223], v29 offset:12288\n"
  "v_mfma_f32_16x16x32_bf16 v[136:139], v[208:211], a[48:51], v[136:139]\n"
  "v_mfma_f32_16x16x32_bf16 v[140:143], v[208:211], a[52:55], v[140:143]\n"
  "buffer_load_dwordx4 v25, s[48:51], s74 offen offset:3072 lds\n"
  "v_mfma_f32_16x16x32_bf16 v[144:147], v[208:211], a[56:59], v[144:147]\n"
  "s_waitcnt lgkmcnt(4)\n"
  "v_mfma_f32_16x16x32_bf16 v[148:151], v[212:215], a[40:43], v[148:151]\n"
  "v_mfma_f32_16x16x32_bf16 v[152:155], v[212:215], a[44:47], v[152:155]\n"
  "v_mfma_f32_16x16x32_bf16 v[156:159], v[212:215], a[48:51], v[156:159]\n"
  "v_mfma_f32_16x16x32_bf16 v[160:163], v[212:215], a[52:55], v[160:163]\n"
  "v_mfma_f32_16x16x32_bf16 v[164:167], v[212:215], a[56:59], v[164:167]\n"
  "s_waitcnt lgkmcnt(2)\n"
  "v_mfma_f32_16x16x32_bf16 v[128:131], v[216:219], a[60:63], v[128:131]\n"
  "s_waitcnt vmcnt(18)\n"
  "s_waitcnt lgkmcnt(0)\n"
  "Lh28_%=:\n"
  "s_cbranch_execz Lh29_%=\n"
  "s_barrier\n"
  "v_mfma_f32_16x16x32_bf16 v[132:135], v[216:219], a[64:67], v[132:135]\n"
  "ds_read_b64_tr_b16 v[208:209], v28 offset:49152\n"
  "ds_read_b64_tr_b16 v[210:211], v28 offset:53248\n"
  "v_mfma_f32_16x16x32_bf16 v[136:139], v[216:219], a[68:71], v[136:139]\n"
  "ds_read_b64_tr_b16 v[212:213], v29 offset:49152\n"
  "ds_read_b64_tr_b16 v[214:215], v29 offset:53248\n"
  "s_add_u32 m0, s76, 0x8000\n"
  "s_xor_b32 s74, s81, 0x70000\n"
  "v_mfma_f32_16x16x32_bf16 v[140:143], v[216:219], a[72:75], v[140:143]\n"
  "buffer_load_dwordx4 v22, s[48:51], s74 offen lds\n"
  "v_mfma_f32_16x16x32_bf16 v[144:147], v[216:219], a[76:79], v[144:147]\n"
  "v_mfma_f32_16x16x32_bf16 v[148:151], v[220:223], a[60:63], v[148:151]\n"
  "v_mfma_f32_16x16x32_bf16 v[152:155], v[220:223], a[64:67], v[152:155]\n"
  "v_mfma_f32_16x16x32_bf16 v[156:159], v[220:223], a[68:71], v[156:159]\n"
  "v_mfma_f32_16x16x32_bf16 v[160:163], v[220:223], a[72:75], v[160:163]\n"
  "v_mfma_f32_16x16x32_bf16 v[164:167], v[220:223], a[76:79], v[164:167]\n"
  "s_waitcnt lgkmcnt(2)\n"
  "v_mfma_f32_16x16x32_bf16 v[128:131], v[208:211], a[80:83], v[128:131]\n"
  "ds_read_b64_tr_b16 v[216:217], v28 offset:57344\n"
  "ds_read_b64_tr_b16 v[218:219], v28 offset:61440\n"
  "v_mfma_f32_16x16x32_bf16 v[132:135], v[208:211], a[84:87], v[132:135]\n"
  "ds_read_b64_tr_b16 v[220:221], v29 offset:57344\n"
  "ds_read_b64_tr_b16 v[222:223], v29 offset:61440\n"
  "v_mfma_f32_16x16x32_bf16 v[136:139], v[208:211], a[88:91], v[136:139]\n"
  "v_mfma_f32_16x16x32_bf16 v[140:143], v[208:211], a[92:95], v[140:143]\n"
  "buffer_load_dwordx4 v23, s[48:51], s74 offen offset:1024 lds\n"
  "v_mfma_f32_16x16x32_bf16 v[144:147], v[208:211], a[96:99], v[144:147]\n"
  "s_waitcnt lgkmcnt(4)\n"
  "v_mfma_f32_16x16x32_bf16 v[148:151], v[212:215], a[80:83], v[148:151]\n"
  "buffer_load_dwordx2 v[50:51], v34, s[56:59], 0 offen offset:1024 sc1\n"
  "v_mfma_f32_16x16x32_bf16 v[152:155], v[212:215], a[84:87], v[152:155]\n"
  "buffer_load_dwordx4 v24, s[48:51], s74 offen offset:2048 lds\n"
  "v_mfma_f32_16x16x32_bf16 v[156:159], v[212:215], a[88:91], v[156:159]\n"
  "v_mfma_f32_16x16x32_bf16 v[160:163], v[212:215], a[92:95], v[160:163]\n"
  "v_mfma_f32_16x16x32_bf16 v[164:167], v[212:215], a[96:99], v[164:167]\n"
  "s_waitcnt lgkmcnt(2)\n"
  "v_mfma_f32_16x16x32_bf16 v[128:131], v[216:219], a[100:103], v[128:131]\n"
  "ds_read_b64_tr_b16 v[208:209], v30\n"
  "ds_read_b64_tr_b16 v[210:211], v30 offset:4096\n"
  "v_mfma_f32_16x16x32_bf16 v[132:135], v[216:219], a[104:107], v[132:135]\n"
  "ds_read_b64_tr_b16 v[212:213], v31\n"
  "ds_read_b64_tr_b16 v[214:215], v31 offset:4096\n"
  "v_mfma_f32_16x16x32_bf16 v[136:139], v[216:219], a[108:111], v[136:139]\n"
  "v_mfma_f32_16x16x32_bf16 v[140:143], v[216:219], a[112:115], v[140:143]\n"
  "buffer_load_dwordx4 v25, s[48:51], s74 offen offset:3072 lds\n"
  "v_mfma_f32_16x16x32_bf16 v[144:147], v[216:219], a[116:119], v[144:147]\n"
  "s_waitcnt lgkmcnt(4)\n"
  "v_mfma_f32_16x16x32_bf16 v[148:151], v[220:223], a[100:103], v[148:151]\n"
  "v_mfma_f32_16x16x32_bf16 v[152:155], v[220:223], a[104:107], v[152:155]\n"
  "v_mfma_f32_16x16x32_bf16 v[156:159], v[220:223], a[108:111], v[156:159]\n"
  "v_mfma_f32_16x16x32_bf16 v[160:163], v[220:223], a[112:115], v[160:163]\n"
  "v_mfma_f32_16x16x32_bf16 v[164:167], v[220:223], a[116:119], v[164:167]\n"
  "s_waitcnt lgkmcnt(2)\n"
  "v_mfma_f32_16x16x32_bf16 v[128:131], v[208:211], a[120:123], v[128:131]\n"
  "ds_read_b64_tr_b16 v[216:217], v30 offset:8192\n"
  "ds_read_b64_tr_b16 v[218:219], v30 offset:12288\n"
  "v_mfma_f32_16x16x32_bf16 v[132:135], v[208:211], a[124:127], v[132:135]\n"
  "ds_read_b64_tr_b16 v[220:221], v31 offset:8192\n"
  "ds_read_b64_tr_b16 v[222:223], v31 offset:12288\n"
  "v_mfma_f32_16x16x32_bf16 v[136:139], v[208:211], a[128:131], v[136:139]\n"
  "s_add_u32 m0, s76, 0x18000\n"
  "s_xor_b32 s74, s82, 0\n"
  "v_mfma_f32_16x16x32_bf16 v[140:143], v[208:211], a[132:135], v[140:143]\n"
  "buffer_load_dwordx4 v40, s[48:51], s74 offen lds\n"
  "v_mfma_f32_16x16x32_bf16 v[144:147], v[208:211], a[136:139], v[144:147]\n"
  "s_waitcnt lgkmcnt(4)\n"
  "v_mfma_f32_16x16x32_bf16 v[148:151], v[212:215], a[120:123], v[148:151]\n"
  "Lh29_%=:\n"
  "s_cbranch_execz Lret_%=\n"
  "v_mfma_f32_16x16x32_bf16 v[152:155], v[212:215], a[124:127], v[152:155]\n"
  "buffer_load_dwordx4 v41, s[48:51], s74 offen offset:1024 lds\n"
  "v_mfma_f32_16x16x32_bf16 v[156:159], v[212:215], a[128:131], v[156:159]\n"
  "v_mfma_f32_16x16x32_bf16 v[160:163], v[212:215], a[132:135], v[160:163]\n"
  "v_mfma_f32_16x16x32_bf16 v[164:167], v[212:215], a[136:139], v[164:167]\n"
  "s_waitcnt lgkmcnt(2)\n"
  "v_mfma_f32_16x16x32_bf16 v[128:131], v[216:219], a[140:143], v[128:131]\n"
  "s_waitcnt vmcnt(17)\n"
  "s_waitcnt lgkmcnt(0)\n"
  "s_barrier\n"
  "v_mfma_f32_16x16x32_bf16 v[132:135], v[216:219], a[144:147], v[132:135]\n"
  "ds_read_b64_tr_b16 v[208:209], v26 offset:16384\n"
  "ds_read_b64_tr_b16 v[210:211], v26 offset:20480\n"
  "v_mfma_f32_16x16x32_bf16 v[136:139], v[216:219], a[148:151], v[136:139]\n"
  "ds_read_b64_tr_b16 v[212:213], v27 offset:16384\n"
  "ds_read_b64_tr_b16 v[214:215], v27 offset:20480\n"
  "v_mfma_f32_16x16x32_bf16 v[140:143], v[216:219], a[152:155], v[140:143]\n"
  "buffer_load_dwordx4 v42, s[48:51], s74 offen offset:2048 lds\n"
  "v_mfma_f32_16x16x32_bf16 v[144:147], v[216:219], a[156:159], v[144:147]\n"
  "v_mfma_f32_16x16x32_bf16 v[148:151], v[220:223], a[140:143], v[148:151]\n"
  "v_mfma_f32_16x16x32_bf16 v[152:155], v[220:223], a[144:147], v[152:155]\n"
  "buffer_load_dwordx4 v43, s[48:51], s74 offen offset:3072 lds\n"
  "v_mfma_f32_16x16x32_bf16 v[156:159], v[220:223], a[148:151], v[156:159]\n"
  "v_mfma_f32_16x16x32_bf16 v[160:163], v[220:223], a[152:155], v[160:163]\n"
  "v_mfma_f32_16x16x32_bf16 v[164:167], v[220:223], a[156:159], v[164:167]\n"
  "s_waitcnt lgkmcnt(2)\n"
  "v_mfma_f32_16x16x32_bf16 v[128:131], v[208:211], a[160:163], v[128:131]\n"
  "ds_read_b64_tr_b16 v[216:217], v26 offset:24576\n"
  "ds_read_b64_tr_b16 v[218:219], v26 offset:28672\n"
  "v_mfma_f32_16x16x32_bf16 v[132:135], v[208:211], a[164:167], v[132:135]\n"
  "ds_read_b64_tr_b16 v[220:221], v27 offset:24576\n"
  "ds_read_b64_tr_b16 v[222:223], v27 offset:28672\n"
  "v_mfma_f32_16x16x32_bf16 v[136:139], v[208:211], a[168:171], v[136:139]\n"
  "s_add_u32 m0, s76, 0\n"
  "s_xor_b32 s74, s82, 0x20000\n"
  "v_mfma_f32_16x16x32_bf16 v[140:143], v[208:211], a[172:175], v[140:143]\n"
  "buffer_load_dwordx4 v40, s[48:51], s74 offen lds\n"
  "v_mfma_f32_16x16x32_bf16 v[144:147], v[208:211], a[176:179], v[144:147]\n"
  "s_waitcnt lgkmcnt(4)\n"
  "v_mfma_f32_16x16x32_bf16 v[148:151], v[212:215], a[160:163], v[148:151]\n"
  "v_mfma_f32_16x16x32_bf16 v[152:155], v[212:215], a[164:167], v[152:155]\n"
  "buffer_load_dwordx4 v41, s[48:51], s74 offen offset:1024 lds\n"
  "v_mfma_f32_16x16x32_bf16 v[156:159], v[212:215], a[168:171], v[156:159]\n"
  "v_mfma_f32_16x16x32_bf16 v[160:163], v[212:215], a[172:175], v[160:163]\n"
  "buffer_load_dwordx4 v42, s[48:51], s74 offen offset:2048 lds\n"
  "v_mfma_f32_16x16x32_bf16 v[164:167], v[212:215], a[176:179], v[164:167]\n"
  "s_waitcnt lgkmcnt(2)\n"
  "v_mfma_f32_16x16x32_bf16 v[128:131], v[216:219], a[180:183], v[128:131]\n"
  "ds_read_b64_tr_b16 v[208:209], v30 offset:16384\n"
  "ds_read_b64_tr_b16 v[210:211], v30 offset:20480\n"
  "v_mfma_f32_16x16x32_bf16 v[132:135], v[216:219], a[184:187], v[132:135]\n"
  "ds_read_b64_tr_b16 v[212:213], v31 offset:16384\n"
  "ds_read_b64_tr_b16 v[214:215], v31 offset:20480\n"
  "v_mfma_f32_16x16x32_bf16 v[136:139], v[216:219], a[188:191], v[136:139]\n"
  "v_mfma_f32_16x16x32_bf16 v[140:143], v[216:219], a[192:195], v[140:143]\n"
  "buffer_load_dwordx4 v43, s[48:51], s74 offen offset:3072 lds\n"
  "v_mfma_f32_16x16x32_bf16 v[144:147], v[216:219], a[196:199], v[144:147]\n"
  "s_waitcnt lgkmcnt(4)\n"
  "v_mfma_f32_16x16x32_bf16 v[148:151], v[220:223], a[180:183], v[148:151]\n"
  "s_add_u32 m0, s76, 0xc000\n"
  "s_xor_b32 s74, s82, 0x40000\n"
  "s_waitcnt vmcnt(10)\n"
  "v_cmp_eq_u64 vcc, v[50:51], s[90:91]\n"
  "s_mov_b32 s89, 0x10000\n"
  "Lp0_%=:\n"
  "s_nop 0\n"
  "s_andn2_b32 s8, s24, vcc_lo\n"
  "s_cbranch_scc0 Lq0_%=\n"
  "s_sleep 1\n"
  "buffer_load_dwordx2 v[50:51], v34, s[56:59], 0 offen offset:1024 sc1\n"
  "s_sub_u32 s89, s89, 1\n"
  "s_cbranch_scc1 Lq0_%=\n"
  "Lh30_%=:\n"
  "s_cbranch_execz Lh31_%=\n"
  "s_waitcnt vmcnt(0)\n"
  "v_cmp_eq_u64 vcc, v[50:51], s[90:91]\n"
  "s_branch Lp0_%=\n"
  "Lq0_%=:\n"
  "v_mfma_f32_16x16x32_bf16 v[152:155], v[220:223], a[184:187], v[152:155]\n"
  "buffer_load_dwordx4 v40, s[48:51], s74 offen lds\n"
  "buffer_load_dwordx4 v[224:227], v32, s[52:55], s20 offen sc1\n"
  "v_mfma_f32_16x16x32_bf16 v[156:159], v[220:223], a[188:191], v[156:159]\n"
  "v_mfma_f32_16x16x32_bf16 v[160:163], v[220:223], a[192:195], v[160:163]\n"
  "v_mfma_f32_16x16x32_bf16 v[164:167], v[220:223], a[196:199], v[164:167]\n"
  "buffer_load_dwordx4 v[232:235], v32, s[52:55], s20 offen offset:1024 sc1\n"
  "s_waitcnt lgkmcnt(2)\n"
  "v_mfma_f32_16x16x32_bf16 v[128:131], v[208:211], a[200:203], v[128:131]\n"
  "ds_read_b64_tr_b16 v[216:217], v30 offset:24576\n"
  "ds_read_b64_tr_b16 v[218:219], v30 offset:28672\n"
  "v_mfma_f32_16x16x32_bf16 v[132:135], v[208:211], a[204:207], v[132:135]\n"
  "ds_read_b64_tr_b16 v[220:221], v31 offset:24576\n"
  "ds_read_b64_tr_b16 v[222:223], v31 offset:28672\n"
  "v_mfma_f32_16x16x32_bf16 v[136:139], v[208:211], a[208:211], v[136:139]\n"
  "buffer_load_dwordx2 v[240:241], v35, s[52:55], s20 offen sc1\n"
  "v_mfma_f32_16x16x32_bf16 v[140:143], v[208:211], a[212:215], v[140:143]\n"
  "buffer_load_dwordx4 v41, s[48:51], s74 offen offset:1024 lds\n"
  "v_mfma_f32_16x16x32_bf16 v[144:147], v[208:211], a[216:219], v[144:147]\n"
  "s_waitcnt lgkmcnt(4)\n"
  "v_mfma_f32_16x16x32_bf16 v[148:151], v[212:215], a[200:203], v[148:151]\n"
  "s_add_u32 s98, s20, 0x2800\n"
  "buffer_load_dwordx4 v[228:231], v32, s[52:55], s98 offen sc1\n"
  "v_mfma_f32_16x16x32_bf16 v[152:155], v[212:215], a[204:207], v[152:155]\n"
  "buffer_load_dwordx4 v42, s[48:51], s74 offen offset:2048 lds\n"
  "v_mfma_f32_16x16x32_bf16 v[156:159], v[212:215], a[208:211], v[156:159]\n"
  "v_mfma_f32_16x16x32_bf16 v[160:163], v[212:215], a[212:215], v[160:163]\n"
  "buffer_load_dwordx4 v43, s[48:51], s74 offen offset:3072 lds\n"
  "s_add_u32 s98, s20, 0x2800\n"
  "buffer_load_dwordx4 v[236:239], v32, s[52:55], s98 offen offset:1024 sc1\n"
  "v_mfma_f32_16x16x32_bf16 v[164:167], v[212:215], a[216:219], v[164:167]\n"
  "s_waitcnt lgkmcnt(2)\n"
  "v_mfma_f32_16x16x32_bf16 v[128:131], v[216:219], a[220:223], v[128:131]\n"
  "s_waitcnt vmcnt(17)\n"
  "s_waitcnt lgkmcnt(0)\n"
  "s_barrier\n"
  "v_mfma_f32_16x16x32_bf16 v[132:135], v[216:219], a[224:227], v[132:135]\n"
  "ds_read_b64_tr_b16 v[208:209], v28 offset:16384\n"
  "ds_read_b64_tr_b16 v[210:211], v28 offset:20480\n"
  "s_add_u32 s98, s20, 0x2800\n"
  "buffer_load_dwordx2 v[242:243], v35, s[52:55], s98 offen sc1\n"
  "v_mfma_f32_16x16x32_bf16 v[136:139], v[216:219], a[228:231], v[136:139]\n"
  "ds_read_b64_tr_b16 v[212:213], v29 offset:16384\n"
  "ds_read_b64_tr_b16 v[214:215], v29 offset:20480\n"
  "s_add_u32 m0, s76, 0x10000\n"
  "s_xor_b32 s74, s82, 0x60000\n"
  "v_mov_b32 v48, s90\n"
  "v_mov_b32 v49, s91\n"
  "buffer_store_dwordx2 v[48:49], v33, s[56:59], 0 offen offset:64 sc1\n"
  "v_mfma_f32_16x16x32_bf16 v[140:143], v[216:219], a[232:235], v[140:143]\n"
  "buffer_load_dwordx4 v40, s[48:51], s74 offen lds\n"
  "v_mfma_f32_16x16x32_bf16 v[144:147], v[216:219], a[236:239], v[144:147]\n"
  "v_mfma_f32_16x16x32_bf16 v[148:151], v[220:223], a[220:223], v[148:151]\n"
  "v_mfma_f32_16x16x32_bf16 v[152:155], v[220:223], a[224:227], v[152:155]\n"
  "buffer_load_dwordx4 v41, s[48:51], s74 offen offset:1024 lds\n"
  "v_mfma_f32_16x16x32_bf16 v[156:159], v[220:223], a[228:231], v[156:159]\n"
  "v_mfma_f32_16x16x32_bf16 v[160:163], v[220:223], a[232:235], v[160:163]\n"
  "v_mfma_f32_16x16x32_bf16 v[164:167], v[220:223], a[236:239], v[164:167]\n"
  "s_waitcnt lgkmcnt(2)\n"
  "v_mfma_f32_16x16x32_bf16 v[128:131], v[208:211], a[240:243], v[128:131]\n"
  "ds_read_b64_tr_b16 v[216:217], v28 offset:24576\n"
  "ds_read_b64_tr_b16 v[218:219], v28 offset:28672\n"
  "v_mfma_f32_16x16x32_bf16 v[132:135], v[208:211], a[244:247], v[132:135]\n"
  "ds_read_b64_tr_b16 v[220:221], v29 offset:24576\n"
  "ds_read_b64_tr_b16 v[222:223], v29 offset:28672\n"
  "v_mfma_f32_16x16x32_bf16 v[136:139], v[208:211], a[248:251], v[136:139]\n"
  "Lh31_%=:\n"
  "s_cbranch_execz Lh32_%=\n"
  "v_mfma_f32_16x16x32_bf16 v[140:143], v[208:211], a[252:255], v[140:143]\n"
  "buffer_load_dwordx4 v42, s[48:51], s74 offen offset:2048 lds\n"
  "v_mfma_f32_16x16x32_bf16 v[144:147], v[208:211], v[64:67], v[144:147]\n"
  "s_waitcnt lgkmcnt(4)\n"
  "v_mfma_f32_16x16x32_bf16 v[148:151], v[212:215], a[240:243], v[148:151]\n"
  "v_mfma_f32_16x16x32_bf16 v[152:155], v[212:215], a[244:247], v[152:155]\n"
  "buffer_load_dwordx4 v43, s[48:51], s74 offen offset:3072 lds\n"
  "v_mfma_f32_16x16x32_bf16 v[156:159], v[212:215], a[248:251], v[156:159]\n"
  "v_mfma_f32_16x16x32_bf16 v[160:163], v[212:215], a[252:255], v[160:163]\n"
  "v_mfma_f32_16x16x32_bf16 v[164:167], v[212:215], v[64:67], v[164:167]\n"
  "s_waitcnt lgkmcnt(2)\n"
  "v_mfma_f32_16x16x32_bf16 v[128:131], v[216:219], v[68:71], v[128:131]\n"
  "ds_read_b64_tr_b16 v[208:209], v26 offset:32768\n"
  "ds_read_b64_tr_b16 v[210:211], v26 offset:36864\n"
  "v_mfma_f32_16x16x32_bf16 v[132:135], v[216:219], v[72:75], v[132:135]\n"
  "ds_read_b64_tr_b16 v[212:213], v27 offset:32768\n"
  "ds_read_b64_tr_b16 v[214:215], v27 offset:36864\n"
  "v_mfma_f32_16x16x32_bf16 v[136:139], v[216:219], v[76:79], v[136:139]\n"
  "s_add_u32 m0, s76, 0x1c000\n"
  "s_xor_b32 s74, s83, 0\n"
  "v_mfma_f32_16x16x32_bf16 v[140:143], v[216:219], v[80:83], v[140:143]\n"
  "buffer_load_dwordx4 v40, s[48:51], s74 offen lds\n"
  "v_mfma_f32_16x16x32_bf16 v[144:147], v[216:219], v[84:87], v[144:147]\n"
  "s_waitcnt lgkmcnt(4)\n"
  "v_mfma_f32_16x16x32_bf16 v[148:151], v[220:223], v[68:71], v[148:151]\n"
  "v_mfma_f32_16x16x32_bf16 v[152:155], v[220:223], v[72:75], v[152:155]\n"
  "buffer_load_dwordx4 v41, s[48:51], s74 offen offset:1024 lds\n"
  "v_mfma_f32_16x16x32_bf16 v[156:159], v[220:223], v[76:79], v[156:159]\n"
  "v_mfma_f32_16x16x32_bf16 v[160:163], v[220:223], v[80:83], v[160:163]\n"
  "buffer_load_dwordx4 v42, s[48:51], s74 offen offset:2048 lds\n"
  "v_mfma_f32_16x16x32_bf16 v[164:167], v[220:223], v[84:87], v[164:167]\n"
  "s_waitcnt lgkmcnt(2)\n"
  "v_mfma_f32_16x16x32_bf16 v[128:131], v[208:211], v[88:91], v[128:131]\n"
  "ds_read_b64_tr_b16 v[216:217], v26 offset:40960\n"
  "ds_read_b64_tr_b16 v[218:219], v26 offset:45056\n"
  "v_mfma_f32_16x16x32_bf16 v[132:135], v[208:211], v[92:95], v[132:135]\n"
  "ds_read_b64_tr_b16 v[220:221], v27 offset:40960\n"
  "ds_read_b64_tr_b16 v[222:223], v27 offset:45056\n"
  "v_mfma_f32_16x16x32_bf16 v[136:139], v[208:211], v[96:99], v[136:139]\n"
  "v_mfma_f32_16x16x32_bf16 v[140:143], v[208:211], v[100:103], v[140:143]\n"
  "buffer_load_dwordx4 v43, s[48:51], s74 offen offset:3072 lds\n"
  "v_mfma_f32_16x16x32_bf16 v[144:147], v[208:211], v[104:107], v[144:147]\n"
  "s_waitcnt lgkmcnt(4)\n"
  "v_mfma_f32_16x16x32_bf16 v[148:151], v[212:215], v[88:91], v[148:151]\n"
  "s_add_u32 m0, s76, 0x20000\n"
  "s_xor_b32 s74, s83, 0x20000\n"
  "v_mfma_f32_16x16x32_bf16 v[152:155], v[212:215], v[92:95], v[152:155]\n"
  "buffer_load_dwordx4 v40, s[48:51], s74 offen lds\n"
  "v_mfma_f32_16x16x32_bf16 v[156:159], v[212:215], v[96:99], v[156:159]\n"
  "v_mfma_f32_16x16x32_bf16 v[160:163], v[212:215], v[100:103], v[160:163]\n"
  "v_mfma_f32_16x16x32_bf16 v[164:167], v[212:215], v[104:107], v[164:167]\n"
  "s_waitcnt lgkmcnt(2)\n"
  "v_mfma_f32_16x16x32_bf16 v[128:131], v[216:219], v[108:111], v[128:131]\n"
  "s_waitcnt vmcnt(20)\n"
  "s_waitcnt lgkmcnt(0)\n"
  "s_barrier\n"
  "v_mfma_f32_16x16x32_bf16 v[132:135], v[216:219], v[112:115], v[132:135]\n"
  "ds_read_b64_tr_b16 v[208:209], v45 offset:32768\n"
  "ds_read_b64_tr_b16 v[210:211], v45 offset:34816\n"
  "v_mfma_f32_16x16x32_bf16 v[136:139], v[216:219], v[116:119], v[136:139]\n"
  "ds_read_b64_tr_b16 v[212:213], v45 offset:36864\n"
  "ds_read_b64_tr_b16 v[214:215], v45 offset:38912\n"
  "v_mfma_f32_16x16x32_bf16 v[140:143], v[216:219], v[120:123], v[140:143]\n"
  "buffer_load_dwordx4 v41, s[48:51], s74 offen offset:1024 lds\n"
  "v_mfma_f32_16x16x32_bf16 v[144:147], v[216:219], v[124:127], v[144:147]\n"
  "v_mfma_f32_16x16x32_bf16 v[148:151], v[220:223], v[108:111], v[148:151]\n"
  "v_mfma_f32_16x16x32_bf16 v[152:155], v[220:223], v[112:115], v[152:155]\n"
  "buffer_load_dwordx4 v42, s[48:51], s74 offen offset:2048 lds\n"
  "v_mfma_f32_16x16x32_bf16 v[156:159], v[220:223], v[116:119], v[156:159]\n"
  "Lh32_%=:\n"
  "s_cbranch_execz Lh33_%=\n"
  "v_mfma_f32_16x16x32_bf16 v[160:163], v[220:223], v[120:123], v[160:163]\n"
  "v_mfma_f32_16x16x32_bf16 v[164:167], v[220:223], v[124:127], v[164:167]\n"
  "s_waitcnt lgkmcnt(2)\n"
  "v_mfma_f32_16x16x32_bf16 v[168:171], v[208:211], a[0:3], 0\n"
  "ds_read_b64_tr_b16 v[216:217], v45 offset:40960\n"
  "ds_read_b64_tr_b16 v[218:219], v45 offset:43008\n"
  "v_mfma_f32_16x16x32_bf16 v[172:175], v[208:211], a[4:7], 0\n"
  "ds_read_b64_tr_b16 v[220:221], v45 offset:45056\n"
  "ds_read_b64_tr_b16 v[222:223], v45 offset:47104\n"
  "v_mfma_f32_16x16x32_bf16 v[176:179], v[208:211], a[8:11], 0\n"
  "v_cvt_pk_bf16_f32 v128, v128, v129\n"
  "v_cvt_pk_bf16_f32 v129, v130, v131\n"
  "v_mfma_f32_16x16x32_bf16 v[180:183], v[208:211], a[12:15], 0\n"
  "buffer_load_dwordx4 v43, s[48:51], s74 offen offset:3072 lds\n"
  "v_cvt_pk_bf16_f32 v130, v132, v133\n"
  "v_cvt_pk_bf16_f32 v131, v134, v135\n"
  "v_mfma_f32_16x16x32_bf16 v[184:187], v[208:211], a[16:19], 0\n"
  "buffer_store_dwordx4 v[128:131], v32, s[52:55], s18 offen sc1\n"
  "s_waitcnt lgkmcnt(4)\n"
  "v_mfma_f32_16x16x32_bf16 v[168:171], v[212:215], a[20:23], v[168:171]\n"
  "v_cvt_pk_bf16_f32 v136, v136, v137\n"
  "v_cvt_pk_bf16_f32 v137, v138, v139\n"
  "v_mfma_f32_16x16x32_bf16 v[172:175], v[212:215], a[24:27], v[172:175]\n"
  "v_cvt_pk_bf16_f32 v138, v140, v141\n"
  "v_cvt_pk_bf16_f32 v139, v142, v143\n"
  "v_mfma_f32_16x16x32_bf16 v[176:179], v[212:215], a[28:31], v[176:179]\n"
  "buffer_store_dwordx4 v[136:139], v32, s[52:55], s18 offen offset:1024 sc1\n"
  "v_mfma_f32_16x16x32_bf16 v[180:183], v[212:215], a[32:35], v[180:183]\n"
  "v_mfma_f32_16x16x32_bf16 v[184:187], v[212:215], a[36:39], v[184:187]\n"
  "s_waitcnt lgkmcnt(2)\n"
  "v_mfma_f32_16x16x32_bf16 v[168:171], v[216:219], a[40:43], v[168:171]\n"
  "ds_read_b64_tr_b16 v[208:209], v44\n"
  "ds_read_b64_tr_b16 v[210:211], v44 offset:2048\n"
  "v_mfma_f32_16x16x32_bf16 v[172:175], v[216:219], a[44:47], v[172:175]\n"
  "ds_read_b64_tr_b16 v[212:213], v44 offset:4096\n"
  "ds_read_b64_tr_b16 v[214:215], v44 offset:6144\n"
  "v_mfma_f32_16x16x32_bf16 v[176:179], v[216:219], a[48:51], v[176:179]\n"
  "s_add_u32 m0, s76, 0x4000\n"
  "s_xor_b32 s74, s83, 0x40000\n"
  "v_cvt_pk_bf16_f32 v148, v148, v149\n"
  "v_cvt_pk_bf16_f32 v149, v150, v151\n"
  "v_mfma_f32_16x16x32_bf16 v[180:183], v[216:219], a[52:55], v[180:183]\n"
  "buffer_load_dwordx4 v40, s[48:51], s74 offen lds\n"
  "v_cvt_pk_bf16_f32 v150, v152, v153\n"
  "v_cvt_pk_bf16_f32 v151, v154, v155\n"
  "v_mfma_f32_16x16x32_bf16 v[184:187], v[216:219], a[56:59], v[184:187]\n"
  "s_add_u32 s78, s18, 0x14000\n"
  "buffer_store_dwordx4 v[148:151], v32, s[52:55], s78 offen sc1\n"
  "s_waitcnt lgkmcnt(4)\n"
  "v_mfma_f32_16x16x32_bf16 v[168:171], v[220:223], a[60:63], v[168:171]\n"
  "v_cvt_pk_bf16_f32 v156, v156, v157\n"
  "v_cvt_pk_bf16_f32 v157, v158, v159\n"
  "v_mfma_f32_16x16x32_bf16 v[172:175], v[220:223], a[64:67], v[172:175]\n"
  "buffer_load_dwordx4 v41, s[48:51], s74 offen offset:1024 lds\n"
  "v_cvt_pk_bf16_f32 v158, v160, v161\n"
  "v_cvt_pk_bf16_f32 v159, v162, v163\n"
  "v_mfma_f32_16x16x32_bf16 v[176:179], v[220:223], a[68:71], v[176:179]\n"
  "buffer_store_dwordx4 v[156:159], v32, s[52:55], s78 offen offset:1024 sc1\n"
  "v_cvt_pk_bf16_f32 v144, v144, v145\n"
  "v_cvt_pk_bf16_f32 v145, v146, v147\n"
  "v_cvt_pk_bf16_f32 v146, v164, v165\n"
  "v_cvt_pk_bf16_f32 v147, v166, v167\n"
  "v_mfma_f32_16x16x32_bf16 v[180:183], v[220:223], a[72:75], v[180:183]\n"
  "v_permlane32_swap_b32 v144, v146\n"
  "v_permlane32_swap_b32 v145, v147\n"
  "v_mfma_f32_16x16x32_bf16 v[184:187], v[220:223], a[76:79], v[184:187]\n"
  "Lh33_%=:\n"
  "s_cbranch_execz Lh34_%=\n"
  "buffer_store_dwordx4 v[144:147], v38, s[52:55], s18 offen sc1\n"
  "s_waitcnt lgkmcnt(2)\n"
  "v_mfma_f32_16x16x32_bf16 v[168:171], v[208:211], a[80:83], v[168:171]\n"
  "ds_read_b64_tr_b16 v[216:217], v44 offset:8192\n"
  "ds_read_b64_tr_b16 v[218:219], v44 offset:10240\n"
  "v_mfma_f32_16x16x32_bf16 v[172:175], v[208:211], a[84:87], v[172:175]\n"
  "ds_read_b64_tr_b16 v[220:221], v44 offset:12288\n"
  "ds_read_b64_tr_b16 v[222:223], v44 offset:14336\n"
  "v_mfma_f32_16x16x32_bf16 v[176:179], v[208:211], a[88:91], v[176:179]\n"
  "v_mfma_f32_16x16x32_bf16 v[180:183], v[208:211], a[92:95], v[180:183]\n"
  "buffer_load_dwordx4 v42, s[48:51], s74 offen offset:2048 lds\n"
  "v_mfma_f32_16x16x32_bf16 v[184:187], v[208:211], a[96:99], v[184:187]\n"
  "s_waitcnt lgkmcnt(4)\n"
  "v_mfma_f32_16x16x32_bf16 v[168:171], v[212:215], a[100:103], v[168:171]\n"
  "v_mfma_f32_16x16x32_bf16 v[172:175], v[212:215], a[104:107], v[172:175]\n"
  "v_mfma_f32_16x16x32_bf16 v[176:179], v[212:215], a[108:111], v[176:179]\n"
  "v_mfma_f32_16x16x32_bf16 v[180:183], v[212:215], a[112:115], v[180:183]\n"
  "v_mfma_f32_16x16x32_bf16 v[184:187], v[212:215], a[116:119], v[184:187]\n"
  "s_waitcnt lgkmcnt(2)\n"
  "v_mfma_f32_16x16x32_bf16 v[168:171], v[216:219], a[120:123], v[168:171]\n"
  "s_waitcnt vmcnt(16)\n"
  "s_waitcnt lgkmcnt(0)\n"
  "s_barrier\n"
  "v_mfma_f32_16x16x32_bf16 v[172:175], v[216:219], a[124:127], v[172:175]\n"
  "ds_read_b64_tr_b16 v[208:209], v44 offset:49152\n"
  "ds_read_b64_tr_b16 v[210:211], v44 offset:51200\n"
  "v_mfma_f32_16x16x32_bf16 v[176:179], v[216:219], a[128:131], v[176:179]\n"
  "ds_read_b64_tr_b16 v[212:213], v44 offset:53248\n"
  "ds_read_b64_tr_b16 v[214:215], v44 offset:55296\n"
  "v_mfma_f32_16x16x32_bf16 v[180:183], v[216:219], a[132:135], v[180:183]\n"
  "buffer_load_dwordx4 v43, s[48:51], s74 offen offset:3072 lds\n"
  "v_mfma_f32_16x16x32_bf16 v[184:187], v[216:219], a[136:139], v[184:187]\n"
  "v_mfma_f32_16x16x32_bf16 v[168:171], v[220:223], a[140:143], v[168:171]\n"
  "s_add_u32 m0, s76, 0x24000\n"
  "s_xor_b32 s74, s83, 0x60000\n"
  "v_mfma_f32_16x16x32_bf16 v[172:175], v[220:223], a[144:147], v[172:175]\n"
  "buffer_load_dwordx4 v40, s[48:51], s74 offen lds\n"
  "v_mfma_f32_16x16x32_bf16 v[176:179], v[220:223], a[148:151], v[176:179]\n"
  "v_mfma_f32_16x16x32_bf16 v[180:183], v[220:223], a[152:155], v[180:183]\n"
  "v_mfma_f32_16x16x32_bf16 v[184:187], v[220:223], a[156:159], v[184:187]\n"
  "s_waitcnt lgkmcnt(2)\n"
  "v_mfma_f32_16x16x32_bf16 v[168:171], v[208:211], a[160:163], v[168:171]\n"
  "ds_read_b64_tr_b16 v[216:217], v44 offset:57344\n"
  "ds_read_b64_tr_b16 v[218:219], v44 offset:59392\n"
  "v_mfma_f32_16x16x32_bf16 v[172:175], v[208:211], a[164:167], v[172:175]\n"
  "ds_read_b64_tr_b16 v[220:221], v44 offset:61440\n"
  "ds_read_b64_tr_b16 v[222:223], v44 offset:63488\n"
  "v_mfma_f32_16x16x32_bf16 v[176:179], v[208:211], a[168:171], v[176:179]\n"
  "v_mfma_f32_16x16x32_bf16 v[180:183], v[208:211], a[172:175], v[180:183]\n"
  "buffer_load_dwordx4 v41, s[48:51], s74 offen offset:1024 lds\n"
  "v_mfma_f32_16x16x32_bf16 v[184:187], v[208:211], a[176:179], v[184:187]\n"
  "s_waitcnt lgkmcnt(4)\n"
  "v_mfma_f32_16x16x32_bf16 v[168:171], v[212:215], a[180:183], v[168:171]\n"
  "v_mfma_f32_16x16x32_bf16 v[172:175], v[212:215], a[184:187], v[172:175]\n"
  "v_mfma_f32_16x16x32_bf16 v[176:179], v[212:215], a[188:191], v[176:179]\n"
  "v_lshlrev_b32 v31, 22, v1\n"
  "v_and_b32 v31, 0x80000000, v31\n"
  "v_or_b32 v31, v31, v35\n"
  "v_mfma_f32_16x16x32_bf16 v[180:183], v[212:215], a[192:195], v[180:183]\n"
  "v_mfma_f32_16x16x32_bf16 v[184:187], v[212:215], a[196:199], v[184:187]\n"
  "s_waitcnt lgkmcnt(2)\n"
  "v_mfma_f32_16x16x32_bf16 v[168:171], v[216:219], a[200:203], v[168:171]\n"
  "ds_read_b64_tr_b16 v[208:209], v45\n"
  "ds_read_b64_tr_b16 v[210:211], v45 offset:2048\n"
  "v_mfma_f32_16x16x32_bf16 v[172:175], v[216:219], a[204:207], v[172:175]\n"
  "ds_read_b64_tr_b16 v[212:213], v45 offset:4096\n"
  "ds_read_b64_tr_b16 v[214:215], v45 offset:6144\n"
  "v_mfma_f32_16x16x32_bf16 v[176:179], v[216:219], a[208:211], v[176:179]\n"
  "v_mfma_f32_16x16x32_bf16 v[180:183], v[216:219], a[212:215], v[180:183]\n"
  "Lh34_%=:\n"
  "s_cbranch_execz Lh35_%=\n"
  "buffer_load_dwordx4 v42, s[48:51], s74 offen offset:2048 lds\n"
  "v_mfma_f32_16x16x32_bf16 v[184:187], v[216:219], a[216:219], v[184:187]\n"
  "s_waitcnt lgkmcnt(4)\n"
  "v_mfma_f32_16x16x32_bf16 v[168:171], v[220:223], a[220:223], v[168:171]\n"
  "buffer_load_dwordx2 v[50:51], v34, s[56:59], 0 offen offset:1024 sc1\n"
  "v_mfma_f32_16x16x32_bf16 v[172:175], v[220:223], a[224:227], v[172:175]\n"
  "buffer_load_dwordx4 v43, s[48:51], s74 offen offset:3072 lds\n"
  "v_mfma_f32_16x16x32_bf16 v[176:179], v[220:223], a[228:231], v[176:179]\n"
  "v_mfma_f32_16x16x32_bf16 v[180:183], v[220:223], a[232:235], v[180:183]\n"
  "v_mfma_f32_16x16x32_bf16 v[184:187], v[220:223], a[236:239], v[184:187]\n"
  "s_waitcnt lgkmcnt(2)\n"
  "v_mfma_f32_16x16x32_bf16 v[168:171], v[208:211], a[240:243], v[168:171]\n"
  "ds_read_b64_tr_b16 v[216:217], v45 offset:8192\n"
  "ds_read_b64_tr_b16 v[218:219], v45 offset:10240\n"
  "v_mfma_f32_16x16x32_bf16 v[172:175], v[208:211], a[244:247], v[172:175]\n"
  "ds_read_b64_tr_b16 v[220:221], v45 offset:12288\n"
  "ds_read_b64_tr_b16 v[222:223], v45 offset:14336\n"
  "v_mfma_f32_16x16x32_bf16 v[176:179], v[208:211], a[248:251], v[176:179]\n"
  "v_mfma_f32_16x16x32_bf16 v[180:183], v[208:211], a[252:255], v[180:183]\n"
  "v_mfma_f32_16x16x32_bf16 v[184:187], v[208:211], v[64:67], v[184:187]\n"
  "s_waitcnt lgkmcnt(4)\n"
  "v_mfma_f32_16x16x32_bf16 v[168:171], v[212:215], v[68:71], v[168:171]\n"
  "v_mfma_f32_16x16x32_bf16 v[172:175], v[212:215], v[72:75], v[172:175]\n"
  "v_mfma_f32_16x16x32_bf16 v[176:179], v[212:215], v[76:79], v[176:179]\n"
  "v_mfma_f32_16x16x32_bf16 v[180:183], v[212:215], v[80:83], v[180:183]\n"
  "v_mfma_f32_16x16x32_bf16 v[184:187], v[212:215], v[84:87], v[184:187]\n"
  "s_waitcnt lgkmcnt(2)\n"
  "v_mfma_f32_16x16x32_bf16 v[168:171], v[216:219], v[88:91], v[168:171]\n"
  "s_waitcnt vmcnt(14)\n"
  "s_waitcnt lgkmcnt(0)\n"
  "s_barrier\n"
  "v_mfma_f32_16x16x32_bf16 v[172:175], v[216:219], v[92:95], v[172:175]\n"
  "ds_read_b64_tr_b16 v[208:209], v45 offset:49152\n"
  "ds_read_b64_tr_b16 v[210:211], v45 offset:51200\n"
  "v_mfma_f32_16x16x32_bf16 v[176:179], v[216:219], v[96:99], v[176:179]\n"
  "ds_read_b64_tr_b16 v[212:213], v45 offset:53248\n"
  "ds_read_b64_tr_b16 v[214:215], v45 offset:55296\n"
  "v_mfma_f32_16x16x32_bf16 v[180:183], v[216:219], v[100:103], v[180:183]\n"
  "v_mfma_f32_16x16x32_bf16 v[184:187], v[216:219], v[104:107], v[184:187]\n"
  "v_mfma_f32_16x16x32_bf16 v[168:171], v[220:223], v[108:111], v[168:171]\n"
  "v_mfma_f32_16x16x32_bf16 v[172:175], v[220:223], v[112:115], v[172:175]\n"
  "v_mfma_f32_16x16x32_bf16 v[176:179], v[220:223], v[116:119], v[176:179]\n"
  "v_mfma_f32_16x16x32_bf16 v[180:183], v[220:223], v[120:123], v[180:183]\n"
  "s_nop 4\n"
  "v_cvt_pk_bf16_f32 v168, v168, v169\n"
  "v_cvt_pk_bf16_f32 v169, v170, v171\n"
  "v_mfma_f32_16x16x32_bf16 v[184:187], v[220:223], v[124:127], v[184:187]\n"
  "v_cvt_pk_bf16_f32 v170, v172, v173\n"
  "v_cvt_pk_bf16_f32 v171, v174, v175\n"
  "s_waitcnt lgkmcnt(2)\n"
  "v_mfma_f32_16x16x32_bf16 v[188:191], v[208:211], a[0:3], 0\n"
  "ds_read_b64_tr_b16 v[216:217], v45 offset:57344\n"
  "ds_read_b64_tr_b16 v[218:219], v45 offset:59392\n"
  "buffer_store_dwordx4 v[168:171], v32, s[52:55], s19 offen sc1\n"
  "v_cvt_pk_bf16_f32 v176, v176, v177\n"
  "v_cvt_pk_bf16_f32 v177, v178, v179\n"
  "v_mfma_f32_16x16x32_bf16 v[192:195], v[208:211], a[4:7], 0\n"
  "ds_read_b64_tr_b16 v[220:221], v45 offset:61440\n"
  "ds_read_b64_tr_b16 v[222:223], v45 offset:63488\n"
  "v_cvt_pk_bf16_f32 v178, v180, v181\n"
  "v_cvt_pk_bf16_f32 v179, v182, v183\n"
  "v_mfma_f32_16x16x32_bf16 v[196:199], v[208:211], a[8:11], 0\n"
  "buffer_store_dwordx4 v[176:179], v32, s[52:55], s19 offen offset:1024 sc1\n"
  "v_cvt_pk_bf16_f32 v184, v184, v185\n"
  "v_cvt_pk_bf16_f32 v185, v186, v187\n"
  "v_mov_b32 v186, v184\n"
  "v_mov_b32 v187, v185\n"
  "v_mfma_f32_16x16x32_bf16 v[200:203], v[208:211], a[12:15], 0\n"
  "v_permlane32_swap_b32 v184, v186\n"
  "v_permlane32_swap_b32 v185, v187\n"
  "Lh35_%=:\n"
  "s_cbranch_execz Lh36_%=\n"
  "v_mfma_f32_16x16x32_bf16 v[204:207], v[208:211], a[16:19], 0\n"
  "buffer_store_dwordx4 v[184:187], v31, s[52:55], s19 offen sc1\n"
  "s_waitcnt lgkmcnt(4)\n"
  "v_mfma_f32_16x16x32_bf16 v[188:191], v[212:215], a[20:23], v[188:191]\n"
  "v_mfma_f32_16x16x32_bf16 v[192:195], v[212:215], a[24:27], v[192:195]\n"
  "v_mfma_f32_16x16x32_bf16 v[196:199], v[212:215], a[28:31], v[196:199]\n"
  "v_mfma_f32_16x16x32_bf16 v[200:203], v[212:215], a[32:35], v[200:203]\n"
  "v_mfma_f32_16x16x32_bf16 v[204:207], v[212:215], a[36:39], v[204:207]\n"
  "s_waitcnt lgkmcnt(2)\n"
  "v_mfma_f32_16x16x32_bf16 v[188:191], v[216:219], a[40:43], v[188:191]\n"
  "ds_read_b64_tr_b16 v[208:209], v46\n"
  "ds_read_b64_tr_b16 v[210:211], v46 offset:2048\n"
  "v_mfma_f32_16x16x32_bf16 v[192:195], v[216:219], a[44:47], v[192:195]\n"
  "ds_read_b64_tr_b16 v[212:213], v46 offset:4096\n"
  "ds_read_b64_tr_b16 v[214:215], v46 offset:6144\n"
  "v_mfma_f32_16x16x32_bf16 v[196:199], v[216:219], a[48:51], v[196:199]\n"
  "v_mfma_f32_16x16x32_bf16 v[200:203], v[216:219], a[52:55], v[200:203]\n"
  "v_mfma_f32_16x16x32_bf16 v[204:207], v[216:219], a[56:59], v[204:207]\n"
  "v_mov_b32 v60, v36\n"
  "v_mov_b32 v61, v37\n"
  "v_mov_b32 v62, v36\n"
  "v_mov_b32 v63, v37\n"
  "s_waitcnt lgkmcnt(4)\n"
  "v_mfma_f32_16x16x32_bf16 v[188:191], v[220:223], a[60:63], v[188:191]\n"
  "v_mfma_f32_16x16x32_bf16 v[192:195], v[220:223], a[64:67], v[192:195]\n"
  "v_swap_b32 v226, v228\n"
  "v_swap_b32 v227, v229\n"
  "v_mfma_f32_16x16x32_bf16 v[196:199], v[220:223], a[68:71], v[196:199]\n"
  "v_mfma_f32_16x16x32_bf16 v[200:203], v[220:223], a[72:75], v[200:203]\n"
  "v_swap_b32 v234, v236\n"
  "v_swap_b32 v235, v237\n"
  "v_mfma_f32_16x16x32_bf16 v[204:207], v[220:223], a[76:79], v[204:207]\n"
  "s_waitcnt lgkmcnt(2)\n"
  "v_mfma_f32_16x16x32_bf16 v[188:191], v[208:211], a[80:83], v[188:191]\n"
  "ds_read_b64_tr_b16 v[216:217], v46 offset:8192\n"
  "ds_read_b64_tr_b16 v[218:219], v46 offset:10240\n"
  "v_mfma_f32_16x16x32_bf16 v[192:195], v[208:211], a[84:87], v[192:195]\n"
  "ds_read_b64_tr_b16 v[220:221], v46 offset:12288\n"
  "ds_read_b64_tr_b16 v[222:223], v46 offset:14336\n"
  "v_mfma_f32_16x16x32_bf16 v[196:199], v[208:211], a[88:91], v[196:199]\n"
  "v_mfma_f32_16x16x32_bf16 v[200:203], v[208:211], a[92:95], v[200:203]\n"
  "v_mfma_f32_16x16x32_bf16 v[204:207], v[208:211], a[96:99], v[204:207]\n"
  "s_waitcnt lgkmcnt(4)\n"
  "v_mfma_f32_16x16x32_bf16 v[188:191], v[212:215], a[100:103], v[188:191]\n"
  "s_waitcnt vmcnt(4)\n"
  "v_cmp_eq_u64 vcc, v[50:51], s[90:91]\n"
  "s_mov_b32 s89, 0x10000\n"
  "Lp1_%=:\n"
  "s_nop 0\n"
  "s_andn2_b32 s8, s25, vcc_lo\n"
  "s_cbranch_scc0 Lq1_%=\n"
  "s_sleep 1\n"
  "buffer_load_dwordx2 v[50:51], v34, s[56:59], 0 offen offset:1024 sc1\n"
  "s_sub_u32 s89, s89, 1\n"
  "s_cbranch_scc1 Lq1_%=\n"
  "s_waitcnt vmcnt(0)\n"
  "v_cmp_eq_u64 vcc, v[50:51], s[90:91]\n"
  "s_branch Lp1_%=\n"
  "Lq1_%=:\n"
  "v_mfma_f32_16x16x32_bf16 v[192:195], v[212:215], a[104:107], v[192:195]\n"
  "buffer_load_dwordx4 v[2:5], v32, s[52:55], s21 offen sc1\n"
  "v_mfma_f32_16x16x32_bf16 v[196:199], v[212:215], a[108:111], v[196:199]\n"
  "v_mfma_f32_16x16x32_bf16 v[200:203], v[212:215], a[112:115], v[200:203]\n"
  "buffer_load_dwordx4 v[10:13], v32, s[52:55], s21 offen offset:1024 sc1\n"
  "v_mfma_f32_16x16x32_bf16 v[204:207], v[212:215], a[116:119], v[204:207]\n"
  "s_waitcnt lgkmcnt(2)\n"
  "v_mfma_f32_16x16x32_bf16 v[188:191], v[216:219], a[120:123], v[188:191]\n"
  "s_waitcnt vmcnt(5)\n"
  "s_waitcnt lgkmcnt(0)\n"
  "s_barrier\n"
  "buffer_load_dwordx2 v[18:19], v35, s[52:55], s21 offen sc1\n"
  "v_mfma_f32_16x16x32_bf16 v[192:195], v[216:219], a[124:127], v[192:195]\n"
  "ds_read_b64_tr_b16 v[208:209], v44 offset:16384\n"
  "ds_read_b64_tr_b16 v[210:211], v44 offset:18432\n"
  "v_mfma_f32_16x16x32_bf16 v[196:199], v[216:219], a[128:131], v[196:199]\n"
  "ds_read_b64_tr_b16 v[212:213], v44 offset:20480\n"
  "ds_read_b64_tr_b16 v[214:215], v44 offset:22528\n"
  "s_add_u32 s98, s21, 0x2800\n"
  "buffer_load_dwordx4 v[6:9], v32, s[52:55], s98 offen sc1\n"
  "Lh36_%=:\n"
  "s_cbranch_execz Lh37_%=\n"
  "v_mfma_f32_16x16x32_bf16 v[200:203], v[216:219], a[132:135], v[200:203]\n"
  "v_mfma_f32_16x16x32_bf16 v[204:207], v[216:219], a[136:139], v[204:207]\n"
  "s_add_u32 s98, s21, 0x2800\n"
  "buffer_load_dwordx4 v[14:17], v32, s[52:55], s98 offen offset:1024 sc1\n"
  "v_mfma_f32_16x16x32_bf16 v[188:191], v[220:223], a[140:143], v[188:191]\n"
  "v_mfma_f32_16x16x32_bf16 v[192:195], v[220:223], a[144:147], v[192:195]\n"
  "s_add_u32 s98, s21, 0x2800\n"
  "buffer_load_dwordx2 v[20:21], v35, s[52:55], s98 offen sc1\n"
  "v_mfma_f32_16x16x32_bf16 v[196:199], v[220:223], a[148:151], v[196:199]\n"
  "v_mfma_f32_16x16x32_bf16 v[200:203], v[220:223], a[152:155], v[200:203]\n"
  "buffer_load_dwordx4 v[128:131], v32, s[52:55], s22 offen sc1\n"
  "v_mfma_f32_16x16x32_bf16 v[204:207], v[220:223], a[156:159], v[204:207]\n"
  "s_waitcnt lgkmcnt(2)\n"
  "v_mfma_f32_16x16x32_bf16 v[188:191], v[208:211], a[160:163], v[188:191]\n"
  "ds_read_b64_tr_b16 v[216:217], v44 offset:24576\n"
  "ds_read_b64_tr_b16 v[218:219], v44 offset:26624\n"
  "buffer_load_dwordx4 v[136:139], v32, s[52:55], s22 offen offset:1024 sc1\n"
  "v_mfma_f32_16x16x32_bf16 v[192:195], v[208:211], a[164:167], v[192:195]\n"
  "ds_read_b64_tr_b16 v[220:221], v44 offset:28672\n"
  "ds_read_b64_tr_b16 v[222:223], v44 offset:30720\n"
  "v_mfma_f32_16x16x32_bf16 v[196:199], v[208:211], a[168:171], v[196:199]\n"
  "buffer_load_dwordx2 v[144:145], v35, s[52:55], s22 offen sc1\n"
  "v_mfma_f32_16x16x32_bf16 v[200:203], v[208:211], a[172:175], v[200:203]\n"
  "v_mfma_f32_16x16x32_bf16 v[204:207], v[208:211], a[176:179], v[204:207]\n"
  "s_add_u32 s98, s22, 0x2800\n"
  "buffer_load_dwordx4 v[132:135], v32, s[52:55], s98 offen sc1\n"
  "s_waitcnt lgkmcnt(4)\n"
  "v_mfma_f32_16x16x32_bf16 v[188:191], v[212:215], a[180:183], v[188:191]\n"
  "v_mfma_f32_16x16x32_bf16 v[192:195], v[212:215], a[184:187], v[192:195]\n"
  "s_add_u32 s98, s22, 0x2800\n"
  "buffer_load_dwordx4 v[140:143], v32, s[52:55], s98 offen offset:1024 sc1\n"
  "v_mfma_f32_16x16x32_bf16 v[196:199], v[212:215], a[188:191], v[196:199]\n"
  "v_mfma_f32_16x16x32_bf16 v[200:203], v[212:215], a[192:195], v[200:203]\n"
  "s_add_u32 s98, s22, 0x2800\n"
  "buffer_load_dwordx2 v[146:147], v35, s[52:55], s98 offen sc1\n"
  "v_mfma_f32_16x16x32_bf16 v[204:207], v[212:215], a[196:199], v[204:207]\n"
  "s_waitcnt lgkmcnt(2)\n"
  "v_mfma_f32_16x16x32_bf16 v[188:191], v[216:219], a[200:203], v[188:191]\n"
  "ds_read_b64_tr_b16 v[208:209], v46 offset:16384\n"
  "ds_read_b64_tr_b16 v[210:211], v46 offset:18432\n"
  "buffer_load_dwordx4 v[244:247], v32, s[52:55], s23 offen sc1\n"
  "v_mfma_f32_16x16x32_bf16 v[192:195], v[216:219], a[204:207], v[192:195]\n"
  "ds_read_b64_tr_b16 v[212:213], v46 offset:20480\n"
  "ds_read_b64_tr_b16 v[214:215], v46 offset:22528\n"
  "v_mfma_f32_16x16x32_bf16 v[196:199], v[216:219], a[208:211], v[196:199]\n"
  "buffer_load_dwordx4 v[248:251], v32, s[52:55], s23 offen offset:1024 sc1\n"
  "v_mfma_f32_16x16x32_bf16 v[200:203], v[216:219], a[212:215], v[200:203]\n"
  "v_mfma_f32_16x16x32_bf16 v[204:207], v[216:219], a[216:219], v[204:207]\n"
  "buffer_load_dwordx2 v[252:253], v35, s[52:55], s23 offen sc1\n"
  "s_waitcnt lgkmcnt(4)\n"
  "v_mfma_f32_16x16x32_bf16 v[188:191], v[220:223], a[220:223], v[188:191]\n"
  "v_mfma_f32_16x16x32_bf16 v[192:195], v[220:223], a[224:227], v[192:195]\n"
  "v_mfma_f32_16x16x32_bf16 v[196:199], v[220:223], a[228:231], v[196:199]\n"
  "v_mfma_f32_16x16x32_bf16 v[200:203], v[220:223], a[232:235], v[200:203]\n"
  "v_mfma_f32_16x16x32_bf16 v[204:207], v[220:223], a[236:239], v[204:207]\n"
  "s_waitcnt lgkmcnt(2)\n"
  "v_mfma_f32_16x16x32_bf16 v[188:191], v[208:211], a[240:243], v[188:191]\n"
  "ds_read_b64_tr_b16 v[216:217], v46 offset:24576\n"
  "ds_read_b64_tr_b16 v[218:219], v46 offset:26624\n"
  "v_mfma_f32_16x16x32_bf16 v[192:195], v[208:211], a[244:247], v[192:195]\n"
  "ds_read_b64_tr_b16 v[220:221], v46 offset:28672\n"
  "ds_read_b64_tr_b16 v[222:223], v46 offset:30720\n"
  "s_lshl_b32 s13, s64, 1\n"
  "v_lshrrev_b32 v56, 9, v1\n"
  "v_or_b32 v56, s13, v56\n"
  "v_bfe_u32 v59, v1, 5, 3\n"
  "v_xor_b32 v56, v56, v59\n"
  "v_mfma_f32_16x16x32_bf16 v[196:199], v[208:211], a[248:251], v[196:199]\n"
  "Lh37_%=:\n"
  "s_cbranch_execz Lh38_%=\n"
  "v_mfma_f32_16x16x32_bf16 v[200:203], v[208:211], a[252:255], v[200:203]\n"
  "v_and_b32 v59, 0xf0, v1\n"
  "v_lshlrev_b32 v59, 3, v59\n"
  "v_lshl_or_b32 v56, v56, 4, v59\n"
  "v_mfma_f32_16x16x32_bf16 v[204:207], v[208:211], v[64:67], v[204:207]\n"
  "s_waitcnt lgkmcnt(4)\n"
  "v_mfma_f32_16x16x32_bf16 v[188:191], v[212:215], v[68:71], v[188:191]\n"
  "v_lshrrev_b32 v59, 5, v1\n"
  "v_and_b32 v59, 8, v59\n"
  "v_or_b32 v56, v56, v59\n"
  "v_add_u32 v56, 0x1c000, v56\n"
  "v_mfma_f32_16x16x32_bf16 v[192:195], v[212:215], v[72:75], v[192:195]\n"
  "v_mfma_f32_16x16x32_bf16 v[196:199], v[212:215], v[76:79], v[196:199]\n"
  "s_and_b32 s13, s64, 1\n"
  "s_lshl_b32 s13, s13, 6\n"
  "v_lshrrev_b32 v57, 4, v1\n"
  "v_and_b32 v57, 48, v57\n"
  "v_or_b32 v57, s13, v57\n"
  "v_xor_b32 v57, v57, v1\n"
  "v_mfma_f32_16x16x32_bf16 v[200:203], v[212:215], v[80:83], v[200:203]\n"
  "v_mfma_f32_16x16x32_bf16 v[204:207], v[212:215], v[84:87], v[204:207]\n"
  "s_add_u32 s13, s96, 0x1c000\n"
  "v_add_u32 v57, s13, v57\n"
  "v_lshrrev_b32 v58, 7, v1\n"
  "v_and_b32 v59, 0x70, v1\n"
  "v_lshl_or_b32 v58, v58, 11, v59\n"
  "s_waitcnt lgkmcnt(2)\n"
  "v_mfma_f32_16x16x32_bf16 v[188:191], v[216:219], v[88:91], v[188:191]\n"
  "v_mfma_f32_16x16x32_bf16 v[192:195], v[216:219], v[92:95], v[192:195]\n"
  "s_lshl_b32 s13, s64, 14\n"
  "v_add_u32 v58, s13, v58\n"
  "v_mfma_f32_16x16x32_bf16 v[196:199], v[216:219], v[96:99], v[196:199]\n"
  "v_mfma_f32_16x16x32_bf16 v[200:203], v[216:219], v[100:103], v[200:203]\n"
  "v_mfma_f32_16x16x32_bf16 v[204:207], v[216:219], v[104:107], v[204:207]\n"
  "s_waitcnt lgkmcnt(0)\n"
  "v_mfma_f32_16x16x32_bf16 v[188:191], v[220:223], v[108:111], v[188:191]\n"
  "v_mfma_f32_16x16x32_bf16 v[192:195], v[220:223], v[112:115], v[192:195]\n"
  "v_mfma_f32_16x16x32_bf16 v[196:199], v[220:223], v[116:119], v[196:199]\n"
  "v_mfma_f32_16x16x32_bf16 v[200:203], v[220:223], v[120:123], v[200:203]\n"
  "v_mfma_f32_16x16x32_bf16 v[204:207], v[220:223], v[124:127], v[204:207]\n"
  "s_or_b32 s13, s31, s64\n"
  "s_cmp_eq_u32 s13, 0\n"
  "s_cselect_b32 s13, 0, 0x40000000\n"
  "v_add_u32 v52, 0xfffffc00, v33\n"
  "v_or_b32 v52, s13, v52\n"
  "buffer_store_dwordx2 v[48:49], v52, s[56:59], 0 offen sc1\n"
  "v_mfma_f32_16x16x32_bf16 v[188:191], v[60:63], v[224:227], v[188:191]\n"
  "v_mfma_f32_16x16x32_bf16 v[192:195], v[60:63], v[228:231], v[192:195]\n"
  "s_waitcnt vmcnt(12)\n"
  "v_swap_b32 v4, v6\n"
  "v_swap_b32 v5, v7\n"
  "v_mfma_f32_16x16x32_bf16 v[196:199], v[60:63], v[232:235], v[196:199]\n"
  "s_waitcnt vmcnt(11)\n"
  "v_swap_b32 v12, v14\n"
  "v_swap_b32 v13, v15\n"
  "v_mfma_f32_16x16x32_bf16 v[200:203], v[60:63], v[236:239], v[200:203]\n"
  "v_mfma_f32_16x16x32_bf16 v[204:207], v[60:63], v[240:243], v[204:207]\n"
  "v_mfma_f32_16x16x32_bf16 v[188:191], v[60:63], v[2:5], v[188:191]\n"
  "s_add_u32 s98, s22, 0x2800\n"
  "s_mov_b32 s89, 0x10000\n"
  "s_waitcnt vmcnt(4)\n"
  "v_max3_u32 v50, v128, v129, v130\n"
  "v_max3_u32 v51, v131, v132, v133\n"
  "v_max3_u32 v50, v50, v134, v135\n"
  "v_mfma_f32_16x16x32_bf16 v[192:195], v[60:63], v[6:9], v[192:195]\n"
  "v_max3_u32 v51, v51, v136, v137\n"
  "v_max3_u32 v50, v50, v138, v139\n"
  "v_max3_u32 v51, v51, v140, v141\n"
  "v_mfma_f32_16x16x32_bf16 v[204:207], v[60:63], v[18:21], v[204:207]\n"
  "v_max3_u32 v50, v50, v142, v143\n"
  "v_max3_u32 v51, v51, v144, v145\n"
  "v_max3_u32 v50, v50, v146, v147\n"
  "v_mfma_f32_16x16x32_bf16 v[196:199], v[60:63], v[10:13], v[196:199]\n"
  "v_max_u32 v50, v50, v51\n"
  "v_cmp_eq_u32 vcc, -1, v50\n"
  "v_mfma_f32_16x16x32_bf16 v[200:203], v[60:63], v[14:17], v[200:203]\n"
  "s_nop 1\n"
  "s_cbranch_vccnz Ls2_%=\n"
  "Lt2_%=:\n"
  "v_mfma_f32_16x16x32_bf16 v[204:207], v[60:63], v[144:147], v[204:207]\n"
  "Lh38_%=:\n"
  "s_cbranch_execz Lh39_%=\n"
  "v_swap_b32 v130, v132\n"
  "v_swap_b32 v131, v133\n"
  "s_nop 1\n"
  "v_mfma_f32_16x16x32_bf16 v[188:191], v[60:63], v[128:131], v[188:191]\n"
  "v_swap_b32 v138, v140\n"
  "v_swap_b32 v139, v141\n"
  "s_mov_b32 s89, 0x10000\n"
  "s_waitcnt vmcnt(1)\n"
  "v_max3_u32 v50, v244, v245, v246\n"
  "v_max3_u32 v51, v247, v248, v249\n"
  "v_mfma_f32_16x16x32_bf16 v[192:195], v[60:63], v[132:135], v[192:195]\n"
  "v_max3_u32 v50, v50, v250, v251\n"
  "v_max3_u32 v51, v51, v252, v253\n"
  "v_mfma_f32_16x16x32_bf16 v[196:199], v[60:63], v[136:139], v[196:199]\n"
  "v_max_u32 v50, v50, v51\n"
  "v_cmp_eq_u32 vcc, -1, v50\n"
  "v_mfma_f32_16x16x32_bf16 v[200:203], v[60:63], v[140:143], v[200:203]\n"
  "s_nop 1\n"
  "s_cbranch_vccnz Ls3_%=\n"
  "Lt3_%=:\n"
  "v_mfma_f32_16x16x16_bf16 v[204:207], v[36:37], v[252:253], v[204:207]\n"
  "v_mfma_f32_16x16x16_bf16 v[188:191], v[36:37], v[244:245], v[188:191]\n"
  "v_mfma_f32_16x16x16_bf16 v[192:195], v[36:37], v[246:247], v[192:195]\n"
  "v_mfma_f32_16x16x16_bf16 v[196:199], v[36:37], v[248:249], v[196:199]\n"
  "s_nop 4\n"
  "v_cvt_pk_bf16_f32 v204, v204, v205\n"
  "v_cvt_pk_bf16_f32 v205, v206, v207\n"
  "ds_write_b64 v56, v[204:205] offset:8192\n"
  "v_mfma_f32_16x16x16_bf16 v[200:203], v[36:37], v[250:251], v[200:203]\n"
  "v_cvt_pk_bf16_f32 v188, v188, v189\n"
  "v_cvt_pk_bf16_f32 v189, v190, v191\n"
  "ds_write_b64 v56, v[188:189]\n"
  "v_cvt_pk_bf16_f32 v192, v192, v193\n"
  "v_cvt_pk_bf16_f32 v193, v194, v195\n"
  "ds_write_b64 v56, v[192:193] offset:2048\n"
  "v_cvt_pk_bf16_f32 v196, v196, v197\n"
  "v_cvt_pk_bf16_f32 v197, v198, v199\n"
  "ds_write_b64 v56, v[196:197] offset:4096\n"
  "v_cvt_pk_bf16_f32 v200, v200, v201\n"
  "v_cvt_pk_bf16_f32 v201, v202, v203\n"
  "ds_write_b64 v56, v[200:201] offset:6144\n"
  "s_waitcnt lgkmcnt(0)\n"
  "s_barrier\n"
  "ds_read_b128 v[224:227], v57\n"
  "ds_read_b128 v[228:231], v57 offset:4096\n"
  "ds_read_b128 v[232:235], v57 offset:8192\n"
  "s_cmp_ge_u32 s64, 2\n"
  "s_waitcnt lgkmcnt(2)\n"
  "buffer_store_dwordx4 v[224:227], v58, s[60:63], 0 offen\n"
  "s_waitcnt lgkmcnt(1)\n"
  "buffer_store_dwordx4 v[228:231], v58, s[60:63], s86 offen\n"
  "s_waitcnt lgkmcnt(0)\n"
  "s_cbranch_scc1 Lo_%=\n"
  "buffer_store_dwordx4 v[232:235], v58, s[60:63], s88 offen\n"
  "Lo_%=:\n"
  "s_endpgm\n"
  "Ls2_%=:\n"
  "buffer_load_dwordx4 v[128:131], v32, s[52:55], s22 offen sc1\n"
  "buffer_load_dwordx4 v[136:139], v32, s[52:55], s22 offen offset:1024 sc1\n"
  "buffer_load_dwordx2 v[144:145], v35, s[52:55], s22 offen sc1\n"
  "buffer_load_dwordx4 v[132:135], v32, s[52:55], s98 offen sc1\n"
  "buffer_load_dwordx4 v[140:143], v32, s[52:55], s98 offen offset:1024 sc1\n"
  "buffer_load_dwordx2 v[146:147], v35, s[52:55], s98 offen sc1\n"
  "s_sub_u32 s89, s89, 1\n"
  "s_waitcnt vmcnt(0)\n"
  "v_max3_u32 v50, v128, v129, v130\n"
  "v_max3_u32 v51, v131, v132, v133\n"
  "v_max3_u32 v50, v50, v134, v135\n"
  "v_max3_u32 v51, v51, v136, v137\n"
  "v_max3_u32 v50, v50, v138, v139\n"
  "v_max3_u32 v51, v51, v140, v141\n"
  "v_max3_u32 v50, v50, v142, v143\n"
  "v_max3_u32 v51, v51, v144, v145\n"
  "v_max3_u32 v50, v50, v146, v147\n"
  "v_max_u32 v50, v50, v51\n"
  "v_cmp_eq_u32 vcc, -1, v50\n"
  "s_nop 1\n"
  "s_cbranch_vccz Lt2_%=\n"
  "s_cbranch_scc0 Ls2_%=\n"
  "s_branch Lt2_%=\n"
  "Ls3_%=:\n"
  "Lh39_%=:\n"
  "s_cbranch_execz Lret_%=\n"
  "buffer_load_dwordx4 v[244:247], v32, s[52:55], s23 offen sc1\n"
  "buffer_load_dwordx4 v[248:251], v32, s[52:55], s23 offen offset:1024 sc1\n"
  "buffer_load_dwordx2 v[252:253], v35, s[52:55], s23 offen sc1\n"
  "s_sub_u32 s89, s89, 1\n"
  "s_waitcnt vmcnt(0)\n"
  "v_max3_u32 v50, v244, v245, v246\n"
  "v_max3_u32 v51, v247, v248, v249\n"
  "v_max3_u32 v50, v50, v250, v251\n"
  "v_max3_u32 v51, v51, v252, v253\n"
  "v_max_u32 v50, v50, v51\n"
  "v_cmp_eq_u32 vcc, -1, v50\n"
  "s_nop 1\n"
  "s_cbranch_vccz Lt3_%=\n"
  "s_cbranch_scc0 Ls3_%=\n"
  "s_branch Lt3_%=\n"
  "Lend_%=:\n"
  : "+{v0}"(tid)
  : "{s[4:5]}"(__builtin_amdgcn_kernarg_segment_ptr()), "{s6}"(blockIdx.x), "{s7}"((unsigned)(size_t)smem)
  : "v1", "v2", "v3", "v4", "v5", "v6", "v7", "v8", "v9", "v10", "v11", "v12", "v13", "v14", "v15", "v16", "v17", "v18", "v19", "v20", "v21", "v22", "v23", "v24", "v25", "v26", "v27", "v28", "v29", "v30", "v31", "v32", "v33", "v34", "v35", "v36", "v37", "v38", "v39", "v40", "v41", "v42", "v43", "v44", "v45", "v46", "v47", "v48", "v49", "v50", "v51", "v52", "v53", "v54", "v55", "v56", "v57", "v58", "v59", "v60", "v61", "v62", "v63", "v64", "v65", "v66", "v67", "v68", "v69", "v70", "v71", "v72", "v73", "v74", "v75", "v76", "v77", "v78", "v79", "v80", "v81", "v82", "v83", "v84", "v85", "v86", "v87", "v88", "v89", "v90", "v91", "v92", "v93", "v94", "v95", "v96", "v97", "v98", "v99", "v100", "v101", "v102", "v103", "v104", "v105", "v106", "v107", "v108", "v109", "v110", "v111", "v112", "v113", "v114", "v115", "v116", "v117", "v118", "v119", "v120", "v121", "v122", "v123", "v124", "v125", "v126", "v127", "v128", "v129", "v130", "v131", "v132", "v133", "v134", "v135", "v136", "v137", "v138", "v139", "v140", "v141", "v142", "v143", "v144", "v145", "v146", "v147", "v148", "v149", "v150", "v151", "v152", "v153", "v154", "v155", "v156", "v157", "v158", "v159", "v160", "v161", "v162", "v163", "v164", "v165", "v166", "v167", "v168", "v169", "v170", "v171", "v172", "v173", "v174", "v175", "v176", "v177", "v178", "v179", "v180", "v181", "v182", "v183", "v184", "v185", "v186", "v187", "v188", "v189", "v190", "v191", "v192", "v193", "v194", "v195", "v196", "v197", "v198", "v199", "v200", "v201", "v202", "v203", "v204", "v205", "v206", "v207", "v208", "v209", "v210", "v211", "v212", "v213", "v214", "v215", "v216", "v217", "v218", "v219", "v220", "v221", "v222", "v223", "v224", "v225", "v226", "v227", "v228", "v229", "v230", "v231", "v232", "v233", "v234", "v235", "v236", "v237", "v238", "v239", "v240", "v241", "v242", "v243", "v244", "v245", "v246", "v247", "v248", "v249", "v250", "v251", "v252", "v253", "v254", "v255", "a0", "a1", "a2", "a3", "a4", "a5", "a6", "a7", "a8", "a9", "a10", "a11", "a12", "a13", "a14", "a15", "a16", "a17", "a18", "a19", "a20", "a21", "a22", "a23", "a24", "a25", "a26", "a27", "a28", "a29", "a30", "a31", "a32", "a33", "a34", "a35", "a36", "a37", "a38", "a39", "a40", "a41", "a42", "a43", "a44", "a45", "a46", "a47", "a48", "a49", "a50", "a51", "a52", "a53", "a54", "a55", "a56", "a57", "a58", "a59", "a60", "a61", "a62", "a63", "a64", "a65", "a66", "a67", "a68", "a69", "a70", "a71", "a72", "a73", "a74", "a75", "a76", "a77", "a78", "a79", "a80", "a81", "a82", "a83", "a84", "a85", "a86", "a87", "a88", "a89", "a90", "a91", "a92", "a93", "a94", "a95", "a96", "a97", "a98", "a99", "a100", "a101", "a102", "a103", "a104", "a105", "a106", "a107", "a108", "a109", "a110", "a111", "a112", "a113", "a114", "a115", "a116", "a117", "a118", "a119", "a120", "a121", "a122", "a123", "a124", "a125", "a126", "a127", "a128", "a129", "a130", "a131", "a132", "a133", "a134", "a135", "a136", "a137", "a138", "a139", "a140", "a141", "a142", "a143", "a144", "a145", "a146", "a147", "a148", "a149", "a150", "a151", "a152", "a153", "a154", "a155", "a156", "a157", "a158", "a159", "a160", "a161", "a162", "a163", "a164", "a165", "a166", "a167", "a168", "a169", "a170", "a171", "a172", "a173", "a174", "a175", "a176", "a177", "a178", "a179", "a180", "a181", "a182", "a183", "a184", "a185", "a186", "a187", "a188", "a189", "a190", "a191", "a192", "a193", "a194", "a195", "a196", "a197", "a198", "a199", "a200", "a201", "a202", "a203", "a204", "a205", "a206", "a207", "a208", "a209", "a210", "a211", "a212", "a213", "a214", "a215", "a216", "a217", "a218", "a219", "a220", "a221", "a222", "a223", "a224", "a225", "a226", "a227", "a228", "a229", "a230", "a231", "a232", "a233", "a234", "a235", "a236", "a237", "a238", "a239", "a240", "a241", "a242", "a243", "a244", "a245", "a246", "a247", "a248", "a249", "a250", "a251", "a252", "a253", "a254", "a255", "s8", "s9", "s10", "s11", "s12", "s13", "s14", "s15", "s16", "s17", "s18", "s19", "s20", "s21", "s22", "s23", "s24", "s25", "s26", "s27", "s28", "s29", "s30", "s31", "s33", "s34", "s35", "s36", "s37", "s38", "s39", "s40", "s41", "s42", "s43", "s44", "s45", "s46", "s47", "s48", "s49", "s50", "s51", "s52", "s53", "s54", "s55", "s56", "s57", "s58", "s59", "s60", "s61", "s62", "s63", "s64", "s65", "s66", "s67", "s68", "s69", "s70", "s71", "s72", "s73", "s74", "s75", "s76", "s77", "s78", "s79", "s80", "s81", "s82", "s83", "s84", "s85", "s86", "s87", "s88", "s89", "s90", "s91", "s92", "s93", "s94", "s95", "s96", "s97", "s98", "s99", "vcc", "scc", "memory");
}


extern "C" void kernel_launch(void* const* d_in, const int* in_sizes, int n_in, void* d_out, int out_size, void* d_ws,
                              size_t ws_size, hipStream_t stream) {
  Args a;
  for (int i = 0; i < 2; ++i) {
    a.side[i].x = d_in[5 * i + 0];
    a.side[i].k = d_in[5 * i + 1];
    a.side[i].v = d_in[5 * i + 2];
    a.side[i].wq = d_in[5 * i + 3];
    a.side[i].wo = d_in[5 * i + 4];
    a.side[i].out = d_out;
    a.side[i].ws = d_ws;
    a.side[i].spare = d_ws;
  }

  fused_attn_kernel<<<256, 256, 0, stream>>>(a);

}
```
